```python
import torch

def _softmax(x, axis=-1):
    return torch.softmax(x, dim=axis)
def _gelu(x):
    return torch.nn.functional.gelu(x, approximate="tanh")
def _silu(x):
    return torch.nn.functional.silu(x)
def _sigmoid(x):
    return torch.sigmoid(x)
def _layernorm(x, axis=-1, eps=1e-5):
    m = x.mean(dim=axis, keepdim=True)
    v = x.var(dim=axis, keepdim=True, unbiased=False)
    return (x - m) / torch.sqrt(v + eps)
def _scatter_add(tgt, idx, src, axis=0):
    return tgt.clone().index_add_(axis, idx, src)
def _segment_sum(src, seg, n):
    out = torch.zeros((n,) + src.shape[1:], dtype=src.dtype)
    return out.index_add_(0, seg, src)
def _pad(x, pw):
    flat = tuple(v for pair in reversed(pw) for v in pair)
    return torch.nn.functional.pad(x, flat)

kernel_name = 'recurrent_scan__gqa_block_cached__glu_gated_swiglu__prompt'

def setup_inputs():

    _w = (lambda a: (lambda t: t.to(torch.bfloat16) if t.is_floating_point() else t)(torch.as_tensor(a)))
    def _A_setup():
        B, Sq, D = 1, 2048, 576
        Hq, Hkv, Sk, Dh = 32, 16, 2048, 128
        assert Hq % Hkv == 0, (Hq, Hkv)
        return {
            "x": _w((lambda *s: torch.randn(*s, dtype=torch.bfloat16) * 0.05)(B, Sq, D)),
            "k_cache": _w((lambda *s: torch.randn(*s, dtype=torch.bfloat16) * 1.6)(B, Hkv, Sk, Dh)),
            "v_cache": _w((lambda *s: torch.randn(*s, dtype=torch.bfloat16) * 1.6)(B, Hkv, Sk, Dh)),
            "wq": _w((lambda *s: torch.randn(*s, dtype=torch.bfloat16) * 1.6)(D, Hq * Dh) * (D ** -0.5)),
            "wo": _w((lambda *s: torch.randn(*s, dtype=torch.bfloat16) * 1.6)(Hq * Dh, D) * ((Hq * Dh) ** -0.5)),
        }
    _A_in = _A_setup()

    def _B_setup():
        B, S, D, F = 1, 2048, 576, 4608
        return {
            "x": _w((lambda *s: torch.randn(*s, dtype=torch.bfloat16) * 0.05)(B, S, D)),
            "w1": _w((lambda *s: torch.randn(*s, dtype=torch.bfloat16) * 0.2)(D, F) * (2.0 / D) ** 0.5),
            "w3": _w((lambda *s: torch.randn(*s, dtype=torch.bfloat16) * 0.2)(D, F) * (2.0 / D) ** 0.5),
            "w2": _w((lambda *s: torch.randn(*s, dtype=torch.bfloat16) * 0.2)(F, D) * (2.0 / F) ** 0.5),
        }
    _B_in = _B_setup()

    return {
        "A_x": _A_in["x"],
        "A_k_cache": _A_in["k_cache"],
        "A_v_cache": _A_in["v_cache"],
        "A_wq": _A_in["wq"],
        "A_wo": _A_in["wo"],
        "B_x": _B_in["x"],
        "B_w1": _B_in["w1"],
        "B_w3": _B_in["w3"],
        "B_w2": _B_in["w2"],
    }

def reference(A_x, A_k_cache, A_v_cache, A_wq, A_wo, B_x, B_w1, B_w3, B_w2):
    def _A_ref(x, k_cache, v_cache, wq, wo):
        B, Sq, D = x.shape
        Hq, Dh = 32, 128
        Hkv = k_cache.shape[1]
        rep = Hq // Hkv
        q = (lambda x, *d: x.permute(*d))((x @ wq).reshape(B, Sq, Hq, Dh), 0, 2, 1, 3)
        k = (lambda x, n, axis: torch.repeat_interleave(x, n, dim=axis))(k_cache, rep, axis=1)
        v = (lambda x, n, axis: torch.repeat_interleave(x, n, dim=axis))(v_cache, rep, axis=1)
        scores = torch.einsum("bhqd,bhkd->bhqk", q, k) * (Dh ** -0.5)
        p = _softmax(scores, axis=-1)
        o = torch.einsum("bhqk,bhkd->bhqd", p, v)
        return (lambda x, *d: x.permute(*d))(o, 0, 2, 1, 3).reshape(B, Sq, Hq * Dh) @ wo

    def _B_ref(x, w1, w3, w2):
        gate = torch.einsum("bsd,df->bsf", x, w1)
        up = torch.einsum("bsd,df->bsf", x, w3)
        h = _silu(gate) * up
        out = torch.einsum("bsf,fd->bsd", h, w2)
        return out

    x = A_x
    xn = _layernorm(x, axis=-1)
    s = _A_ref(xn, A_k_cache, A_v_cache, A_wq, A_wo)
    g = _silu(_B_ref(xn, B_w1, B_w3, B_w2))
    return x + s * g
```

```cpp
#include <hip/hip_runtime.h>
#include <hip/hip_bf16.h>

extern "C" __global__ __launch_bounds__(256, 1) void fused_block(const void* x, const void* w1, const void* w3, const void* wq, const void* K, const void* V,
                                                                 void* Hf, void* Of, void* dbg, const void* w2, const void* wo, void* Wf, void* flags, void* Pf, void* out) {
  extern __shared__ __attribute__((aligned(16))) char lds[];
  const unsigned ldsb = (unsigned)(size_t)lds;
  asm volatile(
    "L_code_begin_%=:\n"
    "v_mov_b32 v1, %0\n"
    "s_mov_b32 s38, %2\n"
    "s_mov_b32 s85, %3\n"
    "s_load_dwordx4 s[20:23], %1, 0\n"
    "s_load_dwordx4 s[24:27], %1, 16\n"
    "s_load_dwordx4 s[28:31], %1, 32\n"
    "s_load_dwordx4 s[4:7], %1, 48\n"
    "s_load_dwordx2 s[94:95], %1, 64\n"
    "s_load_dwordx4 s[8:11], %1, 72\n"
    "s_load_dwordx2 s[12:13], %1, 88\n"
    "s_load_dwordx2 s[2:3], %1, 96\n"
    "s_waitcnt lgkmcnt(0)\n"
    "s_mov_b64 s[34:35], s[4:5]\n"
    "s_mov_b64 s[36:37], s[6:7]\n"
    "s_mov_b64 s[6:7], s[2:3]\n"
    "v_mov_b32 v217, 0\n"
    "global_load_dword v216, v217, s[6:7] sc1\n"
    "v_and_b32 v2, 63, v1\n"
    "v_lshrrev_b32 v3, 6, v1\n"
    "s_nop 0\n"
    "v_readfirstlane_b32 s39, v3\n"
    "s_getpc_b64 s[92:93]\n"
    "L_pc_%=:\n"
    "s_sub_u32 s92, s92, L_pc_%=-L_code_begin_%=\n"
    "s_subb_u32 s93, s93, 0\n"
    "s_lshr_b32 s96, s38, 3\n"
    "s_lshl_b32 s96, s96, 11\n"
    "v_and_b32 v4, 15, v2\n"
    "v_lshlrev_b32 v4, 7, v4\n"
    "v_add_u32 v4, s96, v4\n"
    "s_mov_b32 s96, L_code_end_%=-L_code_begin_%=-4\n"
    "v_min_u32 v4, s96, v4\n"
    "v_and_b32 v4, 0xfffffffc, v4\n"
    "global_load_dword v255, v4, s[92:93]\n"
    "s_lshr_b32 s96, s38, 3\n"
    "s_and_b32 s88, s96, 7\n"
    "s_lshl_b32 s96, s88, 8\n"
    "s_lshl_b32 s97, s39, 6\n"
    "s_add_u32 s96, s96, s97\n"
    "s_mul_i32 s82, s96, 1152\n"
    "s_mov_b32 s56, s20\n"
    "s_and_b32 s57, s21, 0xffff\n"
    "s_mov_b32 s58, 2359296\n"
    "s_mov_b32 s59, 0x20000\n"
    "s_mul_i32 s96, s39, 18432\n"
    "s_add_u32 s83, s96, s85\n"
    "v_lshrrev_b32 v4, 3, v2\n"
    "v_add_u32 v4, 0, v4\n"
    "v_lshrrev_b32 v5, 1, v4\n"
    "v_and_b32 v5, 7, v5\n"
    "v_and_b32 v6, 7, v2\n"
    "v_xor_b32 v5, v5, v6\n"
    "v_mul_u32_u24 v4, 1152, v4\n"
    "v_lshl_add_u32 v204, v5, 4, v4\n"
    "v_lshrrev_b32 v4, 3, v2\n"
    "v_add_u32 v4, 8, v4\n"
    "v_lshrrev_b32 v5, 1, v4\n"
    "v_and_b32 v5, 7, v5\n"
    "v_and_b32 v6, 7, v2\n"
    "v_xor_b32 v5, v5, v6\n"
    "v_mul_u32_u24 v4, 1152, v4\n"
    "v_lshl_add_u32 v205, v5, 4, v4\n"
    "s_and_b32 s96, s38, 7\n"
    "s_lshr_b32 s97, s38, 6\n"
    "s_lshl_b32 s96, s96, 2\n"
    "s_add_u32 s96, s96, s97\n"
    "s_lshl_b32 s96, s96, 2\n"
    "s_add_u32 s96, s96, s39\n"
    "s_mul_i32 s96, s96, 2304\n"
    "s_mul_i32 s97, s88, 294912\n"
    "s_add_u32 s96, s96, s97\n"
    "v_mul_u32_u24 v4, 3641, v2\n"
    "v_lshrrev_b32 v4, 16, v4\n"
    "v_mul_u32_u24 v4, 18, v4\n"
    "v_sub_u32 v4, v2, v4\n"
    "v_lshlrev_b32 v4, 7, v4\n"
    "s_add_u32 m0, s85, 163328\n"
    "s_nop 0\n"
    "buffer_load_dword v4, s[56:59], s96 offen lds\n"
    "s_add_u32 m0, s83, 0\n"
    "s_add_u32 s4, s82, 0\n"
    "buffer_load_dwordx4 v204, s[56:59], s4 offen lds\n"
    "s_and_b32 s86, s38, 7\n"
    "s_lshr_b32 s90, s38, 3\n"
    "s_lshr_b32 s96, s90, 4\n"
    "s_lshl_b32 s86, s86, 1\n"
    "s_add_u32 s86, s86, s96\n"
    "s_add_u32 m0, s83, 1024\n"
    "s_add_u32 s4, s82, 0\n"
    "buffer_load_dwordx4 v205, s[56:59], s4 offen lds\n"
    "s_lshr_b32 s96, s90, 3\n"
    "s_and_b32 s96, s96, 1\n"
    "s_lshl_b32 s97, s86, 1\n"
    "s_add_u32 s87, s96, s97\n"
    "s_add_u32 m0, s83, 2048\n"
    "s_add_u32 s4, s82, 128\n"
    "buffer_load_dwordx4 v204, s[56:59], s4 offen lds\n"
    "s_mov_b32 s40, s22\n"
    "s_and_b32 s41, s23, 0xffff\n"
    "s_mov_b32 s42, 5308416\n"
    "s_mov_b32 s43, 0x20000\n"
    "s_add_u32 m0, s83, 3072\n"
    "s_add_u32 s4, s82, 128\n"
    "buffer_load_dwordx4 v205, s[56:59], s4 offen lds\n"
    "s_mov_b32 s44, s24\n"
    "s_and_b32 s45, s25, 0xffff\n"
    "s_mov_b32 s46, 5308416\n"
    "s_mov_b32 s47, 0x20000\n"
    "s_add_u32 m0, s83, 4096\n"
    "s_add_u32 s4, s82, 256\n"
    "buffer_load_dwordx4 v204, s[56:59], s4 offen lds\n"
    "s_mov_b32 s48, s34\n"
    "s_and_b32 s49, s35, 0xffff\n"
    "s_mov_b32 s51, 0x20000\n"
    "s_add_u32 m0, s83, 5120\n"
    "s_add_u32 s4, s82, 256\n"
    "buffer_load_dwordx4 v205, s[56:59], s4 offen lds\n"
    "s_mov_b32 s52, s34\n"
    "s_and_b32 s53, s35, 0xffff\n"
    "s_mov_b32 s55, 0x20000\n"
    "s_add_u32 m0, s83, 6144\n"
    "s_add_u32 s4, s82, 384\n"
    "buffer_load_dwordx4 v204, s[56:59], s4 offen lds\n"
    "s_mov_b32 s50, 0\n"
    "s_mov_b32 s54, 18874368\n"
    "s_mov_b32 s72, 18874368\n"
    "s_add_u32 m0, s83, 7168\n"
    "s_add_u32 s4, s82, 384\n"
    "buffer_load_dwordx4 v205, s[56:59], s4 offen lds\n"
    "s_mul_i32 s96, s87, 144\n"
    "s_and_b32 s97, s87, 1\n"
    "s_lshl_b32 s98, s97, 4\n"
    "s_add_u32 s98, s98, s96\n"
    "s_add_u32 m0, s83, 8192\n"
    "s_add_u32 s4, s82, 512\n"
    "buffer_load_dwordx4 v204, s[56:59], s4 offen lds\n"
    "s_xor_b32 s99, s97, 1\n"
    "s_lshl_b32 s99, s99, 7\n"
    "s_add_u32 s99, s99, s96\n"
    "s_add_u32 m0, s83, 9216\n"
    "s_add_u32 s4, s82, 512\n"
    "buffer_load_dwordx4 v205, s[56:59], s4 offen lds\n"
    "v_mul_u32_u24 v4, 3641, v2\n"
    "v_lshrrev_b32 v4, 16, v4\n"
    "v_mul_u32_u24 v5, 18, v4\n"
    "v_sub_u32 v5, v2, v5\n"
    "v_and_b32 v4, 1, v4\n"
    "v_mul_u32_u24 v4, 60, v4\n"
    "s_mul_i32 s92, s88, 72\n"
    "s_mul_i32 s93, s39, 18\n"
    "s_add_u32 s92, s92, s93\n"
    "v_add_u32 v5, s92, v5\n"
    "s_mov_b32 s92, 9216\n"
    "v_mul_lo_u32 v6, s92, v5\n"
    "v_add_u32 v211, v6, v4\n"
    "v_lshlrev_b32 v6, 13, v5\n"
    "v_add_u32 v212, v6, v4\n"
    "s_add_u32 m0, s83, 10240\n"
    "s_add_u32 s4, s82, 640\n"
    "buffer_load_dwordx4 v204, s[56:59], s4 offen lds\n"
    "s_lshl_b32 s86, s88, 4\n"
    "s_lshl_b32 s90, s39, 2\n"
    "s_add_u32 s86, s86, s90\n"
    "s_mul_i32 s86, s86, 147456\n"
    "s_add_u32 m0, s83, 11264\n"
    "s_add_u32 s4, s82, 640\n"
    "buffer_load_dwordx4 v205, s[56:59], s4 offen lds\n"
    "s_add_u32 m0, s83, 12288\n"
    "s_add_u32 s4, s82, 768\n"
    "buffer_load_dwordx4 v204, s[56:59], s4 offen lds\n"
    "s_lshr_b32 s90, s98, 5\n"
    "s_sub_u32 s90, s90, 1\n"
    "s_lshl_b32 s90, s90, 10\n"
    "s_add_u32 s73, s86, s90\n"
    "s_add_u32 m0, s83, 13312\n"
    "s_add_u32 s4, s82, 768\n"
    "buffer_load_dwordx4 v205, s[56:59], s4 offen lds\n"
    "s_lshr_b32 s90, s99, 5\n"
    "s_lshl_b32 s90, s90, 10\n"
    "s_add_u32 s77, s86, s90\n"
    "s_lshl_b32 s90, s97, 3\n"
    "s_add_u32 s77, s77, s90\n"
    "s_add_u32 m0, s83, 14336\n"
    "s_add_u32 s4, s82, 896\n"
    "buffer_load_dwordx4 v204, s[56:59], s4 offen lds\n"
    "s_add_u32 s74, s73, 147456\n"
    "s_add_u32 s78, s77, 147456\n"
    "s_add_u32 m0, s83, 15360\n"
    "s_add_u32 s4, s82, 896\n"
    "buffer_load_dwordx4 v205, s[56:59], s4 offen lds\n"
    "s_add_u32 s75, s73, 294912\n"
    "s_add_u32 s79, s77, 294912\n"
    "s_add_u32 m0, s83, 16384\n"
    "s_add_u32 s4, s82, 1024\n"
    "buffer_load_dwordx4 v204, s[56:59], s4 offen lds\n"
    "s_add_u32 s76, s73, 442368\n"
    "s_add_u32 s80, s77, 442368\n"
    "s_add_u32 m0, s83, 17408\n"
    "s_add_u32 s4, s82, 1024\n"
    "buffer_load_dwordx4 v205, s[56:59], s4 offen lds\n"
    "v_lshrrev_b32 v4, 4, v2\n"
    "v_lshlrev_b32 v4, 3, v4\n"
    "v_bfe_u32 v5, v2, 2, 2\n"
    "v_add_u32 v4, v4, v5\n"
    "s_add_u32 m0, s83, 73728\n"
    "s_add_u32 s4, s82, 18432\n"
    "buffer_load_dwordx4 v204, s[56:59], s4 offen lds\n"
    "v_and_b32 v5, 3, v2\n"
    "v_lshrrev_b32 v6, 4, v2\n"
    "v_and_b32 v6, 1, v6\n"
    "v_lshlrev_b32 v6, 1, v6\n"
    "v_xor_b32 v5, v5, v6\n"
    "v_lshlrev_b32 v5, 4, v5\n"
    "s_add_u32 m0, s83, 74752\n"
    "s_add_u32 s4, s82, 18432\n"
    "buffer_load_dwordx4 v205, s[56:59], s4 offen lds\n"
    "s_mov_b32 s96, 9216\n"
    "v_mul_lo_u32 v6, s96, v4\n"
    "v_add_u32 v250, v6, v5\n"
    "s_add_u32 m0, s83, 75776\n"
    "s_add_u32 s4, s82, 18560\n"
    "buffer_load_dwordx4 v204, s[56:59], s4 offen lds\n"
    "v_lshlrev_b32 v6, 13, v4\n"
    "v_add_u32 v251, v6, v5\n"
    "s_add_u32 m0, s83, 76800\n"
    "s_add_u32 s4, s82, 18560\n"
    "buffer_load_dwordx4 v205, s[56:59], s4 offen lds\n"
    "v_lshlrev_b32 v252, 4, v2\n"
    "s_add_u32 m0, s83, 77824\n"
    "s_add_u32 s4, s82, 18688\n"
    "buffer_load_dwordx4 v204, s[56:59], s4 offen lds\n"
    "s_lshl_b32 s86, s39, 10\n"
    "s_add_u32 s91, s85, s86\n"
    "s_add_u32 m0, s83, 78848\n"
    "s_add_u32 s4, s82, 18688\n"
    "buffer_load_dwordx4 v205, s[56:59], s4 offen lds\n"
    "s_add_u32 m0, s83, 79872\n"
    "s_add_u32 s4, s82, 18816\n"
    "buffer_load_dwordx4 v204, s[56:59], s4 offen lds\n"
    "s_lshr_b32 s86, s39, 1\n"
    "s_lshl_b32 s86, s86, 5\n"
    "s_and_b32 s90, s39, 1\n"
    "s_lshl_b32 s90, s90, 2\n"
    "s_add_u32 s97, s86, s90\n"
    "s_add_u32 m0, s83, 80896\n"
    "s_add_u32 s4, s82, 18816\n"
    "buffer_load_dwordx4 v205, s[56:59], s4 offen lds\n"
    "s_mul_i32 s86, s97, 9216\n"
    "s_mov_b32 s81, s86\n"
    "s_add_u32 m0, s83, 81920\n"
    "s_add_u32 s4, s82, 18944\n"
    "buffer_load_dwordx4 v204, s[56:59], s4 offen lds\n"
    "s_mul_i32 s90, s97, 8192\n"
    "s_mov_b32 s2, s90\n"
    "s_lshl_b32 s96, s87, 8\n"
    "s_add_u32 s89, s96, s90\n"
    "s_add_u32 m0, s83, 82944\n"
    "s_add_u32 s4, s82, 18944\n"
    "buffer_load_dwordx4 v205, s[56:59], s4 offen lds\n"
    "s_mov_b32 s68, 589824\n"
    "s_add_u32 m0, s83, 83968\n"
    "s_add_u32 s4, s82, 19072\n"
    "buffer_load_dwordx4 v204, s[56:59], s4 offen lds\n"
    "s_lshl_b32 s90, s99, 1\n"
    "s_add_u32 s65, s86, s90\n"
    "s_mov_b32 s66, s65\n"
    "s_add_u32 s64, s91, 0\n"
    "s_add_u32 m0, s83, 84992\n"
    "s_add_u32 s4, s82, 19072\n"
    "buffer_load_dwordx4 v205, s[56:59], s4 offen lds\n"
    "s_lshl_b32 s90, s98, 1\n"
    "s_add_u32 s5, s86, s90\n"
    "s_add_u32 m0, s83, 86016\n"
    "s_add_u32 s4, s82, 19200\n"
    "buffer_load_dwordx4 v204, s[56:59], s4 offen lds\n"
    "v_and_b32 v4, 15, v2\n"
    "v_lshrrev_b32 v5, 1, v4\n"
    "v_and_b32 v5, 7, v5\n"
    "v_lshrrev_b32 v6, 4, v2\n"
    "v_or_b32 v6, 0, v6\n"
    "v_xor_b32 v5, v5, v6\n"
    "v_lshlrev_b32 v5, 4, v5\n"
    "v_lshl_add_u32 v206, v4, 7, v5\n"
    "v_add_u32 v206, s83, v206\n"
    "v_add_u32 v208, 73728, v206\n"
    "s_add_u32 m0, s83, 87040\n"
    "s_add_u32 s4, s82, 19200\n"
    "buffer_load_dwordx4 v205, s[56:59], s4 offen lds\n"
    "v_and_b32 v4, 15, v2\n"
    "v_lshrrev_b32 v5, 1, v4\n"
    "v_and_b32 v5, 7, v5\n"
    "v_lshrrev_b32 v6, 4, v2\n"
    "v_or_b32 v6, 4, v6\n"
    "v_xor_b32 v5, v5, v6\n"
    "v_lshlrev_b32 v5, 4, v5\n"
    "v_lshl_add_u32 v207, v4, 7, v5\n"
    "v_add_u32 v207, s83, v207\n"
    "v_add_u32 v209, 73728, v207\n"
    "s_add_u32 m0, s83, 88064\n"
    "s_add_u32 s4, s82, 19328\n"
    "buffer_load_dwordx4 v204, s[56:59], s4 offen lds\n"
    "v_lshrrev_b32 v4, 2, v2\n"
    "v_and_b32 v5, 3, v2\n"
    "v_lshlrev_b32 v5, 3, v5\n"
    "v_lshl_add_u32 v5, v4, 6, v5\n"
    "v_add_u32 v5, s85, v5\n"
    "v_lshrrev_b32 v6, 4, v2\n"
    "v_and_b32 v6, 1, v6\n"
    "v_xor_b32 v7, 0, v6\n"
    "v_lshl_add_u32 v200, v7, 5, v5\n"
    "v_add_u32 v202, 36864, v200\n"
    "v_xor_b32 v7, 1, v6\n"
    "v_lshl_add_u32 v201, v7, 5, v5\n"
    "v_add_u32 v203, 36864, v201\n"
    "s_add_u32 m0, s83, 89088\n"
    "s_add_u32 s4, s82, 19328\n"
    "buffer_load_dwordx4 v205, s[56:59], s4 offen lds\n"
    "s_mov_b32 s69, 73728\n"
    "s_mov_b32 s70, 73728\n"
    "s_add_u32 m0, s83, 90112\n"
    "s_add_u32 s4, s82, 19456\n"
    "buffer_load_dwordx4 v204, s[56:59], s4 offen lds\n"
    "v_mov_b32 v112, 0x3f803f80\n"
    "v_mov_b32 v113, 0x3f803f80\n"
    "v_mov_b32 v114, 0x3f803f80\n"
    "v_mov_b32 v115, 0x3f803f80\n"
    "v_and_b32 v160, 15, v2\n"
    "v_lshrrev_b32 v161, 4, v2\n"
    "v_lshrrev_b32 v162, 2, v160\n"
    "v_cmp_eq_u32 s[18:19], v161, v162\n"
    "v_and_b32 v161, 1, v160\n"
    "v_cmp_eq_u32 s[14:15], 1, v161\n"
    "v_and_b32 v162, 2, v160\n"
    "v_cmp_eq_u32 s[16:17], 2, v162\n"
    "v_lshlrev_b32 v161, 4, v161\n"
    "v_mov_b32 v163, 0x3f80\n"
    "v_lshlrev_b32 v163, v161, v163\n"
    "v_cndmask_b32 v163, 0, v163, s[18:19]\n"
    "v_cndmask_b32 v118, v163, 0, s[16:17]\n"
    "v_cndmask_b32 v119, 0, v163, s[16:17]\n"
    "v_mov_b32 v116, 0\n"
    "v_mov_b32 v117, 0\n"
    "v_mov_b32 v120, 0\n"
    "v_mov_b32 v121, 0\n"
    "v_mov_b32 v170, 0x3ae38e39\n"
    "s_add_u32 m0, s83, 91136\n"
    "s_add_u32 s4, s82, 19456\n"
    "buffer_load_dwordx4 v205, s[56:59], s4 offen lds\n"
    "s_waitcnt vmcnt(18)\n"
    "ds_read_b128 a[0:3], v206 offset:0\n"
    "ds_read_b128 a[4:7], v207 offset:0\n"
    "ds_read_b128 a[8:11], v206 offset:2048\n"
    "ds_read_b128 a[12:15], v207 offset:2048\n"
    "ds_read_b128 a[16:19], v206 offset:4096\n"
    "ds_read_b128 a[20:23], v207 offset:4096\n"
    "ds_read_b128 a[24:27], v206 offset:6144\n"
    "ds_read_b128 a[28:31], v207 offset:6144\n"
    "ds_read_b128 a[32:35], v206 offset:8192\n"
    "ds_read_b128 a[144:147], v207 offset:8192\n"
    "ds_read_b128 a[148:151], v206 offset:10240\n"
    "ds_read_b128 a[152:155], v207 offset:10240\n"
    "ds_read_b128 a[156:159], v206 offset:12288\n"
    "ds_read_b128 a[160:163], v207 offset:12288\n"
    "ds_read_b128 a[164:167], v206 offset:14336\n"
    "ds_read_b128 a[168:171], v207 offset:14336\n"
    "ds_read_b128 a[172:175], v206 offset:16384\n"
    "ds_read_b128 a[176:179], v207 offset:16384\n"
    "s_waitcnt lgkmcnt(0)\n"
    "v_mfma_f32_16x16x32_bf16 v[152:155], v[112:115], a[0:3], 0\n"
    "v_mfma_f32_16x16x32_bf16 v[152:155], v[112:115], a[4:7], v[152:155]\n"
    "v_mfma_f32_16x16x32_bf16 v[152:155], v[112:115], a[8:11], v[152:155]\n"
    "v_mfma_f32_16x16x32_bf16 v[152:155], v[112:115], a[12:15], v[152:155]\n"
    "v_mfma_f32_16x16x32_bf16 v[152:155], v[112:115], a[16:19], v[152:155]\n"
    "v_mfma_f32_16x16x32_bf16 v[152:155], v[112:115], a[20:23], v[152:155]\n"
    "v_mfma_f32_16x16x32_bf16 v[152:155], v[112:115], a[24:27], v[152:155]\n"
    "v_mfma_f32_16x16x32_bf16 v[152:155], v[112:115], a[28:31], v[152:155]\n"
    "v_mfma_f32_16x16x32_bf16 v[152:155], v[112:115], a[32:35], v[152:155]\n"
    "v_mfma_f32_16x16x32_bf16 v[152:155], v[112:115], a[144:147], v[152:155]\n"
    "v_mfma_f32_16x16x32_bf16 v[152:155], v[112:115], a[148:151], v[152:155]\n"
    "v_mfma_f32_16x16x32_bf16 v[152:155], v[112:115], a[152:155], v[152:155]\n"
    "v_mfma_f32_16x16x32_bf16 v[152:155], v[112:115], a[156:159], v[152:155]\n"
    "v_mfma_f32_16x16x32_bf16 v[152:155], v[112:115], a[160:163], v[152:155]\n"
    "v_mfma_f32_16x16x32_bf16 v[152:155], v[112:115], a[164:167], v[152:155]\n"
    "v_mfma_f32_16x16x32_bf16 v[152:155], v[112:115], a[168:171], v[152:155]\n"
    "v_mfma_f32_16x16x32_bf16 v[152:155], v[112:115], a[172:175], v[152:155]\n"
    "v_mfma_f32_16x16x32_bf16 v[152:155], v[112:115], a[176:179], v[152:155]\n"
    "s_nop 7\n"
    "v_mul_f32 v124, 0xbae38e39, v152\n"
    "v_mul_f32 v125, 0xbae38e39, v153\n"
    "v_mul_f32 v126, 0xbae38e39, v154\n"
    "v_mul_f32 v127, 0xbae38e39, v155\n"
    "s_nop 1\n"
    "v_mfma_f32_16x16x32_bf16 v[128:131], v[118:121], a[0:3], v[124:127]\n"
    "v_mfma_f32_16x16x32_bf16 v[132:135], v[116:119], a[0:3], v[124:127]\n"
    "s_add_u32 m0, s83, 0\n"
    "s_add_u32 s4, s82, 36864\n"
    "buffer_load_dwordx4 v204, s[56:59], s4 offen lds\n"
    "s_add_u32 m0, s83, 1024\n"
    "s_add_u32 s4, s82, 36864\n"
    "buffer_load_dwordx4 v205, s[56:59], s4 offen lds\n"
    "v_mfma_f32_16x16x32_bf16 v[136:139], v[118:121], a[4:7], v[124:127]\n"
    "v_mfma_f32_16x16x32_bf16 v[140:143], v[116:119], a[4:7], v[124:127]\n"
    "s_nop 3\n"
    "v_cvt_pk_bf16_f32 v160, v128, v129\n"
    "v_cvt_pk_bf16_f32 v161, v130, v131\n"
    "v_cvt_pk_bf16_f32 v162, v132, v133\n"
    "v_cvt_pk_bf16_f32 v163, v134, v135\n"
    "v_accvgpr_write_b32 a0, v160\n"
    "v_accvgpr_write_b32 a1, v161\n"
    "v_accvgpr_write_b32 a2, v162\n"
    "v_accvgpr_write_b32 a3, v163\n"
    "s_add_u32 m0, s83, 2048\n"
    "s_add_u32 s4, s82, 36992\n"
    "buffer_load_dwordx4 v204, s[56:59], s4 offen lds\n"
    "s_add_u32 m0, s83, 3072\n"
    "s_add_u32 s4, s82, 36992\n"
    "buffer_load_dwordx4 v205, s[56:59], s4 offen lds\n"
    "v_mfma_f32_16x16x32_bf16 v[144:147], v[118:121], a[8:11], v[124:127]\n"
    "v_cvt_pk_bf16_f32 v160, v136, v137\n"
    "v_cvt_pk_bf16_f32 v161, v138, v139\n"
    "v_mfma_f32_16x16x32_bf16 v[148:151], v[116:119], a[8:11], v[124:127]\n"
    "v_cvt_pk_bf16_f32 v162, v140, v141\n"
    "v_cvt_pk_bf16_f32 v163, v142, v143\n"
    "v_accvgpr_write_b32 a4, v160\n"
    "v_accvgpr_write_b32 a5, v161\n"
    "v_mfma_f32_16x16x32_bf16 v[156:159], a[0:3], a[0:3], 0\n"
    "v_accvgpr_write_b32 a6, v162\n"
    "v_accvgpr_write_b32 a7, v163\n"
    "s_add_u32 m0, s83, 4096\n"
    "s_add_u32 s4, s82, 37120\n"
    "buffer_load_dwordx4 v204, s[56:59], s4 offen lds\n"
    "s_add_u32 m0, s83, 5120\n"
    "s_add_u32 s4, s82, 37120\n"
    "buffer_load_dwordx4 v205, s[56:59], s4 offen lds\n"
    "s_waitcnt vmcnt(6)\n"
    "v_mfma_f32_16x16x32_bf16 v[128:131], v[118:121], a[12:15], v[124:127]\n"
    "ds_read_b128 a[36:39], v208 offset:0\n"
    "v_cvt_pk_bf16_f32 v160, v144, v145\n"
    "v_cvt_pk_bf16_f32 v161, v146, v147\n"
    "v_mfma_f32_16x16x32_bf16 v[132:135], v[116:119], a[12:15], v[124:127]\n"
    "ds_read_b128 a[40:43], v209 offset:0\n"
    "v_cvt_pk_bf16_f32 v162, v148, v149\n"
    "v_cvt_pk_bf16_f32 v163, v150, v151\n"
    "ds_read_b128 a[44:47], v208 offset:2048\n"
    "v_accvgpr_write_b32 a8, v160\n"
    "v_accvgpr_write_b32 a9, v161\n"
    "v_mfma_f32_16x16x32_bf16 v[156:159], a[4:7], a[4:7], v[156:159]\n"
    "v_accvgpr_write_b32 a10, v162\n"
    "v_accvgpr_write_b32 a11, v163\n"
    "s_add_u32 m0, s83, 6144\n"
    "s_add_u32 s4, s82, 37248\n"
    "buffer_load_dwordx4 v204, s[56:59], s4 offen lds\n"
    "s_add_u32 m0, s83, 7168\n"
    "s_add_u32 s4, s82, 37248\n"
    "buffer_load_dwordx4 v205, s[56:59], s4 offen lds\n"
    "v_mfma_f32_16x16x32_bf16 v[136:139], v[118:121], a[16:19], v[124:127]\n"
    "ds_read_b128 a[48:51], v209 offset:2048\n"
    "v_cvt_pk_bf16_f32 v160, v128, v129\n"
    "v_cvt_pk_bf16_f32 v161, v130, v131\n"
    "v_mfma_f32_16x16x32_bf16 v[140:143], v[116:119], a[16:19], v[124:127]\n"
    "ds_read_b128 a[52:55], v208 offset:4096\n"
    "v_cvt_pk_bf16_f32 v162, v132, v133\n"
    "v_cvt_pk_bf16_f32 v163, v134, v135\n"
    "ds_read_b128 a[56:59], v209 offset:4096\n"
    "v_accvgpr_write_b32 a12, v160\n"
    "v_accvgpr_write_b32 a13, v161\n"
    "v_mfma_f32_16x16x32_bf16 v[156:159], a[8:11], a[8:11], v[156:159]\n"
    "v_accvgpr_write_b32 a14, v162\n"
    "v_accvgpr_write_b32 a15, v163\n"
    "s_add_u32 m0, s83, 8192\n"
    "s_add_u32 s4, s82, 37376\n"
    "buffer_load_dwordx4 v204, s[56:59], s4 offen lds\n"
    "v_mfma_f32_16x16x32_bf16 v[144:147], v[118:121], a[20:23], v[124:127]\n"
    "ds_read_b128 a[60:63], v208 offset:6144\n"
    "v_cvt_pk_bf16_f32 v160, v136, v137\n"
    "v_cvt_pk_bf16_f32 v161, v138, v139\n"
    "v_mfma_f32_16x16x32_bf16 v[148:151], v[116:119], a[20:23], v[124:127]\n"
    "ds_read_b128 a[64:67], v209 offset:6144\n"
    "v_cvt_pk_bf16_f32 v162, v140, v141\n"
    "v_cvt_pk_bf16_f32 v163, v142, v143\n"
    "ds_read_b128 a[68:71], v208 offset:8192\n"
    "v_accvgpr_write_b32 a16, v160\n"
    "v_accvgpr_write_b32 a17, v161\n"
    "v_mfma_f32_16x16x32_bf16 v[156:159], a[12:15], a[12:15], v[156:159]\n"
    "v_accvgpr_write_b32 a18, v162\n"
    "v_accvgpr_write_b32 a19, v163\n"
    "s_add_u32 m0, s83, 9216\n"
    "s_add_u32 s4, s82, 37376\n"
    "buffer_load_dwordx4 v205, s[56:59], s4 offen lds\n"
    "s_add_u32 m0, s83, 10240\n"
    "s_add_u32 s4, s82, 37504\n"
    "buffer_load_dwordx4 v204, s[56:59], s4 offen lds\n"
    "v_mfma_f32_16x16x32_bf16 v[128:131], v[118:121], a[24:27], v[124:127]\n"
    "ds_read_b128 a[180:183], v209 offset:8192\n"
    "v_cvt_pk_bf16_f32 v160, v144, v145\n"
    "v_cvt_pk_bf16_f32 v161, v146, v147\n"
    "v_mfma_f32_16x16x32_bf16 v[132:135], v[116:119], a[24:27], v[124:127]\n"
    "ds_read_b128 a[184:187], v208 offset:10240\n"
    "v_cvt_pk_bf16_f32 v162, v148, v149\n"
    "v_cvt_pk_bf16_f32 v163, v150, v151\n"
    "ds_read_b128 a[188:191], v209 offset:10240\n"
    "v_accvgpr_write_b32 a20, v160\n"
    "v_accvgpr_write_b32 a21, v161\n"
    "v_mfma_f32_16x16x32_bf16 v[156:159], a[16:19], a[16:19], v[156:159]\n"
    "v_accvgpr_write_b32 a22, v162\n"
    "v_accvgpr_write_b32 a23, v163\n"
    "s_add_u32 m0, s83, 11264\n"
    "s_add_u32 s4, s82, 37504\n"
    "buffer_load_dwordx4 v205, s[56:59], s4 offen lds\n"
    "s_add_u32 m0, s83, 12288\n"
    "s_add_u32 s4, s82, 37632\n"
    "buffer_load_dwordx4 v204, s[56:59], s4 offen lds\n"
    "v_mfma_f32_16x16x32_bf16 v[136:139], v[118:121], a[28:31], v[124:127]\n"
    "ds_read_b128 a[192:195], v208 offset:12288\n"
    "v_cvt_pk_bf16_f32 v160, v128, v129\n"
    "v_cvt_pk_bf16_f32 v161, v130, v131\n"
    "v_mfma_f32_16x16x32_bf16 v[140:143], v[116:119], a[28:31], v[124:127]\n"
    "ds_read_b128 a[196:199], v209 offset:12288\n"
    "v_cvt_pk_bf16_f32 v162, v132, v133\n"
    "v_cvt_pk_bf16_f32 v163, v134, v135\n"
    "ds_read_b128 a[200:203], v208 offset:14336\n"
    "v_accvgpr_write_b32 a24, v160\n"
    "v_accvgpr_write_b32 a25, v161\n"
    "v_mfma_f32_16x16x32_bf16 v[156:159], a[20:23], a[20:23], v[156:159]\n"
    "v_accvgpr_write_b32 a26, v162\n"
    "v_accvgpr_write_b32 a27, v163\n"
    "s_add_u32 m0, s83, 13312\n"
    "s_add_u32 s4, s82, 37632\n"
    "buffer_load_dwordx4 v205, s[56:59], s4 offen lds\n"
    "s_add_u32 m0, s83, 14336\n"
    "s_add_u32 s4, s82, 37760\n"
    "buffer_load_dwordx4 v204, s[56:59], s4 offen lds\n"
    "v_mfma_f32_16x16x32_bf16 v[144:147], v[118:121], a[32:35], v[124:127]\n"
    "ds_read_b128 a[204:207], v209 offset:14336\n"
    "v_cvt_pk_bf16_f32 v160, v136, v137\n"
    "v_cvt_pk_bf16_f32 v161, v138, v139\n"
    "v_mfma_f32_16x16x32_bf16 v[148:151], v[116:119], a[32:35], v[124:127]\n"
    "ds_read_b128 a[208:211], v208 offset:16384\n"
    "v_cvt_pk_bf16_f32 v162, v140, v141\n"
    "v_cvt_pk_bf16_f32 v163, v142, v143\n"
    "ds_read_b128 a[212:215], v209 offset:16384\n"
    "v_accvgpr_write_b32 a28, v160\n"
    "v_accvgpr_write_b32 a29, v161\n"
    "v_mfma_f32_16x16x32_bf16 v[156:159], a[24:27], a[24:27], v[156:159]\n"
    "v_accvgpr_write_b32 a30, v162\n"
    "v_accvgpr_write_b32 a31, v163\n"
    "s_add_u32 m0, s83, 15360\n"
    "s_add_u32 s4, s82, 37760\n"
    "buffer_load_dwordx4 v205, s[56:59], s4 offen lds\n"
    "s_add_u32 m0, s83, 16384\n"
    "s_add_u32 s4, s82, 37888\n"
    "buffer_load_dwordx4 v204, s[56:59], s4 offen lds\n"
    "v_mfma_f32_16x16x32_bf16 v[128:131], v[118:121], a[144:147], v[124:127]\n"
    "v_cvt_pk_bf16_f32 v160, v144, v145\n"
    "v_cvt_pk_bf16_f32 v161, v146, v147\n"
    "v_mfma_f32_16x16x32_bf16 v[132:135], v[116:119], a[144:147], v[124:127]\n"
    "v_cvt_pk_bf16_f32 v162, v148, v149\n"
    "v_cvt_pk_bf16_f32 v163, v150, v151\n"
    "v_accvgpr_write_b32 a32, v160\n"
    "v_accvgpr_write_b32 a33, v161\n"
    "v_mfma_f32_16x16x32_bf16 v[156:159], a[28:31], a[28:31], v[156:159]\n"
    "v_accvgpr_write_b32 a34, v162\n"
    "v_accvgpr_write_b32 a35, v163\n"
    "s_add_u32 m0, s83, 17408\n"
    "s_add_u32 s4, s82, 37888\n"
    "buffer_load_dwordx4 v205, s[56:59], s4 offen lds\n"
    "s_waitcnt lgkmcnt(0)\n"
    "v_mfma_f32_16x16x32_bf16 v[136:139], v[118:121], a[148:151], v[124:127]\n"
    "v_cvt_pk_bf16_f32 v160, v128, v129\n"
    "v_cvt_pk_bf16_f32 v161, v130, v131\n"
    "v_mfma_f32_16x16x32_bf16 v[140:143], v[116:119], a[148:151], v[124:127]\n"
    "v_cvt_pk_bf16_f32 v162, v132, v133\n"
    "v_cvt_pk_bf16_f32 v163, v134, v135\n"
    "v_accvgpr_write_b32 a144, v160\n"
    "v_accvgpr_write_b32 a145, v161\n"
    "v_mfma_f32_16x16x32_bf16 v[156:159], a[32:35], a[32:35], v[156:159]\n"
    "v_accvgpr_write_b32 a146, v162\n"
    "v_accvgpr_write_b32 a147, v163\n"
    "s_add_u32 m0, s83, 73728\n"
    "s_add_u32 s4, s82, 55296\n"
    "buffer_load_dwordx4 v204, s[56:59], s4 offen lds\n"
    "v_mfma_f32_16x16x32_bf16 v[144:147], v[118:121], a[152:155], v[124:127]\n"
    "v_cvt_pk_bf16_f32 v160, v136, v137\n"
    "v_cvt_pk_bf16_f32 v161, v138, v139\n"
    "v_mfma_f32_16x16x32_bf16 v[148:151], v[116:119], a[152:155], v[124:127]\n"
    "v_cvt_pk_bf16_f32 v162, v140, v141\n"
    "v_cvt_pk_bf16_f32 v163, v142, v143\n"
    "v_mfma_f32_16x16x32_bf16 v[152:155], v[112:115], a[36:39], 0\n"
    "v_accvgpr_write_b32 a148, v160\n"
    "v_accvgpr_write_b32 a149, v161\n"
    "v_mfma_f32_16x16x32_bf16 v[156:159], a[144:147], a[144:147], v[156:159]\n"
    "v_accvgpr_write_b32 a150, v162\n"
    "v_accvgpr_write_b32 a151, v163\n"
    "v_mfma_f32_16x16x32_bf16 v[152:155], v[112:115], a[40:43], v[152:155]\n"
    "v_mfma_f32_16x16x32_bf16 v[152:155], v[112:115], a[44:47], v[152:155]\n"
    "s_add_u32 m0, s83, 74752\n"
    "s_add_u32 s4, s82, 55296\n"
    "buffer_load_dwordx4 v205, s[56:59], s4 offen lds\n"
    "v_mfma_f32_16x16x32_bf16 v[128:131], v[118:121], a[156:159], v[124:127]\n"
    "v_cvt_pk_bf16_f32 v160, v144, v145\n"
    "v_cvt_pk_bf16_f32 v161, v146, v147\n"
    "v_mfma_f32_16x16x32_bf16 v[132:135], v[116:119], a[156:159], v[124:127]\n"
    "v_cvt_pk_bf16_f32 v162, v148, v149\n"
    "v_cvt_pk_bf16_f32 v163, v150, v151\n"
    "v_mfma_f32_16x16x32_bf16 v[152:155], v[112:115], a[48:51], v[152:155]\n"
    "v_accvgpr_write_b32 a152, v160\n"
    "v_accvgpr_write_b32 a153, v161\n"
    "v_mfma_f32_16x16x32_bf16 v[156:159], a[148:151], a[148:151], v[156:159]\n"
    "v_accvgpr_write_b32 a154, v162\n"
    "v_accvgpr_write_b32 a155, v163\n"
    "v_mfma_f32_16x16x32_bf16 v[152:155], v[112:115], a[52:55], v[152:155]\n"
    "v_mfma_f32_16x16x32_bf16 v[152:155], v[112:115], a[56:59], v[152:155]\n"
    "s_add_u32 m0, s83, 75776\n"
    "s_add_u32 s4, s82, 55424\n"
    "buffer_load_dwordx4 v204, s[56:59], s4 offen lds\n"
    "v_mfma_f32_16x16x32_bf16 v[136:139], v[118:121], a[160:163], v[124:127]\n"
    "v_cvt_pk_bf16_f32 v160, v128, v129\n"
    "v_cvt_pk_bf16_f32 v161, v130, v131\n"
    "v_mfma_f32_16x16x32_bf16 v[140:143], v[116:119], a[160:163], v[124:127]\n"
    "v_cvt_pk_bf16_f32 v162, v132, v133\n"
    "v_cvt_pk_bf16_f32 v163, v134, v135\n"
    "v_mfma_f32_16x16x32_bf16 v[152:155], v[112:115], a[60:63], v[152:155]\n"
    "v_accvgpr_write_b32 a156, v160\n"
    "v_accvgpr_write_b32 a157, v161\n"
    "v_mfma_f32_16x16x32_bf16 v[156:159], a[152:155], a[152:155], v[156:159]\n"
    "v_accvgpr_write_b32 a158, v162\n"
    "v_accvgpr_write_b32 a159, v163\n"
    "v_mfma_f32_16x16x32_bf16 v[152:155], v[112:115], a[64:67], v[152:155]\n"
    "v_mfma_f32_16x16x32_bf16 v[152:155], v[112:115], a[68:71], v[152:155]\n"
    "s_add_u32 m0, s83, 76800\n"
    "s_add_u32 s4, s82, 55424\n"
    "buffer_load_dwordx4 v205, s[56:59], s4 offen lds\n"
    "v_mfma_f32_16x16x32_bf16 v[144:147], v[118:121], a[164:167], v[124:127]\n"
    "v_cvt_pk_bf16_f32 v160, v136, v137\n"
    "v_cvt_pk_bf16_f32 v161, v138, v139\n"
    "v_mfma_f32_16x16x32_bf16 v[148:151], v[116:119], a[164:167], v[124:127]\n"
    "v_cvt_pk_bf16_f32 v162, v140, v141\n"
    "v_cvt_pk_bf16_f32 v163, v142, v143\n"
    "v_mfma_f32_16x16x32_bf16 v[152:155], v[112:115], a[180:183], v[152:155]\n"
    "v_accvgpr_write_b32 a160, v160\n"
    "v_accvgpr_write_b32 a161, v161\n"
    "v_mfma_f32_16x16x32_bf16 v[156:159], a[156:159], a[156:159], v[156:159]\n"
    "v_accvgpr_write_b32 a162, v162\n"
    "v_accvgpr_write_b32 a163, v163\n"
    "v_mfma_f32_16x16x32_bf16 v[152:155], v[112:115], a[184:187], v[152:155]\n"
    "v_mfma_f32_16x16x32_bf16 v[152:155], v[112:115], a[188:191], v[152:155]\n"
    "s_add_u32 m0, s83, 77824\n"
    "s_add_u32 s4, s82, 55552\n"
    "buffer_load_dwordx4 v204, s[56:59], s4 offen lds\n"
    "v_mfma_f32_16x16x32_bf16 v[128:131], v[118:121], a[168:171], v[124:127]\n"
    "v_cvt_pk_bf16_f32 v160, v144, v145\n"
    "v_cvt_pk_bf16_f32 v161, v146, v147\n"
    "v_mfma_f32_16x16x32_bf16 v[132:135], v[116:119], a[168:171], v[124:127]\n"
    "v_cvt_pk_bf16_f32 v162, v148, v149\n"
    "v_cvt_pk_bf16_f32 v163, v150, v151\n"
    "v_mfma_f32_16x16x32_bf16 v[152:155], v[112:115], a[192:195], v[152:155]\n"
    "v_accvgpr_write_b32 a164, v160\n"
    "v_accvgpr_write_b32 a165, v161\n"
    "v_mfma_f32_16x16x32_bf16 v[156:159], a[160:163], a[160:163], v[156:159]\n"
    "v_accvgpr_write_b32 a166, v162\n"
    "v_accvgpr_write_b32 a167, v163\n"
    "v_mfma_f32_16x16x32_bf16 v[152:155], v[112:115], a[196:199], v[152:155]\n"
    "s_add_u32 m0, s83, 78848\n"
    "s_add_u32 s4, s82, 55552\n"
    "buffer_load_dwordx4 v205, s[56:59], s4 offen lds\n"
    "v_mfma_f32_16x16x32_bf16 v[136:139], v[118:121], a[172:175], v[124:127]\n"
    "v_cvt_pk_bf16_f32 v160, v128, v129\n"
    "v_cvt_pk_bf16_f32 v161, v130, v131\n"
    "v_mfma_f32_16x16x32_bf16 v[140:143], v[116:119], a[172:175], v[124:127]\n"
    "v_cvt_pk_bf16_f32 v162, v132, v133\n"
    "v_cvt_pk_bf16_f32 v163, v134, v135\n"
    "v_mfma_f32_16x16x32_bf16 v[152:155], v[112:115], a[200:203], v[152:155]\n"
    "v_accvgpr_write_b32 a168, v160\n"
    "v_accvgpr_write_b32 a169, v161\n"
    "v_mfma_f32_16x16x32_bf16 v[156:159], a[164:167], a[164:167], v[156:159]\n"
    "v_accvgpr_write_b32 a170, v162\n"
    "v_accvgpr_write_b32 a171, v163\n"
    "v_mfma_f32_16x16x32_bf16 v[152:155], v[112:115], a[204:207], v[152:155]\n"
    "s_add_u32 m0, s83, 79872\n"
    "s_add_u32 s4, s82, 55680\n"
    "buffer_load_dwordx4 v204, s[56:59], s4 offen lds\n"
    "v_mfma_f32_16x16x32_bf16 v[144:147], v[118:121], a[176:179], v[124:127]\n"
    "v_cvt_pk_bf16_f32 v160, v136, v137\n"
    "v_cvt_pk_bf16_f32 v161, v138, v139\n"
    "v_mfma_f32_16x16x32_bf16 v[148:151], v[116:119], a[176:179], v[124:127]\n"
    "v_cvt_pk_bf16_f32 v162, v140, v141\n"
    "v_cvt_pk_bf16_f32 v163, v142, v143\n"
    "v_mfma_f32_16x16x32_bf16 v[152:155], v[112:115], a[208:211], v[152:155]\n"
    "v_accvgpr_write_b32 a172, v160\n"
    "v_accvgpr_write_b32 a173, v161\n"
    "v_mfma_f32_16x16x32_bf16 v[156:159], a[168:171], a[168:171], v[156:159]\n"
    "v_accvgpr_write_b32 a174, v162\n"
    "v_accvgpr_write_b32 a175, v163\n"
    "v_mfma_f32_16x16x32_bf16 v[152:155], v[112:115], a[212:215], v[152:155]\n"
    "s_add_u32 m0, s83, 80896\n"
    "s_add_u32 s4, s82, 55680\n"
    "buffer_load_dwordx4 v205, s[56:59], s4 offen lds\n"
    "s_nop 2\n"
    "v_mfma_f32_16x16x32_bf16 v[156:159], a[172:175], a[172:175], v[156:159]\n"
    "s_nop 2\n"
    "v_cvt_pk_bf16_f32 v160, v144, v145\n"
    "v_cvt_pk_bf16_f32 v161, v146, v147\n"
    "v_cvt_pk_bf16_f32 v162, v148, v149\n"
    "v_cvt_pk_bf16_f32 v163, v150, v151\n"
    "v_accvgpr_write_b32 a176, v160\n"
    "v_accvgpr_write_b32 a177, v161\n"
    "v_accvgpr_write_b32 a178, v162\n"
    "v_accvgpr_write_b32 a179, v163\n"
    "s_nop 1\n"
    "s_nop 1\n"
    "v_mfma_f32_16x16x32_bf16 v[156:159], a[176:179], a[176:179], v[156:159]\n"
    "s_nop 7\n"
    "v_mul_f32 v164, 0xbae38e39, v152\n"
    "v_mul_f32 v165, 0xbae38e39, v153\n"
    "v_mul_f32 v166, 0xbae38e39, v154\n"
    "v_mul_f32 v167, 0xbae38e39, v155\n"
    "v_cndmask_b32 v168, v156, v157, s[14:15]\n"
    "v_cndmask_b32 v169, v158, v159, s[14:15]\n"
    "v_cndmask_b32 v168, v168, v169, s[16:17]\n"
    "v_cndmask_b32 v168, 0, v168, s[18:19]\n"
    "v_mov_b32 v169, v168\n"
    "s_nop 1\n"
    "v_permlane16_swap_b32 v168, v169\n"
    "s_nop 1\n"
    "v_add_f32 v168, v168, v169\n"
    "v_mov_b32 v169, v168\n"
    "s_nop 1\n"
    "v_permlane32_swap_b32 v168, v169\n"
    "s_nop 1\n"
    "v_add_f32 v168, v168, v169\n"
    "v_mov_b32 v169, 0x3727c5ac\n"
    "v_fma_f32 v240, v168, v170, v169\n"
    "v_rsq_f32 v168, v240\n"
    "s_nop 0\n"
    "v_mul_f32 v236, 0xbfb8aa3b, v168\n"
    "v_mul_f32 v253, 0x3e0293ee, v168\n"
    "v_mfma_f32_16x16x32_bf16 v[128:131], v[118:121], a[36:39], v[164:167]\n"
    "v_mfma_f32_16x16x32_bf16 v[132:135], v[116:119], a[36:39], v[164:167]\n"
    "s_add_u32 m0, s83, 81920\n"
    "s_add_u32 s4, s82, 55808\n"
    "buffer_load_dwordx4 v204, s[56:59], s4 offen lds\n"
    "v_mfma_f32_16x16x32_bf16 v[136:139], v[118:121], a[40:43], v[164:167]\n"
    "v_mfma_f32_16x16x32_bf16 v[140:143], v[116:119], a[40:43], v[164:167]\n"
    "s_nop 3\n"
    "v_cvt_pk_bf16_f32 v160, v128, v129\n"
    "v_cvt_pk_bf16_f32 v161, v130, v131\n"
    "v_cvt_pk_bf16_f32 v162, v132, v133\n"
    "v_cvt_pk_bf16_f32 v163, v134, v135\n"
    "v_accvgpr_write_b32 a36, v160\n"
    "v_accvgpr_write_b32 a37, v161\n"
    "v_accvgpr_write_b32 a38, v162\n"
    "v_accvgpr_write_b32 a39, v163\n"
    "s_add_u32 m0, s83, 82944\n"
    "s_add_u32 s4, s82, 55808\n"
    "buffer_load_dwordx4 v205, s[56:59], s4 offen lds\n"
    "v_mfma_f32_16x16x32_bf16 v[144:147], v[118:121], a[44:47], v[164:167]\n"
    "v_cvt_pk_bf16_f32 v160, v136, v137\n"
    "v_cvt_pk_bf16_f32 v161, v138, v139\n"
    "v_mfma_f32_16x16x32_bf16 v[148:151], v[116:119], a[44:47], v[164:167]\n"
    "v_cvt_pk_bf16_f32 v162, v140, v141\n"
    "v_cvt_pk_bf16_f32 v163, v142, v143\n"
    "v_accvgpr_write_b32 a40, v160\n"
    "v_accvgpr_write_b32 a41, v161\n"
    "v_mfma_f32_16x16x32_bf16 v[156:159], a[36:39], a[36:39], 0\n"
    "v_accvgpr_write_b32 a42, v162\n"
    "v_accvgpr_write_b32 a43, v163\n"
    "s_add_u32 m0, s83, 83968\n"
    "s_add_u32 s4, s82, 55936\n"
    "buffer_load_dwordx4 v204, s[56:59], s4 offen lds\n"
    "s_waitcnt vmcnt(11)\n"
    "v_mfma_f32_16x16x32_bf16 v[128:131], v[118:121], a[48:51], v[164:167]\n"
    "ds_read_b128 a[72:75], v206 offset:0\n"
    "v_cvt_pk_bf16_f32 v160, v144, v145\n"
    "v_cvt_pk_bf16_f32 v161, v146, v147\n"
    "v_mfma_f32_16x16x32_bf16 v[132:135], v[116:119], a[48:51], v[164:167]\n"
    "ds_read_b128 a[76:79], v207 offset:0\n"
    "v_cvt_pk_bf16_f32 v162, v148, v149\n"
    "v_cvt_pk_bf16_f32 v163, v150, v151\n"
    "ds_read_b128 a[80:83], v206 offset:2048\n"
    "v_accvgpr_write_b32 a44, v160\n"
    "v_accvgpr_write_b32 a45, v161\n"
    "v_mfma_f32_16x16x32_bf16 v[156:159], a[40:43], a[40:43], v[156:159]\n"
    "v_accvgpr_write_b32 a46, v162\n"
    "v_accvgpr_write_b32 a47, v163\n"
    "s_add_u32 m0, s83, 84992\n"
    "s_add_u32 s4, s82, 55936\n"
    "buffer_load_dwordx4 v205, s[56:59], s4 offen lds\n"
    "v_mfma_f32_16x16x32_bf16 v[136:139], v[118:121], a[52:55], v[164:167]\n"
    "ds_read_b128 a[84:87], v207 offset:2048\n"
    "v_cvt_pk_bf16_f32 v160, v128, v129\n"
    "v_cvt_pk_bf16_f32 v161, v130, v131\n"
    "v_mfma_f32_16x16x32_bf16 v[140:143], v[116:119], a[52:55], v[164:167]\n"
    "ds_read_b128 a[88:91], v206 offset:4096\n"
    "v_cvt_pk_bf16_f32 v162, v132, v133\n"
    "v_cvt_pk_bf16_f32 v163, v134, v135\n"
    "ds_read_b128 a[92:95], v207 offset:4096\n"
    "v_accvgpr_write_b32 a48, v160\n"
    "v_accvgpr_write_b32 a49, v161\n"
    "v_mfma_f32_16x16x32_bf16 v[156:159], a[44:47], a[44:47], v[156:159]\n"
    "v_accvgpr_write_b32 a50, v162\n"
    "v_accvgpr_write_b32 a51, v163\n"
    "s_add_u32 m0, s83, 86016\n"
    "s_add_u32 s4, s82, 56064\n"
    "buffer_load_dwordx4 v204, s[56:59], s4 offen lds\n"
    "v_mfma_f32_16x16x32_bf16 v[144:147], v[118:121], a[56:59], v[164:167]\n"
    "ds_read_b128 a[96:99], v206 offset:6144\n"
    "v_cvt_pk_bf16_f32 v160, v136, v137\n"
    "v_cvt_pk_bf16_f32 v161, v138, v139\n"
    "v_mfma_f32_16x16x32_bf16 v[148:151], v[116:119], a[56:59], v[164:167]\n"
    "ds_read_b128 a[100:103], v207 offset:6144\n"
    "v_cvt_pk_bf16_f32 v162, v140, v141\n"
    "v_cvt_pk_bf16_f32 v163, v142, v143\n"
    "ds_read_b128 a[104:107], v206 offset:8192\n"
    "v_accvgpr_write_b32 a52, v160\n"
    "v_accvgpr_write_b32 a53, v161\n"
    "v_mfma_f32_16x16x32_bf16 v[156:159], a[48:51], a[48:51], v[156:159]\n"
    "v_accvgpr_write_b32 a54, v162\n"
    "v_accvgpr_write_b32 a55, v163\n"
    "s_add_u32 m0, s83, 87040\n"
    "s_add_u32 s4, s82, 56064\n"
    "buffer_load_dwordx4 v205, s[56:59], s4 offen lds\n"
    "v_mfma_f32_16x16x32_bf16 v[128:131], v[118:121], a[60:63], v[164:167]\n"
    "ds_read_b128 a[216:219], v207 offset:8192\n"
    "v_cvt_pk_bf16_f32 v160, v144, v145\n"
    "v_cvt_pk_bf16_f32 v161, v146, v147\n"
    "v_mfma_f32_16x16x32_bf16 v[132:135], v[116:119], a[60:63], v[164:167]\n"
    "ds_read_b128 a[220:223], v206 offset:10240\n"
    "v_cvt_pk_bf16_f32 v162, v148, v149\n"
    "v_cvt_pk_bf16_f32 v163, v150, v151\n"
    "ds_read_b128 a[224:227], v207 offset:10240\n"
    "v_accvgpr_write_b32 a56, v160\n"
    "v_accvgpr_write_b32 a57, v161\n"
    "v_mfma_f32_16x16x32_bf16 v[156:159], a[52:55], a[52:55], v[156:159]\n"
    "v_accvgpr_write_b32 a58, v162\n"
    "v_accvgpr_write_b32 a59, v163\n"
    "s_add_u32 m0, s83, 88064\n"
    "s_add_u32 s4, s82, 56192\n"
    "buffer_load_dwordx4 v204, s[56:59], s4 offen lds\n"
    "v_mfma_f32_16x16x32_bf16 v[136:139], v[118:121], a[64:67], v[164:167]\n"
    "ds_read_b128 a[228:231], v206 offset:12288\n"
    "v_cvt_pk_bf16_f32 v160, v128, v129\n"
    "v_cvt_pk_bf16_f32 v161, v130, v131\n"
    "v_mfma_f32_16x16x32_bf16 v[140:143], v[116:119], a[64:67], v[164:167]\n"
    "ds_read_b128 a[232:235], v207 offset:12288\n"
    "v_cvt_pk_bf16_f32 v162, v132, v133\n"
    "v_cvt_pk_bf16_f32 v163, v134, v135\n"
    "ds_read_b128 a[236:239], v206 offset:14336\n"
    "v_accvgpr_write_b32 a60, v160\n"
    "v_accvgpr_write_b32 a61, v161\n"
    "v_mfma_f32_16x16x32_bf16 v[156:159], a[56:59], a[56:59], v[156:159]\n"
    "v_accvgpr_write_b32 a62, v162\n"
    "v_accvgpr_write_b32 a63, v163\n"
    "s_add_u32 m0, s83, 89088\n"
    "s_add_u32 s4, s82, 56192\n"
    "buffer_load_dwordx4 v205, s[56:59], s4 offen lds\n"
    "v_mfma_f32_16x16x32_bf16 v[144:147], v[118:121], a[68:71], v[164:167]\n"
    "ds_read_b128 a[240:243], v207 offset:14336\n"
    "v_cvt_pk_bf16_f32 v160, v136, v137\n"
    "v_cvt_pk_bf16_f32 v161, v138, v139\n"
    "v_mfma_f32_16x16x32_bf16 v[148:151], v[116:119], a[68:71], v[164:167]\n"
    "ds_read_b128 a[244:247], v206 offset:16384\n"
    "v_cvt_pk_bf16_f32 v162, v140, v141\n"
    "v_cvt_pk_bf16_f32 v163, v142, v143\n"
    "ds_read_b128 a[248:251], v207 offset:16384\n"
    "v_accvgpr_write_b32 a64, v160\n"
    "v_accvgpr_write_b32 a65, v161\n"
    "v_mfma_f32_16x16x32_bf16 v[156:159], a[60:63], a[60:63], v[156:159]\n"
    "v_accvgpr_write_b32 a66, v162\n"
    "v_accvgpr_write_b32 a67, v163\n"
    "s_add_u32 m0, s83, 90112\n"
    "s_add_u32 s4, s82, 56320\n"
    "buffer_load_dwordx4 v204, s[56:59], s4 offen lds\n"
    "v_mfma_f32_16x16x32_bf16 v[128:131], v[118:121], a[180:183], v[164:167]\n"
    "v_cvt_pk_bf16_f32 v160, v144, v145\n"
    "v_cvt_pk_bf16_f32 v161, v146, v147\n"
    "v_mfma_f32_16x16x32_bf16 v[132:135], v[116:119], a[180:183], v[164:167]\n"
    "v_cvt_pk_bf16_f32 v162, v148, v149\n"
    "v_cvt_pk_bf16_f32 v163, v150, v151\n"
    "v_accvgpr_write_b32 a68, v160\n"
    "v_accvgpr_write_b32 a69, v161\n"
    "v_mfma_f32_16x16x32_bf16 v[156:159], a[64:67], a[64:67], v[156:159]\n"
    "v_accvgpr_write_b32 a70, v162\n"
    "v_accvgpr_write_b32 a71, v163\n"
    "s_add_u32 m0, s83, 91136\n"
    "s_add_u32 s4, s82, 56320\n"
    "buffer_load_dwordx4 v205, s[56:59], s4 offen lds\n"
    "s_waitcnt lgkmcnt(0)\n"
    "s_barrier\n"
    "v_mfma_f32_16x16x32_bf16 v[136:139], v[118:121], a[184:187], v[164:167]\n"
    "v_cvt_pk_bf16_f32 v160, v128, v129\n"
    "v_cvt_pk_bf16_f32 v161, v130, v131\n"
    "v_mfma_f32_16x16x32_bf16 v[140:143], v[116:119], a[184:187], v[164:167]\n"
    "v_cvt_pk_bf16_f32 v162, v132, v133\n"
    "v_cvt_pk_bf16_f32 v163, v134, v135\n"
    "v_accvgpr_write_b32 a180, v160\n"
    "v_accvgpr_write_b32 a181, v161\n"
    "v_mfma_f32_16x16x32_bf16 v[156:159], a[68:71], a[68:71], v[156:159]\n"
    "v_accvgpr_write_b32 a182, v162\n"
    "v_accvgpr_write_b32 a183, v163\n"
    "s_mov_b32 s67, s65\n"
    "s_add_u32 m0, s64, 0\n"
    "s_nop 0\n"
    "buffer_load_dwordx4 v250, s[40:43], s67 offen lds\n"
    "s_add_u32 s67, s67, s68\n"
    "v_mfma_f32_16x16x32_bf16 v[144:147], v[118:121], a[188:191], v[164:167]\n"
    "v_cvt_pk_bf16_f32 v160, v136, v137\n"
    "v_cvt_pk_bf16_f32 v161, v138, v139\n"
    "v_mfma_f32_16x16x32_bf16 v[148:151], v[116:119], a[188:191], v[164:167]\n"
    "v_cvt_pk_bf16_f32 v162, v140, v141\n"
    "v_cvt_pk_bf16_f32 v163, v142, v143\n"
    "v_mfma_f32_16x16x32_bf16 v[152:155], v[112:115], a[72:75], 0\n"
    "v_accvgpr_write_b32 a184, v160\n"
    "v_accvgpr_write_b32 a185, v161\n"
    "v_mfma_f32_16x16x32_bf16 v[156:159], a[180:183], a[180:183], v[156:159]\n"
    "v_accvgpr_write_b32 a186, v162\n"
    "v_accvgpr_write_b32 a187, v163\n"
    "v_mfma_f32_16x16x32_bf16 v[152:155], v[112:115], a[76:79], v[152:155]\n"
    "v_mfma_f32_16x16x32_bf16 v[152:155], v[112:115], a[80:83], v[152:155]\n"
    "s_add_u32 m0, s64, 4096\n"
    "s_nop 0\n"
    "buffer_load_dwordx4 v250, s[40:43], s67 offen lds\n"
    "s_add_u32 s67, s67, s68\n"
    "v_mfma_f32_16x16x32_bf16 v[128:131], v[118:121], a[192:195], v[164:167]\n"
    "v_cvt_pk_bf16_f32 v160, v144, v145\n"
    "v_cvt_pk_bf16_f32 v161, v146, v147\n"
    "v_mfma_f32_16x16x32_bf16 v[132:135], v[116:119], a[192:195], v[164:167]\n"
    "v_cvt_pk_bf16_f32 v162, v148, v149\n"
    "v_cvt_pk_bf16_f32 v163, v150, v151\n"
    "v_mfma_f32_16x16x32_bf16 v[152:155], v[112:115], a[84:87], v[152:155]\n"
    "v_accvgpr_write_b32 a188, v160\n"
    "v_accvgpr_write_b32 a189, v161\n"
    "v_mfma_f32_16x16x32_bf16 v[156:159], a[184:187], a[184:187], v[156:159]\n"
    "v_accvgpr_write_b32 a190, v162\n"
    "v_accvgpr_write_b32 a191, v163\n"
    "v_mfma_f32_16x16x32_bf16 v[152:155], v[112:115], a[88:91], v[152:155]\n"
    "v_mfma_f32_16x16x32_bf16 v[152:155], v[112:115], a[92:95], v[152:155]\n"
    "s_add_u32 m0, s64, 8192\n"
    "s_nop 0\n"
    "buffer_load_dwordx4 v250, s[40:43], s67 offen lds\n"
    "s_add_u32 s67, s67, s68\n"
    "v_mfma_f32_16x16x32_bf16 v[136:139], v[118:121], a[196:199], v[164:167]\n"
    "v_cvt_pk_bf16_f32 v160, v128, v129\n"
    "v_cvt_pk_bf16_f32 v161, v130, v131\n"
    "v_mfma_f32_16x16x32_bf16 v[140:143], v[116:119], a[196:199], v[164:167]\n"
    "v_cvt_pk_bf16_f32 v162, v132, v133\n"
    "v_cvt_pk_bf16_f32 v163, v134, v135\n"
    "v_mfma_f32_16x16x32_bf16 v[152:155], v[112:115], a[96:99], v[152:155]\n"
    "v_accvgpr_write_b32 a192, v160\n"
    "v_accvgpr_write_b32 a193, v161\n"
    "v_mfma_f32_16x16x32_bf16 v[156:159], a[188:191], a[188:191], v[156:159]\n"
    "v_accvgpr_write_b32 a194, v162\n"
    "v_accvgpr_write_b32 a195, v163\n"
    "v_mfma_f32_16x16x32_bf16 v[152:155], v[112:115], a[100:103], v[152:155]\n"
    "v_mfma_f32_16x16x32_bf16 v[152:155], v[112:115], a[104:107], v[152:155]\n"
    "v_mfma_f32_16x16x32_bf16 v[144:147], v[118:121], a[200:203], v[164:167]\n"
    "v_cvt_pk_bf16_f32 v160, v136, v137\n"
    "v_cvt_pk_bf16_f32 v161, v138, v139\n"
    "v_mfma_f32_16x16x32_bf16 v[148:151], v[116:119], a[200:203], v[164:167]\n"
    "v_cvt_pk_bf16_f32 v162, v140, v141\n"
    "v_cvt_pk_bf16_f32 v163, v142, v143\n"
    "v_mfma_f32_16x16x32_bf16 v[152:155], v[112:115], a[216:219], v[152:155]\n"
    "v_accvgpr_write_b32 a196, v160\n"
    "v_accvgpr_write_b32 a197, v161\n"
    "v_mfma_f32_16x16x32_bf16 v[156:159], a[192:195], a[192:195], v[156:159]\n"
    "v_accvgpr_write_b32 a198, v162\n"
    "v_accvgpr_write_b32 a199, v163\n"
    "v_mfma_f32_16x16x32_bf16 v[152:155], v[112:115], a[220:223], v[152:155]\n"
    "v_mfma_f32_16x16x32_bf16 v[152:155], v[112:115], a[224:227], v[152:155]\n"
    "s_add_u32 m0, s64, 12288\n"
    "s_nop 0\n"
    "buffer_load_dwordx4 v250, s[40:43], s67 offen lds\n"
    "s_add_u32 s67, s67, s68\n"
    "v_mfma_f32_16x16x32_bf16 v[128:131], v[118:121], a[204:207], v[164:167]\n"
    "v_cvt_pk_bf16_f32 v160, v144, v145\n"
    "v_cvt_pk_bf16_f32 v161, v146, v147\n"
    "v_mfma_f32_16x16x32_bf16 v[132:135], v[116:119], a[204:207], v[164:167]\n"
    "v_cvt_pk_bf16_f32 v162, v148, v149\n"
    "v_cvt_pk_bf16_f32 v163, v150, v151\n"
    "v_mfma_f32_16x16x32_bf16 v[152:155], v[112:115], a[228:231], v[152:155]\n"
    "v_accvgpr_write_b32 a200, v160\n"
    "v_accvgpr_write_b32 a201, v161\n"
    "v_mfma_f32_16x16x32_bf16 v[156:159], a[196:199], a[196:199], v[156:159]\n"
    "v_accvgpr_write_b32 a202, v162\n"
    "v_accvgpr_write_b32 a203, v163\n"
    "v_mfma_f32_16x16x32_bf16 v[152:155], v[112:115], a[232:235], v[152:155]\n"
    "s_add_u32 m0, s64, 16384\n"
    "s_nop 0\n"
    "buffer_load_dwordx4 v250, s[40:43], s67 offen lds\n"
    "s_add_u32 s67, s67, s68\n"
    "v_mfma_f32_16x16x32_bf16 v[136:139], v[118:121], a[208:211], v[164:167]\n"
    "v_cvt_pk_bf16_f32 v160, v128, v129\n"
    "v_cvt_pk_bf16_f32 v161, v130, v131\n"
    "v_mfma_f32_16x16x32_bf16 v[140:143], v[116:119], a[208:211], v[164:167]\n"
    "v_cvt_pk_bf16_f32 v162, v132, v133\n"
    "v_cvt_pk_bf16_f32 v163, v134, v135\n"
    "v_mfma_f32_16x16x32_bf16 v[152:155], v[112:115], a[236:239], v[152:155]\n"
    "v_accvgpr_write_b32 a204, v160\n"
    "v_accvgpr_write_b32 a205, v161\n"
    "v_mfma_f32_16x16x32_bf16 v[156:159], a[200:203], a[200:203], v[156:159]\n"
    "v_accvgpr_write_b32 a206, v162\n"
    "v_accvgpr_write_b32 a207, v163\n"
    "v_mfma_f32_16x16x32_bf16 v[152:155], v[112:115], a[240:243], v[152:155]\n"
    "s_add_u32 m0, s64, 20480\n"
    "s_nop 0\n"
    "buffer_load_dwordx4 v250, s[40:43], s67 offen lds\n"
    "s_add_u32 s67, s67, s68\n"
    "v_mfma_f32_16x16x32_bf16 v[144:147], v[118:121], a[212:215], v[164:167]\n"
    "v_cvt_pk_bf16_f32 v160, v136, v137\n"
    "v_cvt_pk_bf16_f32 v161, v138, v139\n"
    "v_mfma_f32_16x16x32_bf16 v[148:151], v[116:119], a[212:215], v[164:167]\n"
    "v_cvt_pk_bf16_f32 v162, v140, v141\n"
    "v_cvt_pk_bf16_f32 v163, v142, v143\n"
    "v_mfma_f32_16x16x32_bf16 v[152:155], v[112:115], a[244:247], v[152:155]\n"
    "v_accvgpr_write_b32 a208, v160\n"
    "v_accvgpr_write_b32 a209, v161\n"
    "v_mfma_f32_16x16x32_bf16 v[156:159], a[204:207], a[204:207], v[156:159]\n"
    "v_accvgpr_write_b32 a210, v162\n"
    "v_accvgpr_write_b32 a211, v163\n"
    "v_mfma_f32_16x16x32_bf16 v[152:155], v[112:115], a[248:251], v[152:155]\n"
    "s_nop 2\n"
    "v_mfma_f32_16x16x32_bf16 v[156:159], a[208:211], a[208:211], v[156:159]\n"
    "s_nop 2\n"
    "v_cvt_pk_bf16_f32 v160, v144, v145\n"
    "v_cvt_pk_bf16_f32 v161, v146, v147\n"
    "v_cvt_pk_bf16_f32 v162, v148, v149\n"
    "v_cvt_pk_bf16_f32 v163, v150, v151\n"
    "v_accvgpr_write_b32 a212, v160\n"
    "v_accvgpr_write_b32 a213, v161\n"
    "v_accvgpr_write_b32 a214, v162\n"
    "v_accvgpr_write_b32 a215, v163\n"
    "s_nop 1\n"
    "s_nop 1\n"
    "v_mfma_f32_16x16x32_bf16 v[156:159], a[212:215], a[212:215], v[156:159]\n"
    "s_nop 7\n"
    "v_mul_f32 v124, 0xbae38e39, v152\n"
    "v_mul_f32 v125, 0xbae38e39, v153\n"
    "v_mul_f32 v126, 0xbae38e39, v154\n"
    "v_mul_f32 v127, 0xbae38e39, v155\n"
    "v_cndmask_b32 v168, v156, v157, s[14:15]\n"
    "v_cndmask_b32 v169, v158, v159, s[14:15]\n"
    "v_cndmask_b32 v168, v168, v169, s[16:17]\n"
    "v_cndmask_b32 v168, 0, v168, s[18:19]\n"
    "v_mov_b32 v169, v168\n"
    "s_nop 1\n"
    "v_permlane16_swap_b32 v168, v169\n"
    "s_nop 1\n"
    "v_add_f32 v168, v168, v169\n"
    "v_mov_b32 v169, v168\n"
    "s_nop 1\n"
    "v_permlane32_swap_b32 v168, v169\n"
    "s_nop 1\n"
    "v_add_f32 v168, v168, v169\n"
    "v_mov_b32 v169, 0x3727c5ac\n"
    "v_fma_f32 v241, v168, v170, v169\n"
    "v_rsq_f32 v168, v241\n"
    "s_nop 0\n"
    "v_mul_f32 v237, 0xbfb8aa3b, v168\n"
    "v_mul_f32 v255, 0x3e0293ee, v168\n"
    "v_mfma_f32_16x16x32_bf16 v[128:131], v[118:121], a[72:75], v[124:127]\n"
    "v_mfma_f32_16x16x32_bf16 v[132:135], v[116:119], a[72:75], v[124:127]\n"
    "s_add_u32 m0, s64, 24576\n"
    "s_nop 0\n"
    "buffer_load_dwordx4 v250, s[40:43], s67 offen lds\n"
    "s_add_u32 s67, s67, s68\n"
    "s_add_u32 m0, s64, 28672\n"
    "s_nop 0\n"
    "buffer_load_dwordx4 v250, s[40:43], s67 offen lds\n"
    "s_add_u32 s67, s67, s68\n"
    "v_mfma_f32_16x16x32_bf16 v[136:139], v[118:121], a[76:79], v[124:127]\n"
    "v_mfma_f32_16x16x32_bf16 v[140:143], v[116:119], a[76:79], v[124:127]\n"
    "s_nop 3\n"
    "v_cvt_pk_bf16_f32 v160, v128, v129\n"
    "v_cvt_pk_bf16_f32 v161, v130, v131\n"
    "v_cvt_pk_bf16_f32 v162, v132, v133\n"
    "v_cvt_pk_bf16_f32 v163, v134, v135\n"
    "v_accvgpr_write_b32 a72, v160\n"
    "v_accvgpr_write_b32 a73, v161\n"
    "v_accvgpr_write_b32 a74, v162\n"
    "v_accvgpr_write_b32 a75, v163\n"
    "s_add_u32 m0, s64, 32768\n"
    "s_nop 0\n"
    "buffer_load_dwordx4 v250, s[40:43], s67 offen lds\n"
    "s_add_u32 s67, s67, s68\n"
    "v_mfma_f32_16x16x32_bf16 v[144:147], v[118:121], a[80:83], v[124:127]\n"
    "v_cvt_pk_bf16_f32 v160, v136, v137\n"
    "v_cvt_pk_bf16_f32 v161, v138, v139\n"
    "v_mfma_f32_16x16x32_bf16 v[148:151], v[116:119], a[80:83], v[124:127]\n"
    "v_cvt_pk_bf16_f32 v162, v140, v141\n"
    "v_cvt_pk_bf16_f32 v163, v142, v143\n"
    "v_accvgpr_write_b32 a76, v160\n"
    "v_accvgpr_write_b32 a77, v161\n"
    "v_mfma_f32_16x16x32_bf16 v[156:159], a[72:75], a[72:75], 0\n"
    "v_accvgpr_write_b32 a78, v162\n"
    "v_accvgpr_write_b32 a79, v163\n"
    "s_mov_b32 s67, s66\n"
    "s_add_u32 m0, s64, 36864\n"
    "s_nop 0\n"
    "buffer_load_dwordx4 v250, s[44:47], s67 offen lds\n"
    "s_add_u32 s67, s67, s68\n"
    "s_waitcnt vmcnt(10)\n"
    "v_mfma_f32_16x16x32_bf16 v[128:131], v[118:121], a[84:87], v[124:127]\n"
    "ds_read_b128 a[108:111], v208 offset:0\n"
    "v_cvt_pk_bf16_f32 v160, v144, v145\n"
    "v_cvt_pk_bf16_f32 v161, v146, v147\n"
    "v_mfma_f32_16x16x32_bf16 v[132:135], v[116:119], a[84:87], v[124:127]\n"
    "ds_read_b128 a[112:115], v209 offset:0\n"
    "v_cvt_pk_bf16_f32 v162, v148, v149\n"
    "v_cvt_pk_bf16_f32 v163, v150, v151\n"
    "ds_read_b128 a[116:119], v208 offset:2048\n"
    "v_accvgpr_write_b32 a80, v160\n"
    "v_accvgpr_write_b32 a81, v161\n"
    "v_mfma_f32_16x16x32_bf16 v[156:159], a[76:79], a[76:79], v[156:159]\n"
    "v_accvgpr_write_b32 a82, v162\n"
    "v_accvgpr_write_b32 a83, v163\n"
    "s_add_u32 m0, s64, 40960\n"
    "s_nop 0\n"
    "buffer_load_dwordx4 v250, s[44:47], s67 offen lds\n"
    "s_add_u32 s67, s67, s68\n"
    "v_mfma_f32_16x16x32_bf16 v[136:139], v[118:121], a[88:91], v[124:127]\n"
    "ds_read_b128 a[120:123], v209 offset:2048\n"
    "v_cvt_pk_bf16_f32 v160, v128, v129\n"
    "v_cvt_pk_bf16_f32 v161, v130, v131\n"
    "v_mfma_f32_16x16x32_bf16 v[140:143], v[116:119], a[88:91], v[124:127]\n"
    "ds_read_b128 a[124:127], v208 offset:4096\n"
    "v_cvt_pk_bf16_f32 v162, v132, v133\n"
    "v_cvt_pk_bf16_f32 v163, v134, v135\n"
    "ds_read_b128 a[128:131], v209 offset:4096\n"
    "v_accvgpr_write_b32 a84, v160\n"
    "v_accvgpr_write_b32 a85, v161\n"
    "v_mfma_f32_16x16x32_bf16 v[156:159], a[80:83], a[80:83], v[156:159]\n"
    "v_accvgpr_write_b32 a86, v162\n"
    "v_accvgpr_write_b32 a87, v163\n"
    "s_add_u32 m0, s64, 45056\n"
    "s_nop 0\n"
    "buffer_load_dwordx4 v250, s[44:47], s67 offen lds\n"
    "s_add_u32 s67, s67, s68\n"
    "v_mfma_f32_16x16x32_bf16 v[144:147], v[118:121], a[92:95], v[124:127]\n"
    "ds_read_b128 a[132:135], v208 offset:6144\n"
    "v_cvt_pk_bf16_f32 v160, v136, v137\n"
    "v_cvt_pk_bf16_f32 v161, v138, v139\n"
    "v_mfma_f32_16x16x32_bf16 v[148:151], v[116:119], a[92:95], v[124:127]\n"
    "ds_read_b128 a[136:139], v209 offset:6144\n"
    "v_cvt_pk_bf16_f32 v162, v140, v141\n"
    "v_cvt_pk_bf16_f32 v163, v142, v143\n"
    "ds_read_b128 a[140:143], v208 offset:8192\n"
    "v_accvgpr_write_b32 a88, v160\n"
    "v_accvgpr_write_b32 a89, v161\n"
    "v_mfma_f32_16x16x32_bf16 v[156:159], a[84:87], a[84:87], v[156:159]\n"
    "v_accvgpr_write_b32 a90, v162\n"
    "v_accvgpr_write_b32 a91, v163\n"
    "s_add_u32 m0, s64, 49152\n"
    "s_nop 0\n"
    "buffer_load_dwordx4 v250, s[44:47], s67 offen lds\n"
    "s_add_u32 s67, s67, s68\n"
    "s_add_u32 m0, s64, 53248\n"
    "s_nop 0\n"
    "buffer_load_dwordx4 v250, s[44:47], s67 offen lds\n"
    "s_add_u32 s67, s67, s68\n"
    "v_mfma_f32_16x16x32_bf16 v[128:131], v[118:121], a[96:99], v[124:127]\n"
    "ds_read_b128 a[252:255], v209 offset:8192\n"
    "v_cvt_pk_bf16_f32 v160, v144, v145\n"
    "v_cvt_pk_bf16_f32 v161, v146, v147\n"
    "v_mfma_f32_16x16x32_bf16 v[132:135], v[116:119], a[96:99], v[124:127]\n"
    "ds_read_b128 v[8:11], v208 offset:10240\n"
    "v_cvt_pk_bf16_f32 v162, v148, v149\n"
    "v_cvt_pk_bf16_f32 v163, v150, v151\n"
    "ds_read_b128 v[12:15], v209 offset:10240\n"
    "v_accvgpr_write_b32 a92, v160\n"
    "v_accvgpr_write_b32 a93, v161\n"
    "v_mfma_f32_16x16x32_bf16 v[156:159], a[88:91], a[88:91], v[156:159]\n"
    "v_accvgpr_write_b32 a94, v162\n"
    "v_accvgpr_write_b32 a95, v163\n"
    "s_add_u32 m0, s64, 57344\n"
    "s_nop 0\n"
    "buffer_load_dwordx4 v250, s[44:47], s67 offen lds\n"
    "s_add_u32 s67, s67, s68\n"
    "v_mfma_f32_16x16x32_bf16 v[136:139], v[118:121], a[100:103], v[124:127]\n"
    "ds_read_b128 v[16:19], v208 offset:12288\n"
    "v_cvt_pk_bf16_f32 v160, v128, v129\n"
    "v_cvt_pk_bf16_f32 v161, v130, v131\n"
    "v_mfma_f32_16x16x32_bf16 v[140:143], v[116:119], a[100:103], v[124:127]\n"
    "ds_read_b128 v[20:23], v209 offset:12288\n"
    "v_cvt_pk_bf16_f32 v162, v132, v133\n"
    "v_cvt_pk_bf16_f32 v163, v134, v135\n"
    "ds_read_b128 v[24:27], v208 offset:14336\n"
    "v_accvgpr_write_b32 a96, v160\n"
    "v_accvgpr_write_b32 a97, v161\n"
    "v_mfma_f32_16x16x32_bf16 v[156:159], a[92:95], a[92:95], v[156:159]\n"
    "v_accvgpr_write_b32 a98, v162\n"
    "v_accvgpr_write_b32 a99, v163\n"
    "s_add_u32 m0, s64, 61440\n"
    "s_nop 0\n"
    "buffer_load_dwordx4 v250, s[44:47], s67 offen lds\n"
    "s_add_u32 s67, s67, s68\n"
    "v_mfma_f32_16x16x32_bf16 v[144:147], v[118:121], a[104:107], v[124:127]\n"
    "ds_read_b128 v[28:31], v209 offset:14336\n"
    "v_cvt_pk_bf16_f32 v160, v136, v137\n"
    "v_cvt_pk_bf16_f32 v161, v138, v139\n"
    "v_mfma_f32_16x16x32_bf16 v[148:151], v[116:119], a[104:107], v[124:127]\n"
    "ds_read_b128 v[32:35], v208 offset:16384\n"
    "v_cvt_pk_bf16_f32 v162, v140, v141\n"
    "v_cvt_pk_bf16_f32 v163, v142, v143\n"
    "ds_read_b128 v[36:39], v209 offset:16384\n"
    "v_accvgpr_write_b32 a100, v160\n"
    "v_accvgpr_write_b32 a101, v161\n"
    "v_mfma_f32_16x16x32_bf16 v[156:159], a[96:99], a[96:99], v[156:159]\n"
    "v_accvgpr_write_b32 a102, v162\n"
    "v_accvgpr_write_b32 a103, v163\n"
    "s_add_u32 m0, s64, 65536\n"
    "s_nop 0\n"
    "buffer_load_dwordx4 v250, s[44:47], s67 offen lds\n"
    "s_add_u32 s67, s67, s68\n"
    "v_mfma_f32_16x16x32_bf16 v[128:131], v[118:121], a[216:219], v[124:127]\n"
    "v_cvt_pk_bf16_f32 v160, v144, v145\n"
    "v_cvt_pk_bf16_f32 v161, v146, v147\n"
    "v_mfma_f32_16x16x32_bf16 v[132:135], v[116:119], a[216:219], v[124:127]\n"
    "v_cvt_pk_bf16_f32 v162, v148, v149\n"
    "v_cvt_pk_bf16_f32 v163, v150, v151\n"
    "v_accvgpr_write_b32 a104, v160\n"
    "v_accvgpr_write_b32 a105, v161\n"
    "v_mfma_f32_16x16x32_bf16 v[156:159], a[100:103], a[100:103], v[156:159]\n"
    "v_accvgpr_write_b32 a106, v162\n"
    "v_accvgpr_write_b32 a107, v163\n"
    "s_add_u32 m0, s64, 69632\n"
    "s_nop 0\n"
    "buffer_load_dwordx4 v250, s[44:47], s67 offen lds\n"
    "s_add_u32 s67, s67, s68\n"
    "s_waitcnt lgkmcnt(0)\n"
    "s_barrier\n"
    "v_mfma_f32_16x16x32_bf16 v[136:139], v[118:121], a[220:223], v[124:127]\n"
    "v_cvt_pk_bf16_f32 v160, v128, v129\n"
    "v_cvt_pk_bf16_f32 v161, v130, v131\n"
    "v_mfma_f32_16x16x32_bf16 v[140:143], v[116:119], a[220:223], v[124:127]\n"
    "v_cvt_pk_bf16_f32 v162, v132, v133\n"
    "v_cvt_pk_bf16_f32 v163, v134, v135\n"
    "v_accvgpr_write_b32 a216, v160\n"
    "v_accvgpr_write_b32 a217, v161\n"
    "v_mfma_f32_16x16x32_bf16 v[156:159], a[104:107], a[104:107], v[156:159]\n"
    "v_accvgpr_write_b32 a218, v162\n"
    "v_accvgpr_write_b32 a219, v163\n"
    "s_mov_b32 s65, s5\n"
    "s_mov_b32 s66, s5\n"
    "s_add_u32 s64, s91, 73728\n"
    "s_mov_b32 s67, s65\n"
    "s_add_u32 m0, s64, 0\n"
    "s_nop 0\n"
    "buffer_load_dwordx4 v250, s[40:43], s67 offen lds\n"
    "s_add_u32 s67, s67, s68\n"
    "v_mfma_f32_16x16x32_bf16 v[144:147], v[118:121], a[224:227], v[124:127]\n"
    "v_cvt_pk_bf16_f32 v160, v136, v137\n"
    "v_cvt_pk_bf16_f32 v161, v138, v139\n"
    "v_mfma_f32_16x16x32_bf16 v[148:151], v[116:119], a[224:227], v[124:127]\n"
    "v_cvt_pk_bf16_f32 v162, v140, v141\n"
    "v_cvt_pk_bf16_f32 v163, v142, v143\n"
    "v_mfma_f32_16x16x32_bf16 v[152:155], v[112:115], a[108:111], 0\n"
    "v_accvgpr_write_b32 a220, v160\n"
    "v_accvgpr_write_b32 a221, v161\n"
    "v_mfma_f32_16x16x32_bf16 v[156:159], a[216:219], a[216:219], v[156:159]\n"
    "v_accvgpr_write_b32 a222, v162\n"
    "v_accvgpr_write_b32 a223, v163\n"
    "v_mfma_f32_16x16x32_bf16 v[152:155], v[112:115], a[112:115], v[152:155]\n"
    "v_mfma_f32_16x16x32_bf16 v[152:155], v[112:115], a[116:119], v[152:155]\n"
    "v_mfma_f32_16x16x32_bf16 v[128:131], v[118:121], a[228:231], v[124:127]\n"
    "v_cvt_pk_bf16_f32 v160, v144, v145\n"
    "v_cvt_pk_bf16_f32 v161, v146, v147\n"
    "v_mfma_f32_16x16x32_bf16 v[132:135], v[116:119], a[228:231], v[124:127]\n"
    "v_cvt_pk_bf16_f32 v162, v148, v149\n"
    "v_cvt_pk_bf16_f32 v163, v150, v151\n"
    "v_mfma_f32_16x16x32_bf16 v[152:155], v[112:115], a[120:123], v[152:155]\n"
    "v_accvgpr_write_b32 a224, v160\n"
    "v_accvgpr_write_b32 a225, v161\n"
    "v_mfma_f32_16x16x32_bf16 v[156:159], a[220:223], a[220:223], v[156:159]\n"
    "v_accvgpr_write_b32 a226, v162\n"
    "v_accvgpr_write_b32 a227, v163\n"
    "v_mfma_f32_16x16x32_bf16 v[152:155], v[112:115], a[124:127], v[152:155]\n"
    "v_mfma_f32_16x16x32_bf16 v[152:155], v[112:115], a[128:131], v[152:155]\n"
    "s_add_u32 m0, s64, 4096\n"
    "s_nop 0\n"
    "buffer_load_dwordx4 v250, s[40:43], s67 offen lds\n"
    "s_add_u32 s67, s67, s68\n"
    "v_mfma_f32_16x16x32_bf16 v[136:139], v[118:121], a[232:235], v[124:127]\n"
    "v_cvt_pk_bf16_f32 v160, v128, v129\n"
    "v_cvt_pk_bf16_f32 v161, v130, v131\n"
    "v_mfma_f32_16x16x32_bf16 v[140:143], v[116:119], a[232:235], v[124:127]\n"
    "v_cvt_pk_bf16_f32 v162, v132, v133\n"
    "v_cvt_pk_bf16_f32 v163, v134, v135\n"
    "v_mfma_f32_16x16x32_bf16 v[152:155], v[112:115], a[132:135], v[152:155]\n"
    "v_accvgpr_write_b32 a228, v160\n"
    "v_accvgpr_write_b32 a229, v161\n"
    "v_mfma_f32_16x16x32_bf16 v[156:159], a[224:227], a[224:227], v[156:159]\n"
    "v_accvgpr_write_b32 a230, v162\n"
    "v_accvgpr_write_b32 a231, v163\n"
    "v_mfma_f32_16x16x32_bf16 v[152:155], v[112:115], a[136:139], v[152:155]\n"
    "v_mfma_f32_16x16x32_bf16 v[152:155], v[112:115], a[140:143], v[152:155]\n"
    "v_mfma_f32_16x16x32_bf16 v[144:147], v[118:121], a[236:239], v[124:127]\n"
    "v_cvt_pk_bf16_f32 v160, v136, v137\n"
    "v_cvt_pk_bf16_f32 v161, v138, v139\n"
    "v_mfma_f32_16x16x32_bf16 v[148:151], v[116:119], a[236:239], v[124:127]\n"
    "v_cvt_pk_bf16_f32 v162, v140, v141\n"
    "v_cvt_pk_bf16_f32 v163, v142, v143\n"
    "v_mfma_f32_16x16x32_bf16 v[152:155], v[112:115], a[252:255], v[152:155]\n"
    "v_accvgpr_write_b32 a232, v160\n"
    "v_accvgpr_write_b32 a233, v161\n"
    "v_mfma_f32_16x16x32_bf16 v[156:159], a[228:231], a[228:231], v[156:159]\n"
    "v_accvgpr_write_b32 a234, v162\n"
    "v_accvgpr_write_b32 a235, v163\n"
    "v_mfma_f32_16x16x32_bf16 v[152:155], v[112:115], v[8:11], v[152:155]\n"
    "v_mfma_f32_16x16x32_bf16 v[152:155], v[112:115], v[12:15], v[152:155]\n"
    "s_add_u32 m0, s64, 8192\n"
    "s_nop 0\n"
    "buffer_load_dwordx4 v250, s[40:43], s67 offen lds\n"
    "s_add_u32 s67, s67, s68\n"
    "v_mfma_f32_16x16x32_bf16 v[128:131], v[118:121], a[240:243], v[124:127]\n"
    "v_cvt_pk_bf16_f32 v160, v144, v145\n"
    "v_cvt_pk_bf16_f32 v161, v146, v147\n"
    "v_mfma_f32_16x16x32_bf16 v[132:135], v[116:119], a[240:243], v[124:127]\n"
    "v_cvt_pk_bf16_f32 v162, v148, v149\n"
    "v_cvt_pk_bf16_f32 v163, v150, v151\n"
    "v_mfma_f32_16x16x32_bf16 v[152:155], v[112:115], v[16:19], v[152:155]\n"
    "v_accvgpr_write_b32 a236, v160\n"
    "v_accvgpr_write_b32 a237, v161\n"
    "v_mfma_f32_16x16x32_bf16 v[156:159], a[232:235], a[232:235], v[156:159]\n"
    "v_accvgpr_write_b32 a238, v162\n"
    "v_accvgpr_write_b32 a239, v163\n"
    "v_mfma_f32_16x16x32_bf16 v[152:155], v[112:115], v[20:23], v[152:155]\n"
    "v_mfma_f32_16x16x32_bf16 v[136:139], v[118:121], a[244:247], v[124:127]\n"
    "v_cvt_pk_bf16_f32 v160, v128, v129\n"
    "v_cvt_pk_bf16_f32 v161, v130, v131\n"
    "v_mfma_f32_16x16x32_bf16 v[140:143], v[116:119], a[244:247], v[124:127]\n"
    "v_cvt_pk_bf16_f32 v162, v132, v133\n"
    "v_cvt_pk_bf16_f32 v163, v134, v135\n"
    "v_mfma_f32_16x16x32_bf16 v[152:155], v[112:115], v[24:27], v[152:155]\n"
    "v_accvgpr_write_b32 a240, v160\n"
    "v_accvgpr_write_b32 a241, v161\n"
    "v_mfma_f32_16x16x32_bf16 v[156:159], a[236:239], a[236:239], v[156:159]\n"
    "v_accvgpr_write_b32 a242, v162\n"
    "v_accvgpr_write_b32 a243, v163\n"
    "v_mfma_f32_16x16x32_bf16 v[152:155], v[112:115], v[28:31], v[152:155]\n"
    "s_add_u32 m0, s64, 12288\n"
    "s_nop 0\n"
    "buffer_load_dwordx4 v250, s[40:43], s67 offen lds\n"
    "s_add_u32 s67, s67, s68\n"
    "v_mfma_f32_16x16x32_bf16 v[144:147], v[118:121], a[248:251], v[124:127]\n"
    "v_cvt_pk_bf16_f32 v160, v136, v137\n"
    "v_cvt_pk_bf16_f32 v161, v138, v139\n"
    "v_mfma_f32_16x16x32_bf16 v[148:151], v[116:119], a[248:251], v[124:127]\n"
    "v_cvt_pk_bf16_f32 v162, v140, v141\n"
    "v_cvt_pk_bf16_f32 v163, v142, v143\n"
    "v_mfma_f32_16x16x32_bf16 v[152:155], v[112:115], v[32:35], v[152:155]\n"
    "v_accvgpr_write_b32 a244, v160\n"
    "v_accvgpr_write_b32 a245, v161\n"
    "v_mfma_f32_16x16x32_bf16 v[156:159], a[240:243], a[240:243], v[156:159]\n"
    "v_accvgpr_write_b32 a246, v162\n"
    "v_accvgpr_write_b32 a247, v163\n"
    "v_mfma_f32_16x16x32_bf16 v[152:155], v[112:115], v[36:39], v[152:155]\n"
    "s_nop 2\n"
    "v_mfma_f32_16x16x32_bf16 v[156:159], a[244:247], a[244:247], v[156:159]\n"
    "s_nop 2\n"
    "v_cvt_pk_bf16_f32 v160, v144, v145\n"
    "v_cvt_pk_bf16_f32 v161, v146, v147\n"
    "v_cvt_pk_bf16_f32 v162, v148, v149\n"
    "v_cvt_pk_bf16_f32 v163, v150, v151\n"
    "v_accvgpr_write_b32 a248, v160\n"
    "v_accvgpr_write_b32 a249, v161\n"
    "v_accvgpr_write_b32 a250, v162\n"
    "v_accvgpr_write_b32 a251, v163\n"
    "s_nop 1\n"
    "s_nop 1\n"
    "v_mfma_f32_16x16x32_bf16 v[156:159], a[248:251], a[248:251], v[156:159]\n"
    "s_nop 7\n"
    "v_mul_f32 v164, 0xbae38e39, v152\n"
    "v_mul_f32 v165, 0xbae38e39, v153\n"
    "v_mul_f32 v166, 0xbae38e39, v154\n"
    "v_mul_f32 v167, 0xbae38e39, v155\n"
    "v_cndmask_b32 v168, v156, v157, s[14:15]\n"
    "v_cndmask_b32 v169, v158, v159, s[14:15]\n"
    "v_cndmask_b32 v168, v168, v169, s[16:17]\n"
    "v_cndmask_b32 v168, 0, v168, s[18:19]\n"
    "v_mov_b32 v169, v168\n"
    "s_nop 1\n"
    "v_permlane16_swap_b32 v168, v169\n"
    "s_nop 1\n"
    "v_add_f32 v168, v168, v169\n"
    "v_mov_b32 v169, v168\n"
    "s_nop 1\n"
    "v_permlane32_swap_b32 v168, v169\n"
    "s_nop 1\n"
    "v_add_f32 v168, v168, v169\n"
    "v_mov_b32 v169, 0x3727c5ac\n"
    "v_fma_f32 v242, v168, v170, v169\n"
    "v_rsq_f32 v168, v242\n"
    "s_nop 0\n"
    "v_mul_f32 v238, 0xbfb8aa3b, v168\n"
    "v_mul_f32 v3, 0x3e0293ee, v168\n"
    "v_mfma_f32_16x16x32_bf16 v[128:131], v[118:121], a[108:111], v[164:167]\n"
    "v_mfma_f32_16x16x32_bf16 v[132:135], v[116:119], a[108:111], v[164:167]\n"
    "s_add_u32 m0, s64, 16384\n"
    "s_nop 0\n"
    "buffer_load_dwordx4 v250, s[40:43], s67 offen lds\n"
    "s_add_u32 s67, s67, s68\n"
    "v_mfma_f32_16x16x32_bf16 v[136:139], v[118:121], a[112:115], v[164:167]\n"
    "v_mfma_f32_16x16x32_bf16 v[140:143], v[116:119], a[112:115], v[164:167]\n"
    "s_nop 3\n"
    "v_cvt_pk_bf16_f32 v160, v128, v129\n"
    "v_cvt_pk_bf16_f32 v161, v130, v131\n"
    "v_cvt_pk_bf16_f32 v162, v132, v133\n"
    "v_cvt_pk_bf16_f32 v163, v134, v135\n"
    "v_accvgpr_write_b32 a108, v160\n"
    "v_accvgpr_write_b32 a109, v161\n"
    "v_accvgpr_write_b32 a110, v162\n"
    "v_accvgpr_write_b32 a111, v163\n"
    "s_add_u32 m0, s64, 20480\n"
    "s_nop 0\n"
    "buffer_load_dwordx4 v250, s[40:43], s67 offen lds\n"
    "s_add_u32 s67, s67, s68\n"
    "v_mfma_f32_16x16x32_bf16 v[144:147], v[118:121], a[116:119], v[164:167]\n"
    "v_cvt_pk_bf16_f32 v160, v136, v137\n"
    "v_cvt_pk_bf16_f32 v161, v138, v139\n"
    "v_mfma_f32_16x16x32_bf16 v[148:151], v[116:119], a[116:119], v[164:167]\n"
    "v_cvt_pk_bf16_f32 v162, v140, v141\n"
    "v_cvt_pk_bf16_f32 v163, v142, v143\n"
    "v_accvgpr_write_b32 a112, v160\n"
    "v_accvgpr_write_b32 a113, v161\n"
    "v_mfma_f32_16x16x32_bf16 v[156:159], a[108:111], a[108:111], 0\n"
    "v_accvgpr_write_b32 a114, v162\n"
    "v_accvgpr_write_b32 a115, v163\n"
    "s_add_u32 m0, s64, 24576\n"
    "s_nop 0\n"
    "buffer_load_dwordx4 v250, s[40:43], s67 offen lds\n"
    "s_add_u32 s67, s67, s68\n"
    "v_mfma_f32_16x16x32_bf16 v[128:131], v[118:121], a[120:123], v[164:167]\n"
    "v_cvt_pk_bf16_f32 v160, v144, v145\n"
    "v_cvt_pk_bf16_f32 v161, v146, v147\n"
    "v_mfma_f32_16x16x32_bf16 v[132:135], v[116:119], a[120:123], v[164:167]\n"
    "v_cvt_pk_bf16_f32 v162, v148, v149\n"
    "v_cvt_pk_bf16_f32 v163, v150, v151\n"
    "v_accvgpr_write_b32 a116, v160\n"
    "v_accvgpr_write_b32 a117, v161\n"
    "v_mfma_f32_16x16x32_bf16 v[156:159], a[112:115], a[112:115], v[156:159]\n"
    "v_accvgpr_write_b32 a118, v162\n"
    "v_accvgpr_write_b32 a119, v163\n"
    "s_add_u32 m0, s64, 28672\n"
    "s_nop 0\n"
    "buffer_load_dwordx4 v250, s[40:43], s67 offen lds\n"
    "s_add_u32 s67, s67, s68\n"
    "v_mfma_f32_16x16x32_bf16 v[136:139], v[118:121], a[124:127], v[164:167]\n"
    "v_cvt_pk_bf16_f32 v160, v128, v129\n"
    "v_cvt_pk_bf16_f32 v161, v130, v131\n"
    "v_mfma_f32_16x16x32_bf16 v[140:143], v[116:119], a[124:127], v[164:167]\n"
    "v_cvt_pk_bf16_f32 v162, v132, v133\n"
    "v_cvt_pk_bf16_f32 v163, v134, v135\n"
    "v_accvgpr_write_b32 a120, v160\n"
    "v_accvgpr_write_b32 a121, v161\n"
    "v_mfma_f32_16x16x32_bf16 v[156:159], a[116:119], a[116:119], v[156:159]\n"
    "v_accvgpr_write_b32 a122, v162\n"
    "v_accvgpr_write_b32 a123, v163\n"
    "v_mfma_f32_16x16x32_bf16 v[144:147], v[118:121], a[128:131], v[164:167]\n"
    "v_cvt_pk_bf16_f32 v160, v136, v137\n"
    "v_cvt_pk_bf16_f32 v161, v138, v139\n"
    "v_mfma_f32_16x16x32_bf16 v[148:151], v[116:119], a[128:131], v[164:167]\n"
    "v_cvt_pk_bf16_f32 v162, v140, v141\n"
    "v_cvt_pk_bf16_f32 v163, v142, v143\n"
    "v_accvgpr_write_b32 a124, v160\n"
    "v_accvgpr_write_b32 a125, v161\n"
    "v_mfma_f32_16x16x32_bf16 v[156:159], a[120:123], a[120:123], v[156:159]\n"
    "v_accvgpr_write_b32 a126, v162\n"
    "v_accvgpr_write_b32 a127, v163\n"
    "s_add_u32 m0, s64, 32768\n"
    "s_nop 0\n"
    "buffer_load_dwordx4 v250, s[40:43], s67 offen lds\n"
    "s_add_u32 s67, s67, s68\n"
    "v_mfma_f32_16x16x32_bf16 v[128:131], v[118:121], a[132:135], v[164:167]\n"
    "v_cvt_pk_bf16_f32 v160, v144, v145\n"
    "v_cvt_pk_bf16_f32 v161, v146, v147\n"
    "v_mfma_f32_16x16x32_bf16 v[132:135], v[116:119], a[132:135], v[164:167]\n"
    "v_cvt_pk_bf16_f32 v162, v148, v149\n"
    "v_cvt_pk_bf16_f32 v163, v150, v151\n"
    "v_accvgpr_write_b32 a128, v160\n"
    "v_accvgpr_write_b32 a129, v161\n"
    "v_mfma_f32_16x16x32_bf16 v[156:159], a[124:127], a[124:127], v[156:159]\n"
    "v_accvgpr_write_b32 a130, v162\n"
    "v_accvgpr_write_b32 a131, v163\n"
    "s_mov_b32 s67, s66\n"
    "s_add_u32 m0, s64, 36864\n"
    "s_nop 0\n"
    "buffer_load_dwordx4 v250, s[44:47], s67 offen lds\n"
    "s_add_u32 s67, s67, s68\n"
    "v_mfma_f32_16x16x32_bf16 v[136:139], v[118:121], a[136:139], v[164:167]\n"
    "v_cvt_pk_bf16_f32 v160, v128, v129\n"
    "v_cvt_pk_bf16_f32 v161, v130, v131\n"
    "v_mfma_f32_16x16x32_bf16 v[140:143], v[116:119], a[136:139], v[164:167]\n"
    "v_cvt_pk_bf16_f32 v162, v132, v133\n"
    "v_cvt_pk_bf16_f32 v163, v134, v135\n"
    "v_accvgpr_write_b32 a132, v160\n"
    "v_accvgpr_write_b32 a133, v161\n"
    "v_mfma_f32_16x16x32_bf16 v[156:159], a[128:131], a[128:131], v[156:159]\n"
    "v_accvgpr_write_b32 a134, v162\n"
    "v_accvgpr_write_b32 a135, v163\n"
    "s_add_u32 m0, s64, 40960\n"
    "s_nop 0\n"
    "buffer_load_dwordx4 v250, s[44:47], s67 offen lds\n"
    "s_add_u32 s67, s67, s68\n"
    "v_mfma_f32_16x16x32_bf16 v[144:147], v[118:121], a[140:143], v[164:167]\n"
    "v_cvt_pk_bf16_f32 v160, v136, v137\n"
    "v_cvt_pk_bf16_f32 v161, v138, v139\n"
    "v_mfma_f32_16x16x32_bf16 v[148:151], v[116:119], a[140:143], v[164:167]\n"
    "v_cvt_pk_bf16_f32 v162, v140, v141\n"
    "v_cvt_pk_bf16_f32 v163, v142, v143\n"
    "v_accvgpr_write_b32 a136, v160\n"
    "v_accvgpr_write_b32 a137, v161\n"
    "v_mfma_f32_16x16x32_bf16 v[156:159], a[132:135], a[132:135], v[156:159]\n"
    "v_accvgpr_write_b32 a138, v162\n"
    "v_accvgpr_write_b32 a139, v163\n"
    "s_add_u32 m0, s64, 45056\n"
    "s_nop 0\n"
    "buffer_load_dwordx4 v250, s[44:47], s67 offen lds\n"
    "s_add_u32 s67, s67, s68\n"
    "v_mfma_f32_16x16x32_bf16 v[128:131], v[118:121], a[252:255], v[164:167]\n"
    "v_cvt_pk_bf16_f32 v160, v144, v145\n"
    "v_cvt_pk_bf16_f32 v161, v146, v147\n"
    "v_mfma_f32_16x16x32_bf16 v[132:135], v[116:119], a[252:255], v[164:167]\n"
    "v_cvt_pk_bf16_f32 v162, v148, v149\n"
    "v_cvt_pk_bf16_f32 v163, v150, v151\n"
    "v_accvgpr_write_b32 a140, v160\n"
    "v_accvgpr_write_b32 a141, v161\n"
    "v_mfma_f32_16x16x32_bf16 v[156:159], a[136:139], a[136:139], v[156:159]\n"
    "v_accvgpr_write_b32 a142, v162\n"
    "v_accvgpr_write_b32 a143, v163\n"
    "v_mfma_f32_16x16x32_bf16 v[136:139], v[118:121], v[8:11], v[164:167]\n"
    "v_cvt_pk_bf16_f32 v160, v128, v129\n"
    "v_cvt_pk_bf16_f32 v161, v130, v131\n"
    "v_mfma_f32_16x16x32_bf16 v[140:143], v[116:119], v[8:11], v[164:167]\n"
    "v_cvt_pk_bf16_f32 v162, v132, v133\n"
    "v_cvt_pk_bf16_f32 v163, v134, v135\n"
    "v_accvgpr_write_b32 a252, v160\n"
    "v_accvgpr_write_b32 a253, v161\n"
    "v_mfma_f32_16x16x32_bf16 v[156:159], a[140:143], a[140:143], v[156:159]\n"
    "v_accvgpr_write_b32 a254, v162\n"
    "v_accvgpr_write_b32 a255, v163\n"
    "s_nop 1\n"
    "v_mfma_f32_16x16x32_bf16 v[144:147], v[118:121], v[12:15], v[164:167]\n"
    "v_cvt_pk_bf16_f32 v8, v136, v137\n"
    "v_cvt_pk_bf16_f32 v9, v138, v139\n"
    "v_mfma_f32_16x16x32_bf16 v[148:151], v[116:119], v[12:15], v[164:167]\n"
    "v_cvt_pk_bf16_f32 v10, v140, v141\n"
    "v_cvt_pk_bf16_f32 v11, v142, v143\n"
    "v_mfma_f32_16x16x32_bf16 v[156:159], a[252:255], a[252:255], v[156:159]\n"
    "s_nop 1\n"
    "v_mfma_f32_16x16x32_bf16 v[128:131], v[118:121], v[16:19], v[164:167]\n"
    "v_cvt_pk_bf16_f32 v12, v144, v145\n"
    "v_cvt_pk_bf16_f32 v13, v146, v147\n"
    "v_mfma_f32_16x16x32_bf16 v[132:135], v[116:119], v[16:19], v[164:167]\n"
    "v_cvt_pk_bf16_f32 v14, v148, v149\n"
    "v_cvt_pk_bf16_f32 v15, v150, v151\n"
    "v_mfma_f32_16x16x32_bf16 v[156:159], v[8:11], v[8:11], v[156:159]\n"
    "s_nop 1\n"
    "v_mfma_f32_16x16x32_bf16 v[136:139], v[118:121], v[20:23], v[164:167]\n"
    "v_cvt_pk_bf16_f32 v16, v128, v129\n"
    "v_cvt_pk_bf16_f32 v17, v130, v131\n"
    "v_mfma_f32_16x16x32_bf16 v[140:143], v[116:119], v[20:23], v[164:167]\n"
    "v_cvt_pk_bf16_f32 v18, v132, v133\n"
    "v_cvt_pk_bf16_f32 v19, v134, v135\n"
    "v_mfma_f32_16x16x32_bf16 v[156:159], v[12:15], v[12:15], v[156:159]\n"
    "s_nop 1\n"
    "v_mfma_f32_16x16x32_bf16 v[144:147], v[118:121], v[24:27], v[164:167]\n"
    "v_cvt_pk_bf16_f32 v20, v136, v137\n"
    "v_cvt_pk_bf16_f32 v21, v138, v139\n"
    "v_mfma_f32_16x16x32_bf16 v[148:151], v[116:119], v[24:27], v[164:167]\n"
    "v_cvt_pk_bf16_f32 v22, v140, v141\n"
    "v_cvt_pk_bf16_f32 v23, v142, v143\n"
    "v_mfma_f32_16x16x32_bf16 v[156:159], v[16:19], v[16:19], v[156:159]\n"
    "s_nop 1\n"
    "v_mfma_f32_16x16x32_bf16 v[128:131], v[118:121], v[28:31], v[164:167]\n"
    "v_cvt_pk_bf16_f32 v24, v144, v145\n"
    "v_cvt_pk_bf16_f32 v25, v146, v147\n"
    "v_mfma_f32_16x16x32_bf16 v[132:135], v[116:119], v[28:31], v[164:167]\n"
    "v_cvt_pk_bf16_f32 v26, v148, v149\n"
    "v_cvt_pk_bf16_f32 v27, v150, v151\n"
    "v_mfma_f32_16x16x32_bf16 v[156:159], v[20:23], v[20:23], v[156:159]\n"
    "s_nop 1\n"
    "v_mfma_f32_16x16x32_bf16 v[136:139], v[118:121], v[32:35], v[164:167]\n"
    "v_cvt_pk_bf16_f32 v28, v128, v129\n"
    "v_cvt_pk_bf16_f32 v29, v130, v131\n"
    "v_mfma_f32_16x16x32_bf16 v[140:143], v[116:119], v[32:35], v[164:167]\n"
    "v_cvt_pk_bf16_f32 v30, v132, v133\n"
    "v_cvt_pk_bf16_f32 v31, v134, v135\n"
    "v_mfma_f32_16x16x32_bf16 v[156:159], v[24:27], v[24:27], v[156:159]\n"
    "s_nop 1\n"
    "v_mfma_f32_16x16x32_bf16 v[144:147], v[118:121], v[36:39], v[164:167]\n"
    "v_cvt_pk_bf16_f32 v32, v136, v137\n"
    "v_cvt_pk_bf16_f32 v33, v138, v139\n"
    "v_mfma_f32_16x16x32_bf16 v[148:151], v[116:119], v[36:39], v[164:167]\n"
    "v_cvt_pk_bf16_f32 v34, v140, v141\n"
    "v_cvt_pk_bf16_f32 v35, v142, v143\n"
    "v_mfma_f32_16x16x32_bf16 v[156:159], v[28:31], v[28:31], v[156:159]\n"
    "s_nop 1\n"
    "s_nop 2\n"
    "v_mfma_f32_16x16x32_bf16 v[156:159], v[32:35], v[32:35], v[156:159]\n"
    "s_nop 2\n"
    "v_cvt_pk_bf16_f32 v36, v144, v145\n"
    "v_cvt_pk_bf16_f32 v37, v146, v147\n"
    "v_cvt_pk_bf16_f32 v38, v148, v149\n"
    "v_cvt_pk_bf16_f32 v39, v150, v151\n"
    "s_nop 1\n"
    "s_nop 1\n"
    "v_mfma_f32_16x16x32_bf16 v[156:159], v[36:39], v[36:39], v[156:159]\n"
    "s_nop 7\n"
    "v_cndmask_b32 v168, v156, v157, s[14:15]\n"
    "v_cndmask_b32 v169, v158, v159, s[14:15]\n"
    "v_cndmask_b32 v168, v168, v169, s[16:17]\n"
    "v_cndmask_b32 v168, 0, v168, s[18:19]\n"
    "v_mov_b32 v169, v168\n"
    "s_nop 1\n"
    "v_permlane16_swap_b32 v168, v169\n"
    "s_nop 1\n"
    "v_add_f32 v168, v168, v169\n"
    "v_mov_b32 v169, v168\n"
    "s_nop 1\n"
    "v_permlane32_swap_b32 v168, v169\n"
    "s_nop 1\n"
    "v_add_f32 v168, v168, v169\n"
    "v_mov_b32 v169, 0x3727c5ac\n"
    "v_fma_f32 v243, v168, v170, v169\n"
    "v_rsq_f32 v168, v243\n"
    "s_nop 0\n"
    "v_mul_f32 v239, 0xbfb8aa3b, v168\n"
    "v_mul_f32 v0, 0x3e0293ee, v168\n"
    "v_readfirstlane_b32 s3, v216\n"
    "s_nop 3\n"
    "s_add_u32 s3, s3, 1\n"
    "s_waitcnt vmcnt(12)\n"
    "s_barrier\n"
    "ds_read_b64_tr_b16 v[40:41], v200 offset:0\n"
    "ds_read_b64_tr_b16 v[42:43], v200 offset:1024\n"
    "ds_read_b64_tr_b16 v[44:45], v200 offset:2048\n"
    "ds_read_b64_tr_b16 v[46:47], v200 offset:3072\n"
    "ds_read_b64_tr_b16 v[48:49], v200 offset:4096\n"
    "ds_read_b64_tr_b16 v[50:51], v200 offset:5120\n"
    "ds_read_b64_tr_b16 v[52:53], v200 offset:6144\n"
    "ds_read_b64_tr_b16 v[54:55], v200 offset:7168\n"
    "ds_read_b64_tr_b16 v[56:57], v200 offset:8192\n"
    "ds_read_b64_tr_b16 v[58:59], v200 offset:9216\n"
    "ds_read_b64_tr_b16 v[60:61], v200 offset:10240\n"
    "ds_read_b64_tr_b16 v[62:63], v200 offset:11264\n"
    "ds_read_b64_tr_b16 v[64:65], v200 offset:12288\n"
    "ds_read_b64_tr_b16 v[66:67], v200 offset:13312\n"
    "ds_read_b64_tr_b16 v[68:69], v200 offset:14336\n"
    "ds_read_b64_tr_b16 v[70:71], v200 offset:15360\n"
    "ds_read_b64_tr_b16 v[72:73], v200 offset:16384\n"
    "ds_read_b64_tr_b16 v[74:75], v200 offset:17408\n"
    "s_waitcnt lgkmcnt(0)\n"
    "s_nop 1\n"
    "v_mfma_f32_16x16x32_bf16 v[144:147], v[40:43], a[0:3], 0\n"
    "ds_read_b64_tr_b16 v[76:77], v200 offset:18432\n"
    "v_mfma_f32_16x16x32_bf16 v[144:147], v[44:47], a[4:7], v[144:147]\n"
    "ds_read_b64_tr_b16 v[78:79], v200 offset:19456\n"
    "v_mfma_f32_16x16x32_bf16 v[144:147], v[48:51], a[8:11], v[144:147]\n"
    "s_add_u32 m0, s64, 49152\n"
    "s_nop 0\n"
    "buffer_load_dwordx4 v250, s[44:47], s67 offen lds\n"
    "s_add_u32 s67, s67, s68\n"
    "v_mfma_f32_16x16x32_bf16 v[144:147], v[52:55], a[12:15], v[144:147]\n"
    "ds_read_b64_tr_b16 v[80:81], v200 offset:20480\n"
    "v_mfma_f32_16x16x32_bf16 v[144:147], v[56:59], a[16:19], v[144:147]\n"
    "ds_read_b64_tr_b16 v[82:83], v200 offset:21504\n"
    "v_mfma_f32_16x16x32_bf16 v[144:147], v[60:63], a[20:23], v[144:147]\n"
    "v_mfma_f32_16x16x32_bf16 v[144:147], v[64:67], a[24:27], v[144:147]\n"
    "ds_read_b64_tr_b16 v[84:85], v200 offset:22528\n"
    "v_mfma_f32_16x16x32_bf16 v[144:147], v[68:71], a[28:31], v[144:147]\n"
    "ds_read_b64_tr_b16 v[86:87], v200 offset:23552\n"
    "v_mfma_f32_16x16x32_bf16 v[144:147], v[72:75], a[32:35], v[144:147]\n"
    "v_mfma_f32_16x16x32_bf16 v[148:151], v[40:43], a[36:39], 0\n"
    "ds_read_b64_tr_b16 v[88:89], v200 offset:24576\n"
    "v_mfma_f32_16x16x32_bf16 v[148:151], v[44:47], a[40:43], v[148:151]\n"
    "ds_read_b64_tr_b16 v[90:91], v200 offset:25600\n"
    "v_mfma_f32_16x16x32_bf16 v[148:151], v[48:51], a[44:47], v[148:151]\n"
    "v_mfma_f32_16x16x32_bf16 v[148:151], v[52:55], a[48:51], v[148:151]\n"
    "s_add_u32 m0, s64, 53248\n"
    "s_nop 0\n"
    "buffer_load_dwordx4 v250, s[44:47], s67 offen lds\n"
    "s_add_u32 s67, s67, s68\n"
    "ds_read_b64_tr_b16 v[92:93], v200 offset:26624\n"
    "v_mfma_f32_16x16x32_bf16 v[148:151], v[56:59], a[52:55], v[148:151]\n"
    "ds_read_b64_tr_b16 v[94:95], v200 offset:27648\n"
    "v_mfma_f32_16x16x32_bf16 v[148:151], v[60:63], a[56:59], v[148:151]\n"
    "v_mfma_f32_16x16x32_bf16 v[148:151], v[64:67], a[60:63], v[148:151]\n"
    "ds_read_b64_tr_b16 v[96:97], v200 offset:28672\n"
    "v_mfma_f32_16x16x32_bf16 v[148:151], v[68:71], a[64:67], v[148:151]\n"
    "ds_read_b64_tr_b16 v[98:99], v200 offset:29696\n"
    "v_mfma_f32_16x16x32_bf16 v[148:151], v[72:75], a[68:71], v[148:151]\n"
    "v_mfma_f32_16x16x32_bf16 v[152:155], v[40:43], a[72:75], 0\n"
    "ds_read_b64_tr_b16 v[100:101], v200 offset:30720\n"
    "v_mfma_f32_16x16x32_bf16 v[152:155], v[44:47], a[76:79], v[152:155]\n"
    "ds_read_b64_tr_b16 v[102:103], v200 offset:31744\n"
    "v_mfma_f32_16x16x32_bf16 v[152:155], v[48:51], a[80:83], v[152:155]\n"
    "v_mfma_f32_16x16x32_bf16 v[152:155], v[52:55], a[84:87], v[152:155]\n"
    "ds_read_b64_tr_b16 v[104:105], v200 offset:32768\n"
    "v_mfma_f32_16x16x32_bf16 v[152:155], v[56:59], a[88:91], v[152:155]\n"
    "s_add_u32 m0, s64, 57344\n"
    "s_nop 0\n"
    "buffer_load_dwordx4 v250, s[44:47], s67 offen lds\n"
    "s_add_u32 s67, s67, s68\n"
    "ds_read_b64_tr_b16 v[106:107], v200 offset:33792\n"
    "v_mfma_f32_16x16x32_bf16 v[152:155], v[60:63], a[92:95], v[152:155]\n"
    "v_mfma_f32_16x16x32_bf16 v[152:155], v[64:67], a[96:99], v[152:155]\n"
    "ds_read_b64_tr_b16 v[108:109], v200 offset:34816\n"
    "v_mfma_f32_16x16x32_bf16 v[152:155], v[68:71], a[100:103], v[152:155]\n"
    "ds_read_b64_tr_b16 v[110:111], v200 offset:35840\n"
    "v_mfma_f32_16x16x32_bf16 v[152:155], v[72:75], a[104:107], v[152:155]\n"
    "v_mfma_f32_16x16x32_bf16 v[156:159], v[40:43], a[108:111], 0\n"
    "v_mfma_f32_16x16x32_bf16 v[156:159], v[44:47], a[112:115], v[156:159]\n"
    "v_mfma_f32_16x16x32_bf16 v[156:159], v[48:51], a[116:119], v[156:159]\n"
    "v_mfma_f32_16x16x32_bf16 v[156:159], v[52:55], a[120:123], v[156:159]\n"
    "v_mfma_f32_16x16x32_bf16 v[156:159], v[56:59], a[124:127], v[156:159]\n"
    "v_mfma_f32_16x16x32_bf16 v[156:159], v[60:63], a[128:131], v[156:159]\n"
    "v_mfma_f32_16x16x32_bf16 v[156:159], v[64:67], a[132:135], v[156:159]\n"
    "v_mfma_f32_16x16x32_bf16 v[156:159], v[68:71], a[136:139], v[156:159]\n"
    "v_mfma_f32_16x16x32_bf16 v[156:159], v[72:75], a[140:143], v[156:159]\n"
    "s_waitcnt lgkmcnt(0)\n"
    "v_mfma_f32_16x16x32_bf16 v[144:147], v[76:79], a[144:147], v[144:147]\n"
    "ds_read_b64_tr_b16 v[40:41], v202 offset:0\n"
    "v_mfma_f32_16x16x32_bf16 v[144:147], v[80:83], a[148:151], v[144:147]\n"
    "ds_read_b64_tr_b16 v[42:43], v202 offset:1024\n"
    "v_mfma_f32_16x16x32_bf16 v[144:147], v[84:87], a[152:155], v[144:147]\n"
    "s_add_u32 m0, s64, 61440\n"
    "s_nop 0\n"
    "buffer_load_dwordx4 v250, s[44:47], s67 offen lds\n"
    "s_add_u32 s67, s67, s68\n"
    "v_mfma_f32_16x16x32_bf16 v[144:147], v[88:91], a[156:159], v[144:147]\n"
    "ds_read_b64_tr_b16 v[44:45], v202 offset:2048\n"
    "v_mfma_f32_16x16x32_bf16 v[144:147], v[92:95], a[160:163], v[144:147]\n"
    "ds_read_b64_tr_b16 v[46:47], v202 offset:3072\n"
    "v_mfma_f32_16x16x32_bf16 v[144:147], v[96:99], a[164:167], v[144:147]\n"
    "v_mfma_f32_16x16x32_bf16 v[144:147], v[100:103], a[168:171], v[144:147]\n"
    "ds_read_b64_tr_b16 v[48:49], v202 offset:4096\n"
    "v_mfma_f32_16x16x32_bf16 v[144:147], v[104:107], a[172:175], v[144:147]\n"
    "ds_read_b64_tr_b16 v[50:51], v202 offset:5120\n"
    "v_mfma_f32_16x16x32_bf16 v[144:147], v[108:111], a[176:179], v[144:147]\n"
    "v_mfma_f32_16x16x32_bf16 v[148:151], v[76:79], a[180:183], v[148:151]\n"
    "ds_read_b64_tr_b16 v[52:53], v202 offset:6144\n"
    "v_mfma_f32_16x16x32_bf16 v[148:151], v[80:83], a[184:187], v[148:151]\n"
    "ds_read_b64_tr_b16 v[54:55], v202 offset:7168\n"
    "v_mfma_f32_16x16x32_bf16 v[148:151], v[84:87], a[188:191], v[148:151]\n"
    "v_mfma_f32_16x16x32_bf16 v[148:151], v[88:91], a[192:195], v[148:151]\n"
    "s_add_u32 m0, s64, 65536\n"
    "s_nop 0\n"
    "buffer_load_dwordx4 v250, s[44:47], s67 offen lds\n"
    "s_add_u32 s67, s67, s68\n"
    "ds_read_b64_tr_b16 v[56:57], v202 offset:8192\n"
    "v_mfma_f32_16x16x32_bf16 v[148:151], v[92:95], a[196:199], v[148:151]\n"
    "ds_read_b64_tr_b16 v[58:59], v202 offset:9216\n"
    "v_mfma_f32_16x16x32_bf16 v[148:151], v[96:99], a[200:203], v[148:151]\n"
    "v_mfma_f32_16x16x32_bf16 v[148:151], v[100:103], a[204:207], v[148:151]\n"
    "ds_read_b64_tr_b16 v[60:61], v202 offset:10240\n"
    "v_mfma_f32_16x16x32_bf16 v[148:151], v[104:107], a[208:211], v[148:151]\n"
    "ds_read_b64_tr_b16 v[62:63], v202 offset:11264\n"
    "v_mfma_f32_16x16x32_bf16 v[148:151], v[108:111], a[212:215], v[148:151]\n"
    "v_mfma_f32_16x16x32_bf16 v[152:155], v[76:79], a[216:219], v[152:155]\n"
    "ds_read_b64_tr_b16 v[64:65], v202 offset:12288\n"
    "v_mfma_f32_16x16x32_bf16 v[152:155], v[80:83], a[220:223], v[152:155]\n"
    "ds_read_b64_tr_b16 v[66:67], v202 offset:13312\n"
    "v_mfma_f32_16x16x32_bf16 v[152:155], v[84:87], a[224:227], v[152:155]\n"
    "v_mfma_f32_16x16x32_bf16 v[152:155], v[88:91], a[228:231], v[152:155]\n"
    "ds_read_b64_tr_b16 v[68:69], v202 offset:14336\n"
    "v_mfma_f32_16x16x32_bf16 v[152:155], v[92:95], a[232:235], v[152:155]\n"
    "s_add_u32 m0, s64, 69632\n"
    "s_nop 0\n"
    "buffer_load_dwordx4 v250, s[44:47], s67 offen lds\n"
    "s_add_u32 s67, s67, s68\n"
    "ds_read_b64_tr_b16 v[70:71], v202 offset:15360\n"
    "v_mfma_f32_16x16x32_bf16 v[152:155], v[96:99], a[236:239], v[152:155]\n"
    "v_mfma_f32_16x16x32_bf16 v[152:155], v[100:103], a[240:243], v[152:155]\n"
    "ds_read_b64_tr_b16 v[72:73], v202 offset:16384\n"
    "v_mfma_f32_16x16x32_bf16 v[152:155], v[104:107], a[244:247], v[152:155]\n"
    "ds_read_b64_tr_b16 v[74:75], v202 offset:17408\n"
    "v_mfma_f32_16x16x32_bf16 v[152:155], v[108:111], a[248:251], v[152:155]\n"
    "v_mfma_f32_16x16x32_bf16 v[156:159], v[76:79], a[252:255], v[156:159]\n"
    "v_mfma_f32_16x16x32_bf16 v[156:159], v[80:83], v[8:11], v[156:159]\n"
    "v_mfma_f32_16x16x32_bf16 v[156:159], v[84:87], v[12:15], v[156:159]\n"
    "v_mfma_f32_16x16x32_bf16 v[156:159], v[88:91], v[16:19], v[156:159]\n"
    "v_mfma_f32_16x16x32_bf16 v[156:159], v[92:95], v[20:23], v[156:159]\n"
    "v_mfma_f32_16x16x32_bf16 v[156:159], v[96:99], v[24:27], v[156:159]\n"
    "v_mfma_f32_16x16x32_bf16 v[156:159], v[100:103], v[28:31], v[156:159]\n"
    "v_mfma_f32_16x16x32_bf16 v[156:159], v[104:107], v[32:35], v[156:159]\n"
    "v_mfma_f32_16x16x32_bf16 v[156:159], v[108:111], v[36:39], v[156:159]\n"
    "s_waitcnt lgkmcnt(0)\n"
    "s_add_u32 s65, s5, 64\n"
    "s_mov_b32 s66, s65\n"
    "s_add_u32 s64, s91, 0\n"
    "v_mfma_f32_16x16x32_bf16 v[160:163], v[40:43], a[0:3], 0\n"
    "ds_read_b64_tr_b16 v[76:77], v202 offset:18432\n"
    "v_mfma_f32_16x16x32_bf16 v[160:163], v[44:47], a[4:7], v[160:163]\n"
    "ds_read_b64_tr_b16 v[78:79], v202 offset:19456\n"
    "v_mfma_f32_16x16x32_bf16 v[160:163], v[48:51], a[8:11], v[160:163]\n"
    "s_add_u32 m0, s85, 163328\n"
    "s_sub_u32 s96, s65, s81\n"
    "buffer_load_dword v211, s[40:43], s96 offen lds\n"
    "v_mfma_f32_16x16x32_bf16 v[160:163], v[52:55], a[12:15], v[160:163]\n"
    "ds_read_b64_tr_b16 v[80:81], v202 offset:20480\n"
    "v_mfma_f32_16x16x32_bf16 v[160:163], v[56:59], a[16:19], v[160:163]\n"
    "ds_read_b64_tr_b16 v[82:83], v202 offset:21504\n"
    "v_mfma_f32_16x16x32_bf16 v[160:163], v[60:63], a[20:23], v[160:163]\n"
    "v_mfma_f32_16x16x32_bf16 v[160:163], v[64:67], a[24:27], v[160:163]\n"
    "ds_read_b64_tr_b16 v[84:85], v202 offset:22528\n"
    "v_mfma_f32_16x16x32_bf16 v[160:163], v[68:71], a[28:31], v[160:163]\n"
    "ds_read_b64_tr_b16 v[86:87], v202 offset:23552\n"
    "v_mfma_f32_16x16x32_bf16 v[160:163], v[72:75], a[32:35], v[160:163]\n"
    "v_mfma_f32_16x16x32_bf16 v[164:167], v[40:43], a[36:39], 0\n"
    "ds_read_b64_tr_b16 v[88:89], v202 offset:24576\n"
    "v_mfma_f32_16x16x32_bf16 v[164:167], v[44:47], a[40:43], v[164:167]\n"
    "ds_read_b64_tr_b16 v[90:91], v202 offset:25600\n"
    "v_mfma_f32_16x16x32_bf16 v[164:167], v[48:51], a[44:47], v[164:167]\n"
    "v_mfma_f32_16x16x32_bf16 v[164:167], v[52:55], a[48:51], v[164:167]\n"
    "ds_read_b64_tr_b16 v[92:93], v202 offset:26624\n"
    "v_mfma_f32_16x16x32_bf16 v[164:167], v[56:59], a[52:55], v[164:167]\n"
    "ds_read_b64_tr_b16 v[94:95], v202 offset:27648\n"
    "v_mfma_f32_16x16x32_bf16 v[164:167], v[60:63], a[56:59], v[164:167]\n"
    "v_mfma_f32_16x16x32_bf16 v[164:167], v[64:67], a[60:63], v[164:167]\n"
    "ds_read_b64_tr_b16 v[96:97], v202 offset:28672\n"
    "v_mfma_f32_16x16x32_bf16 v[164:167], v[68:71], a[64:67], v[164:167]\n"
    "ds_read_b64_tr_b16 v[98:99], v202 offset:29696\n"
    "v_mfma_f32_16x16x32_bf16 v[164:167], v[72:75], a[68:71], v[164:167]\n"
    "s_sub_u32 s96, s66, s81\n"
    "buffer_load_dword v211, s[44:47], s96 offen lds\n"
    "v_mfma_f32_16x16x32_bf16 v[168:171], v[40:43], a[72:75], 0\n"
    "ds_read_b64_tr_b16 v[100:101], v202 offset:30720\n"
    "v_mfma_f32_16x16x32_bf16 v[168:171], v[44:47], a[76:79], v[168:171]\n"
    "ds_read_b64_tr_b16 v[102:103], v202 offset:31744\n"
    "v_mfma_f32_16x16x32_bf16 v[168:171], v[48:51], a[80:83], v[168:171]\n"
    "v_mfma_f32_16x16x32_bf16 v[168:171], v[52:55], a[84:87], v[168:171]\n"
    "ds_read_b64_tr_b16 v[104:105], v202 offset:32768\n"
    "v_mfma_f32_16x16x32_bf16 v[168:171], v[56:59], a[88:91], v[168:171]\n"
    "ds_read_b64_tr_b16 v[106:107], v202 offset:33792\n"
    "v_mfma_f32_16x16x32_bf16 v[168:171], v[60:63], a[92:95], v[168:171]\n"
    "v_mfma_f32_16x16x32_bf16 v[168:171], v[64:67], a[96:99], v[168:171]\n"
    "ds_read_b64_tr_b16 v[108:109], v202 offset:34816\n"
    "v_mfma_f32_16x16x32_bf16 v[168:171], v[68:71], a[100:103], v[168:171]\n"
    "ds_read_b64_tr_b16 v[110:111], v202 offset:35840\n"
    "v_mfma_f32_16x16x32_bf16 v[168:171], v[72:75], a[104:107], v[168:171]\n"
    "v_mfma_f32_16x16x32_bf16 v[172:175], v[40:43], a[108:111], 0\n"
    "v_mfma_f32_16x16x32_bf16 v[172:175], v[44:47], a[112:115], v[172:175]\n"
    "v_mfma_f32_16x16x32_bf16 v[172:175], v[48:51], a[116:119], v[172:175]\n"
    "v_mfma_f32_16x16x32_bf16 v[172:175], v[52:55], a[120:123], v[172:175]\n"
    "v_mfma_f32_16x16x32_bf16 v[172:175], v[56:59], a[124:127], v[172:175]\n"
    "v_mfma_f32_16x16x32_bf16 v[172:175], v[60:63], a[128:131], v[172:175]\n"
    "v_mfma_f32_16x16x32_bf16 v[172:175], v[64:67], a[132:135], v[172:175]\n"
    "v_mfma_f32_16x16x32_bf16 v[172:175], v[68:71], a[136:139], v[172:175]\n"
    "v_mfma_f32_16x16x32_bf16 v[172:175], v[72:75], a[140:143], v[172:175]\n"
    "s_waitcnt vmcnt(0) lgkmcnt(0)\n"
    "s_barrier\n"
    "v_add_u32 v200, s70, v200\n"
    "v_add_u32 v201, s70, v201\n"
    "v_add_u32 v202, s70, v202\n"
    "v_add_u32 v203, s70, v203\n"
    "s_sub_u32 s70, 0, s70\n"
    "v_mfma_f32_16x16x32_bf16 v[160:163], v[76:79], a[144:147], v[160:163]\n"
    "ds_read_b64_tr_b16 v[40:41], v200 offset:0\n"
    "v_mfma_f32_16x16x32_bf16 v[160:163], v[80:83], a[148:151], v[160:163]\n"
    "ds_read_b64_tr_b16 v[42:43], v200 offset:1024\n"
    "v_mfma_f32_16x16x32_bf16 v[160:163], v[84:87], a[152:155], v[160:163]\n"
    "v_mfma_f32_16x16x32_bf16 v[160:163], v[88:91], a[156:159], v[160:163]\n"
    "ds_read_b64_tr_b16 v[44:45], v200 offset:2048\n"
    "v_mfma_f32_16x16x32_bf16 v[160:163], v[92:95], a[160:163], v[160:163]\n"
    "ds_read_b64_tr_b16 v[46:47], v200 offset:3072\n"
    "v_mfma_f32_16x16x32_bf16 v[160:163], v[96:99], a[164:167], v[160:163]\n"
    "v_mfma_f32_16x16x32_bf16 v[160:163], v[100:103], a[168:171], v[160:163]\n"
    "ds_read_b64_tr_b16 v[48:49], v200 offset:4096\n"
    "v_mfma_f32_16x16x32_bf16 v[160:163], v[104:107], a[172:175], v[160:163]\n"
    "ds_read_b64_tr_b16 v[50:51], v200 offset:5120\n"
    "v_mfma_f32_16x16x32_bf16 v[160:163], v[108:111], a[176:179], v[160:163]\n"
    "v_mfma_f32_16x16x32_bf16 v[164:167], v[76:79], a[180:183], v[164:167]\n"
    "ds_read_b64_tr_b16 v[52:53], v200 offset:6144\n"
    "v_mfma_f32_16x16x32_bf16 v[164:167], v[80:83], a[184:187], v[164:167]\n"
    "ds_read_b64_tr_b16 v[54:55], v200 offset:7168\n"
    "v_mfma_f32_16x16x32_bf16 v[164:167], v[84:87], a[188:191], v[164:167]\n"
    "v_mfma_f32_16x16x32_bf16 v[164:167], v[88:91], a[192:195], v[164:167]\n"
    "ds_read_b64_tr_b16 v[56:57], v200 offset:8192\n"
    "v_mfma_f32_16x16x32_bf16 v[164:167], v[92:95], a[196:199], v[164:167]\n"
    "ds_read_b64_tr_b16 v[58:59], v200 offset:9216\n"
    "v_mfma_f32_16x16x32_bf16 v[164:167], v[96:99], a[200:203], v[164:167]\n"
    "v_mfma_f32_16x16x32_bf16 v[164:167], v[100:103], a[204:207], v[164:167]\n"
    "ds_read_b64_tr_b16 v[60:61], v200 offset:10240\n"
    "v_mfma_f32_16x16x32_bf16 v[164:167], v[104:107], a[208:211], v[164:167]\n"
    "ds_read_b64_tr_b16 v[62:63], v200 offset:11264\n"
    "v_mfma_f32_16x16x32_bf16 v[164:167], v[108:111], a[212:215], v[164:167]\n"
    "v_mfma_f32_16x16x32_bf16 v[168:171], v[76:79], a[216:219], v[168:171]\n"
    "ds_read_b64_tr_b16 v[64:65], v200 offset:12288\n"
    "v_mfma_f32_16x16x32_bf16 v[168:171], v[80:83], a[220:223], v[168:171]\n"
    "ds_read_b64_tr_b16 v[66:67], v200 offset:13312\n"
    "v_mfma_f32_16x16x32_bf16 v[168:171], v[84:87], a[224:227], v[168:171]\n"
    "v_mfma_f32_16x16x32_bf16 v[168:171], v[88:91], a[228:231], v[168:171]\n"
    "ds_read_b64_tr_b16 v[68:69], v200 offset:14336\n"
    "v_mfma_f32_16x16x32_bf16 v[168:171], v[92:95], a[232:235], v[168:171]\n"
    "ds_read_b64_tr_b16 v[70:71], v200 offset:15360\n"
    "v_mfma_f32_16x16x32_bf16 v[168:171], v[96:99], a[236:239], v[168:171]\n"
    "v_mfma_f32_16x16x32_bf16 v[168:171], v[100:103], a[240:243], v[168:171]\n"
    "ds_read_b64_tr_b16 v[72:73], v200 offset:16384\n"
    "v_mfma_f32_16x16x32_bf16 v[168:171], v[104:107], a[244:247], v[168:171]\n"
    "ds_read_b64_tr_b16 v[74:75], v200 offset:17408\n"
    "v_mfma_f32_16x16x32_bf16 v[168:171], v[108:111], a[248:251], v[168:171]\n"
    "v_mfma_f32_16x16x32_bf16 v[172:175], v[76:79], a[252:255], v[172:175]\n"
    "v_mfma_f32_16x16x32_bf16 v[172:175], v[80:83], v[8:11], v[172:175]\n"
    "v_mfma_f32_16x16x32_bf16 v[172:175], v[84:87], v[12:15], v[172:175]\n"
    "v_mfma_f32_16x16x32_bf16 v[172:175], v[88:91], v[16:19], v[172:175]\n"
    "v_mfma_f32_16x16x32_bf16 v[172:175], v[92:95], v[20:23], v[172:175]\n"
    "v_mfma_f32_16x16x32_bf16 v[172:175], v[96:99], v[24:27], v[172:175]\n"
    "v_mfma_f32_16x16x32_bf16 v[172:175], v[100:103], v[28:31], v[172:175]\n"
    "v_mfma_f32_16x16x32_bf16 v[172:175], v[104:107], v[32:35], v[172:175]\n"
    "v_mfma_f32_16x16x32_bf16 v[172:175], v[108:111], v[36:39], v[172:175]\n"
    "s_mov_b32 s71, 4\n"
    "L_f_loop_%=:\n"
    "s_waitcnt lgkmcnt(0)\n"
    "v_mfma_f32_16x16x32_bf16 v[112:115], v[40:43], a[0:3], 0\n"
    "ds_read_b64_tr_b16 v[76:77], v200 offset:18432\n"
    "v_mul_f32 v192, v236, v144\n"
    "v_mfma_f32_16x16x32_bf16 v[112:115], v[44:47], a[4:7], v[112:115]\n"
    "ds_read_b64_tr_b16 v[78:79], v200 offset:19456\n"
    "v_mul_f32 v193, v236, v145\n"
    "v_mfma_f32_16x16x32_bf16 v[112:115], v[48:51], a[8:11], v[112:115]\n"
    "v_exp_f32 v192, v192\n"
    "v_mfma_f32_16x16x32_bf16 v[112:115], v[52:55], a[12:15], v[112:115]\n"
    "ds_read_b64_tr_b16 v[80:81], v200 offset:20480\n"
    "v_mul_f32 v194, v236, v146\n"
    "v_mfma_f32_16x16x32_bf16 v[112:115], v[56:59], a[16:19], v[112:115]\n"
    "ds_read_b64_tr_b16 v[82:83], v200 offset:21504\n"
    "v_mul_f32 v195, v236, v147\n"
    "v_mfma_f32_16x16x32_bf16 v[112:115], v[60:63], a[20:23], v[112:115]\n"
    "v_exp_f32 v193, v193\n"
    "v_mfma_f32_16x16x32_bf16 v[112:115], v[64:67], a[24:27], v[112:115]\n"
    "ds_read_b64_tr_b16 v[84:85], v200 offset:22528\n"
    "v_fma_f32 v192, v192, v240, v240\n"
    "v_mfma_f32_16x16x32_bf16 v[112:115], v[68:71], a[28:31], v[112:115]\n"
    "ds_read_b64_tr_b16 v[86:87], v200 offset:23552\n"
    "v_mul_f32 v160, v144, v160\n"
    "v_mfma_f32_16x16x32_bf16 v[112:115], v[72:75], a[32:35], v[112:115]\n"
    "v_exp_f32 v194, v194\n"
    "v_mfma_f32_16x16x32_bf16 v[116:119], v[40:43], a[36:39], 0\n"
    "ds_read_b64_tr_b16 v[88:89], v200 offset:24576\n"
    "v_fma_f32 v193, v193, v240, v240\n"
    "v_mfma_f32_16x16x32_bf16 v[116:119], v[44:47], a[40:43], v[116:119]\n"
    "ds_read_b64_tr_b16 v[90:91], v200 offset:25600\n"
    "v_mul_f32 v161, v145, v161\n"
    "v_mfma_f32_16x16x32_bf16 v[116:119], v[48:51], a[44:47], v[116:119]\n"
    "v_exp_f32 v195, v195\n"
    "v_mfma_f32_16x16x32_bf16 v[116:119], v[52:55], a[48:51], v[116:119]\n"
    "ds_read_b64_tr_b16 v[92:93], v200 offset:26624\n"
    "v_fma_f32 v194, v194, v240, v240\n"
    "v_mfma_f32_16x16x32_bf16 v[116:119], v[56:59], a[52:55], v[116:119]\n"
    "ds_read_b64_tr_b16 v[94:95], v200 offset:27648\n"
    "v_mul_f32 v162, v146, v162\n"
    "v_mfma_f32_16x16x32_bf16 v[116:119], v[60:63], a[56:59], v[116:119]\n"
    "v_rcp_f32 v192, v192\n"
    "v_mfma_f32_16x16x32_bf16 v[116:119], v[64:67], a[60:63], v[116:119]\n"
    "ds_read_b64_tr_b16 v[96:97], v200 offset:28672\n"
    "v_fma_f32 v195, v195, v240, v240\n"
    "v_mfma_f32_16x16x32_bf16 v[116:119], v[68:71], a[64:67], v[116:119]\n"
    "ds_read_b64_tr_b16 v[98:99], v200 offset:29696\n"
    "v_mul_f32 v163, v147, v163\n"
    "v_mfma_f32_16x16x32_bf16 v[116:119], v[72:75], a[68:71], v[116:119]\n"
    "v_rcp_f32 v193, v193\n"
    "v_mfma_f32_16x16x32_bf16 v[120:123], v[40:43], a[72:75], 0\n"
    "ds_read_b64_tr_b16 v[100:101], v200 offset:30720\n"
    "v_mul_f32 v160, v160, v192\n"
    "v_mfma_f32_16x16x32_bf16 v[120:123], v[44:47], a[76:79], v[120:123]\n"
    "ds_read_b64_tr_b16 v[102:103], v200 offset:31744\n"
    "v_mfma_f32_16x16x32_bf16 v[120:123], v[48:51], a[80:83], v[120:123]\n"
    "v_rcp_f32 v194, v194\n"
    "v_mfma_f32_16x16x32_bf16 v[120:123], v[52:55], a[84:87], v[120:123]\n"
    "ds_read_b64_tr_b16 v[104:105], v200 offset:32768\n"
    "v_mul_f32 v161, v161, v193\n"
    "v_mfma_f32_16x16x32_bf16 v[120:123], v[56:59], a[88:91], v[120:123]\n"
    "ds_read_b64_tr_b16 v[106:107], v200 offset:33792\n"
    "v_mul_f32 v162, v162, v194\n"
    "v_mfma_f32_16x16x32_bf16 v[120:123], v[60:63], a[92:95], v[120:123]\n"
    "v_rcp_f32 v195, v195\n"
    "v_mfma_f32_16x16x32_bf16 v[120:123], v[64:67], a[96:99], v[120:123]\n"
    "ds_read_b64_tr_b16 v[108:109], v200 offset:34816\n"
    "v_mfma_f32_16x16x32_bf16 v[120:123], v[68:71], a[100:103], v[120:123]\n"
    "s_mov_b32 s67, s65\n"
    "s_add_u32 m0, s64, 0\n"
    "ds_read_b64_tr_b16 v[110:111], v200 offset:35840\n"
    "v_mul_f32 v163, v163, v195\n"
    "v_mfma_f32_16x16x32_bf16 v[120:123], v[72:75], a[104:107], v[120:123]\n"
    "buffer_load_dwordx4 v250, s[40:43], s67 offen lds\n"
    "v_mfma_f32_16x16x32_bf16 v[124:127], v[40:43], a[108:111], 0\n"
    "s_add_u32 s67, s67, s68\n"
    "v_cvt_pk_bf16_f32 v178, v160, v161\n"
    "v_mfma_f32_16x16x32_bf16 v[124:127], v[44:47], a[112:115], v[124:127]\n"
    "s_add_u32 m0, s64, 4096\n"
    "v_cvt_pk_bf16_f32 v179, v162, v163\n"
    "v_mfma_f32_16x16x32_bf16 v[124:127], v[48:51], a[116:119], v[124:127]\n"
    "buffer_load_dwordx4 v250, s[40:43], s67 offen lds\n"
    "v_mfma_f32_16x16x32_bf16 v[124:127], v[52:55], a[120:123], v[124:127]\n"
    "s_add_u32 s67, s67, s68\n"
    "s_add_u32 m0, s64, 8192\n"
    "v_mfma_f32_16x16x32_bf16 v[124:127], v[56:59], a[124:127], v[124:127]\n"
    "buffer_load_dwordx4 v250, s[40:43], s67 offen lds\n"
    "v_mfma_f32_16x16x32_bf16 v[124:127], v[60:63], a[128:131], v[124:127]\n"
    "s_add_u32 s67, s67, s68\n"
    "s_add_u32 m0, s64, 12288\n"
    "v_mfma_f32_16x16x32_bf16 v[124:127], v[64:67], a[132:135], v[124:127]\n"
    "buffer_load_dwordx4 v250, s[40:43], s67 offen lds\n"
    "v_mfma_f32_16x16x32_bf16 v[124:127], v[68:71], a[136:139], v[124:127]\n"
    "s_add_u32 s67, s67, s68\n"
    "buffer_store_dwordx4 v[176:179], v252, s[48:51], s73 offen sc1\n"
    "v_mfma_f32_16x16x32_bf16 v[124:127], v[72:75], a[140:143], v[124:127]\n"
    "s_add_u32 s73, s73, 1024\n"
    "s_waitcnt lgkmcnt(0)\n"
    "v_mfma_f32_16x16x32_bf16 v[112:115], v[76:79], a[144:147], v[112:115]\n"
    "ds_read_b64_tr_b16 v[40:41], v202 offset:0\n"
    "v_mul_f32 v196, v237, v148\n"
    "v_mfma_f32_16x16x32_bf16 v[112:115], v[80:83], a[148:151], v[112:115]\n"
    "ds_read_b64_tr_b16 v[42:43], v202 offset:1024\n"
    "v_mul_f32 v197, v237, v149\n"
    "v_mfma_f32_16x16x32_bf16 v[112:115], v[84:87], a[152:155], v[112:115]\n"
    "v_exp_f32 v196, v196\n"
    "v_mfma_f32_16x16x32_bf16 v[112:115], v[88:91], a[156:159], v[112:115]\n"
    "ds_read_b64_tr_b16 v[44:45], v202 offset:2048\n"
    "v_mul_f32 v198, v237, v150\n"
    "v_mfma_f32_16x16x32_bf16 v[112:115], v[92:95], a[160:163], v[112:115]\n"
    "ds_read_b64_tr_b16 v[46:47], v202 offset:3072\n"
    "v_mul_f32 v199, v237, v151\n"
    "v_mfma_f32_16x16x32_bf16 v[112:115], v[96:99], a[164:167], v[112:115]\n"
    "v_exp_f32 v197, v197\n"
    "v_mfma_f32_16x16x32_bf16 v[112:115], v[100:103], a[168:171], v[112:115]\n"
    "ds_read_b64_tr_b16 v[48:49], v202 offset:4096\n"
    "v_fma_f32 v196, v196, v241, v241\n"
    "v_mfma_f32_16x16x32_bf16 v[112:115], v[104:107], a[172:175], v[112:115]\n"
    "ds_read_b64_tr_b16 v[50:51], v202 offset:5120\n"
    "v_mul_f32 v164, v148, v164\n"
    "v_mfma_f32_16x16x32_bf16 v[112:115], v[108:111], a[176:179], v[112:115]\n"
    "v_exp_f32 v198, v198\n"
    "v_mfma_f32_16x16x32_bf16 v[116:119], v[76:79], a[180:183], v[116:119]\n"
    "ds_read_b64_tr_b16 v[52:53], v202 offset:6144\n"
    "v_fma_f32 v197, v197, v241, v241\n"
    "v_mfma_f32_16x16x32_bf16 v[116:119], v[80:83], a[184:187], v[116:119]\n"
    "ds_read_b64_tr_b16 v[54:55], v202 offset:7168\n"
    "v_mul_f32 v165, v149, v165\n"
    "v_mfma_f32_16x16x32_bf16 v[116:119], v[84:87], a[188:191], v[116:119]\n"
    "v_exp_f32 v199, v199\n"
    "v_mfma_f32_16x16x32_bf16 v[116:119], v[88:91], a[192:195], v[116:119]\n"
    "ds_read_b64_tr_b16 v[56:57], v202 offset:8192\n"
    "v_fma_f32 v198, v198, v241, v241\n"
    "v_mfma_f32_16x16x32_bf16 v[116:119], v[92:95], a[196:199], v[116:119]\n"
    "ds_read_b64_tr_b16 v[58:59], v202 offset:9216\n"
    "v_mul_f32 v166, v150, v166\n"
    "v_mfma_f32_16x16x32_bf16 v[116:119], v[96:99], a[200:203], v[116:119]\n"
    "v_rcp_f32 v196, v196\n"
    "v_mfma_f32_16x16x32_bf16 v[116:119], v[100:103], a[204:207], v[116:119]\n"
    "ds_read_b64_tr_b16 v[60:61], v202 offset:10240\n"
    "v_fma_f32 v199, v199, v241, v241\n"
    "v_mfma_f32_16x16x32_bf16 v[116:119], v[104:107], a[208:211], v[116:119]\n"
    "ds_read_b64_tr_b16 v[62:63], v202 offset:11264\n"
    "v_mul_f32 v167, v151, v167\n"
    "v_mfma_f32_16x16x32_bf16 v[116:119], v[108:111], a[212:215], v[116:119]\n"
    "v_rcp_f32 v197, v197\n"
    "v_mfma_f32_16x16x32_bf16 v[120:123], v[76:79], a[216:219], v[120:123]\n"
    "ds_read_b64_tr_b16 v[64:65], v202 offset:12288\n"
    "v_mul_f32 v164, v164, v196\n"
    "v_mfma_f32_16x16x32_bf16 v[120:123], v[80:83], a[220:223], v[120:123]\n"
    "ds_read_b64_tr_b16 v[66:67], v202 offset:13312\n"
    "v_mfma_f32_16x16x32_bf16 v[120:123], v[84:87], a[224:227], v[120:123]\n"
    "v_rcp_f32 v198, v198\n"
    "v_mfma_f32_16x16x32_bf16 v[120:123], v[88:91], a[228:231], v[120:123]\n"
    "ds_read_b64_tr_b16 v[68:69], v202 offset:14336\n"
    "v_mul_f32 v165, v165, v197\n"
    "v_mfma_f32_16x16x32_bf16 v[120:123], v[92:95], a[232:235], v[120:123]\n"
    "ds_read_b64_tr_b16 v[70:71], v202 offset:15360\n"
    "v_mul_f32 v166, v166, v198\n"
    "v_mfma_f32_16x16x32_bf16 v[120:123], v[96:99], a[236:239], v[120:123]\n"
    "v_rcp_f32 v199, v199\n"
    "v_mfma_f32_16x16x32_bf16 v[120:123], v[100:103], a[240:243], v[120:123]\n"
    "ds_read_b64_tr_b16 v[72:73], v202 offset:16384\n"
    "v_mfma_f32_16x16x32_bf16 v[120:123], v[104:107], a[244:247], v[120:123]\n"
    "s_add_u32 m0, s64, 16384\n"
    "ds_read_b64_tr_b16 v[74:75], v202 offset:17408\n"
    "v_mul_f32 v167, v167, v199\n"
    "v_mfma_f32_16x16x32_bf16 v[120:123], v[108:111], a[248:251], v[120:123]\n"
    "buffer_load_dwordx4 v250, s[40:43], s67 offen lds\n"
    "v_mfma_f32_16x16x32_bf16 v[124:127], v[76:79], a[252:255], v[124:127]\n"
    "s_add_u32 s67, s67, s68\n"
    "v_cvt_pk_bf16_f32 v182, v164, v165\n"
    "v_mfma_f32_16x16x32_bf16 v[124:127], v[80:83], v[8:11], v[124:127]\n"
    "s_add_u32 m0, s64, 20480\n"
    "v_cvt_pk_bf16_f32 v183, v166, v167\n"
    "v_mfma_f32_16x16x32_bf16 v[124:127], v[84:87], v[12:15], v[124:127]\n"
    "buffer_load_dwordx4 v250, s[40:43], s67 offen lds\n"
    "v_mfma_f32_16x16x32_bf16 v[124:127], v[88:91], v[16:19], v[124:127]\n"
    "s_add_u32 s67, s67, s68\n"
    "s_add_u32 m0, s64, 24576\n"
    "v_mfma_f32_16x16x32_bf16 v[124:127], v[92:95], v[20:23], v[124:127]\n"
    "buffer_load_dwordx4 v250, s[40:43], s67 offen lds\n"
    "v_mfma_f32_16x16x32_bf16 v[124:127], v[96:99], v[24:27], v[124:127]\n"
    "s_add_u32 s67, s67, s68\n"
    "s_add_u32 m0, s64, 28672\n"
    "v_mfma_f32_16x16x32_bf16 v[124:127], v[100:103], v[28:31], v[124:127]\n"
    "buffer_load_dwordx4 v250, s[40:43], s67 offen lds\n"
    "v_mfma_f32_16x16x32_bf16 v[124:127], v[104:107], v[32:35], v[124:127]\n"
    "s_add_u32 s67, s67, s68\n"
    "buffer_store_dwordx4 v[180:183], v252, s[48:51], s74 offen sc1\n"
    "v_mfma_f32_16x16x32_bf16 v[124:127], v[108:111], v[36:39], v[124:127]\n"
    "s_add_u32 s74, s74, 1024\n"
    "s_waitcnt lgkmcnt(0)\n"
    "v_mfma_f32_16x16x32_bf16 v[128:131], v[40:43], a[0:3], 0\n"
    "ds_read_b64_tr_b16 v[76:77], v202 offset:18432\n"
    "v_mul_f32 v192, v238, v152\n"
    "v_mfma_f32_16x16x32_bf16 v[128:131], v[44:47], a[4:7], v[128:131]\n"
    "ds_read_b64_tr_b16 v[78:79], v202 offset:19456\n"
    "v_mul_f32 v193, v238, v153\n"
    "v_mfma_f32_16x16x32_bf16 v[128:131], v[48:51], a[8:11], v[128:131]\n"
    "v_exp_f32 v192, v192\n"
    "v_mfma_f32_16x16x32_bf16 v[128:131], v[52:55], a[12:15], v[128:131]\n"
    "ds_read_b64_tr_b16 v[80:81], v202 offset:20480\n"
    "v_mul_f32 v194, v238, v154\n"
    "v_mfma_f32_16x16x32_bf16 v[128:131], v[56:59], a[16:19], v[128:131]\n"
    "ds_read_b64_tr_b16 v[82:83], v202 offset:21504\n"
    "v_mul_f32 v195, v238, v155\n"
    "v_mfma_f32_16x16x32_bf16 v[128:131], v[60:63], a[20:23], v[128:131]\n"
    "v_exp_f32 v193, v193\n"
    "v_mfma_f32_16x16x32_bf16 v[128:131], v[64:67], a[24:27], v[128:131]\n"
    "ds_read_b64_tr_b16 v[84:85], v202 offset:22528\n"
    "v_fma_f32 v192, v192, v242, v242\n"
    "v_mfma_f32_16x16x32_bf16 v[128:131], v[68:71], a[28:31], v[128:131]\n"
    "ds_read_b64_tr_b16 v[86:87], v202 offset:23552\n"
    "v_mul_f32 v168, v152, v168\n"
    "v_mfma_f32_16x16x32_bf16 v[128:131], v[72:75], a[32:35], v[128:131]\n"
    "v_exp_f32 v194, v194\n"
    "v_mfma_f32_16x16x32_bf16 v[132:135], v[40:43], a[36:39], 0\n"
    "ds_read_b64_tr_b16 v[88:89], v202 offset:24576\n"
    "v_fma_f32 v193, v193, v242, v242\n"
    "v_mfma_f32_16x16x32_bf16 v[132:135], v[44:47], a[40:43], v[132:135]\n"
    "ds_read_b64_tr_b16 v[90:91], v202 offset:25600\n"
    "v_mul_f32 v169, v153, v169\n"
    "v_mfma_f32_16x16x32_bf16 v[132:135], v[48:51], a[44:47], v[132:135]\n"
    "v_exp_f32 v195, v195\n"
    "v_mfma_f32_16x16x32_bf16 v[132:135], v[52:55], a[48:51], v[132:135]\n"
    "ds_read_b64_tr_b16 v[92:93], v202 offset:26624\n"
    "v_fma_f32 v194, v194, v242, v242\n"
    "v_mfma_f32_16x16x32_bf16 v[132:135], v[56:59], a[52:55], v[132:135]\n"
    "ds_read_b64_tr_b16 v[94:95], v202 offset:27648\n"
    "v_mul_f32 v170, v154, v170\n"
    "v_mfma_f32_16x16x32_bf16 v[132:135], v[60:63], a[56:59], v[132:135]\n"
    "v_rcp_f32 v192, v192\n"
    "v_mfma_f32_16x16x32_bf16 v[132:135], v[64:67], a[60:63], v[132:135]\n"
    "ds_read_b64_tr_b16 v[96:97], v202 offset:28672\n"
    "v_fma_f32 v195, v195, v242, v242\n"
    "v_mfma_f32_16x16x32_bf16 v[132:135], v[68:71], a[64:67], v[132:135]\n"
    "ds_read_b64_tr_b16 v[98:99], v202 offset:29696\n"
    "v_mul_f32 v171, v155, v171\n"
    "v_mfma_f32_16x16x32_bf16 v[132:135], v[72:75], a[68:71], v[132:135]\n"
    "v_rcp_f32 v193, v193\n"
    "v_mfma_f32_16x16x32_bf16 v[136:139], v[40:43], a[72:75], 0\n"
    "ds_read_b64_tr_b16 v[100:101], v202 offset:30720\n"
    "v_mul_f32 v168, v168, v192\n"
    "v_mfma_f32_16x16x32_bf16 v[136:139], v[44:47], a[76:79], v[136:139]\n"
    "ds_read_b64_tr_b16 v[102:103], v202 offset:31744\n"
    "v_mfma_f32_16x16x32_bf16 v[136:139], v[48:51], a[80:83], v[136:139]\n"
    "v_rcp_f32 v194, v194\n"
    "v_mfma_f32_16x16x32_bf16 v[136:139], v[52:55], a[84:87], v[136:139]\n"
    "ds_read_b64_tr_b16 v[104:105], v202 offset:32768\n"
    "v_mul_f32 v169, v169, v193\n"
    "v_mfma_f32_16x16x32_bf16 v[136:139], v[56:59], a[88:91], v[136:139]\n"
    "ds_read_b64_tr_b16 v[106:107], v202 offset:33792\n"
    "v_mul_f32 v170, v170, v194\n"
    "v_mfma_f32_16x16x32_bf16 v[136:139], v[60:63], a[92:95], v[136:139]\n"
    "v_rcp_f32 v195, v195\n"
    "v_mfma_f32_16x16x32_bf16 v[136:139], v[64:67], a[96:99], v[136:139]\n"
    "ds_read_b64_tr_b16 v[108:109], v202 offset:34816\n"
    "v_mfma_f32_16x16x32_bf16 v[136:139], v[68:71], a[100:103], v[136:139]\n"
    "s_add_u32 m0, s64, 32768\n"
    "ds_read_b64_tr_b16 v[110:111], v202 offset:35840\n"
    "v_mul_f32 v171, v171, v195\n"
    "v_mfma_f32_16x16x32_bf16 v[136:139], v[72:75], a[104:107], v[136:139]\n"
    "buffer_load_dwordx4 v250, s[40:43], s67 offen lds\n"
    "v_mfma_f32_16x16x32_bf16 v[140:143], v[40:43], a[108:111], 0\n"
    "s_add_u32 s67, s67, s68\n"
    "v_cvt_pk_bf16_f32 v186, v168, v169\n"
    "v_mfma_f32_16x16x32_bf16 v[140:143], v[44:47], a[112:115], v[140:143]\n"
    "s_mov_b32 s67, s66\n"
    "s_add_u32 m0, s64, 36864\n"
    "v_cvt_pk_bf16_f32 v187, v170, v171\n"
    "v_mfma_f32_16x16x32_bf16 v[140:143], v[48:51], a[116:119], v[140:143]\n"
    "buffer_load_dwordx4 v250, s[44:47], s67 offen lds\n"
    "v_mfma_f32_16x16x32_bf16 v[140:143], v[52:55], a[120:123], v[140:143]\n"
    "s_add_u32 s67, s67, s68\n"
    "s_add_u32 m0, s64, 40960\n"
    "v_mfma_f32_16x16x32_bf16 v[140:143], v[56:59], a[124:127], v[140:143]\n"
    "buffer_load_dwordx4 v250, s[44:47], s67 offen lds\n"
    "v_mfma_f32_16x16x32_bf16 v[140:143], v[60:63], a[128:131], v[140:143]\n"
    "s_add_u32 s67, s67, s68\n"
    "s_add_u32 m0, s64, 45056\n"
    "v_mfma_f32_16x16x32_bf16 v[140:143], v[64:67], a[132:135], v[140:143]\n"
    "buffer_load_dwordx4 v250, s[44:47], s67 offen lds\n"
    "v_mfma_f32_16x16x32_bf16 v[140:143], v[68:71], a[136:139], v[140:143]\n"
    "s_add_u32 s67, s67, s68\n"
    "buffer_store_dwordx4 v[184:187], v252, s[48:51], s75 offen sc1\n"
    "v_mfma_f32_16x16x32_bf16 v[140:143], v[72:75], a[140:143], v[140:143]\n"
    "s_add_u32 s75, s75, 1024\n"
    "s_waitcnt lgkmcnt(0)\n"
    "v_mfma_f32_16x16x32_bf16 v[128:131], v[76:79], a[144:147], v[128:131]\n"
    "ds_read_b64_tr_b16 v[40:41], v201 offset:0\n"
    "v_mul_f32 v196, v239, v156\n"
    "v_mfma_f32_16x16x32_bf16 v[128:131], v[80:83], a[148:151], v[128:131]\n"
    "ds_read_b64_tr_b16 v[42:43], v201 offset:1024\n"
    "v_mul_f32 v197, v239, v157\n"
    "v_mfma_f32_16x16x32_bf16 v[128:131], v[84:87], a[152:155], v[128:131]\n"
    "v_exp_f32 v196, v196\n"
    "v_mfma_f32_16x16x32_bf16 v[128:131], v[88:91], a[156:159], v[128:131]\n"
    "ds_read_b64_tr_b16 v[44:45], v201 offset:2048\n"
    "v_mul_f32 v198, v239, v158\n"
    "v_mfma_f32_16x16x32_bf16 v[128:131], v[92:95], a[160:163], v[128:131]\n"
    "ds_read_b64_tr_b16 v[46:47], v201 offset:3072\n"
    "v_mul_f32 v199, v239, v159\n"
    "v_mfma_f32_16x16x32_bf16 v[128:131], v[96:99], a[164:167], v[128:131]\n"
    "v_exp_f32 v197, v197\n"
    "v_mfma_f32_16x16x32_bf16 v[128:131], v[100:103], a[168:171], v[128:131]\n"
    "ds_read_b64_tr_b16 v[48:49], v201 offset:4096\n"
    "v_fma_f32 v196, v196, v243, v243\n"
    "v_mfma_f32_16x16x32_bf16 v[128:131], v[104:107], a[172:175], v[128:131]\n"
    "ds_read_b64_tr_b16 v[50:51], v201 offset:5120\n"
    "v_mul_f32 v172, v156, v172\n"
    "v_mfma_f32_16x16x32_bf16 v[128:131], v[108:111], a[176:179], v[128:131]\n"
    "v_exp_f32 v198, v198\n"
    "v_mfma_f32_16x16x32_bf16 v[132:135], v[76:79], a[180:183], v[132:135]\n"
    "ds_read_b64_tr_b16 v[52:53], v201 offset:6144\n"
    "v_fma_f32 v197, v197, v243, v243\n"
    "v_mfma_f32_16x16x32_bf16 v[132:135], v[80:83], a[184:187], v[132:135]\n"
    "ds_read_b64_tr_b16 v[54:55], v201 offset:7168\n"
    "v_mul_f32 v173, v157, v173\n"
    "v_mfma_f32_16x16x32_bf16 v[132:135], v[84:87], a[188:191], v[132:135]\n"
    "v_exp_f32 v199, v199\n"
    "v_mfma_f32_16x16x32_bf16 v[132:135], v[88:91], a[192:195], v[132:135]\n"
    "ds_read_b64_tr_b16 v[56:57], v201 offset:8192\n"
    "v_fma_f32 v198, v198, v243, v243\n"
    "v_mfma_f32_16x16x32_bf16 v[132:135], v[92:95], a[196:199], v[132:135]\n"
    "ds_read_b64_tr_b16 v[58:59], v201 offset:9216\n"
    "v_mul_f32 v174, v158, v174\n"
    "v_mfma_f32_16x16x32_bf16 v[132:135], v[96:99], a[200:203], v[132:135]\n"
    "v_rcp_f32 v196, v196\n"
    "v_mfma_f32_16x16x32_bf16 v[132:135], v[100:103], a[204:207], v[132:135]\n"
    "ds_read_b64_tr_b16 v[60:61], v201 offset:10240\n"
    "v_fma_f32 v199, v199, v243, v243\n"
    "v_mfma_f32_16x16x32_bf16 v[132:135], v[104:107], a[208:211], v[132:135]\n"
    "ds_read_b64_tr_b16 v[62:63], v201 offset:11264\n"
    "v_mul_f32 v175, v159, v175\n"
    "v_mfma_f32_16x16x32_bf16 v[132:135], v[108:111], a[212:215], v[132:135]\n"
    "v_rcp_f32 v197, v197\n"
    "v_mfma_f32_16x16x32_bf16 v[136:139], v[76:79], a[216:219], v[136:139]\n"
    "ds_read_b64_tr_b16 v[64:65], v201 offset:12288\n"
    "v_mul_f32 v172, v172, v196\n"
    "v_mfma_f32_16x16x32_bf16 v[136:139], v[80:83], a[220:223], v[136:139]\n"
    "ds_read_b64_tr_b16 v[66:67], v201 offset:13312\n"
    "v_mfma_f32_16x16x32_bf16 v[136:139], v[84:87], a[224:227], v[136:139]\n"
    "v_rcp_f32 v198, v198\n"
    "v_mfma_f32_16x16x32_bf16 v[136:139], v[88:91], a[228:231], v[136:139]\n"
    "ds_read_b64_tr_b16 v[68:69], v201 offset:14336\n"
    "v_mul_f32 v173, v173, v197\n"
    "v_mfma_f32_16x16x32_bf16 v[136:139], v[92:95], a[232:235], v[136:139]\n"
    "ds_read_b64_tr_b16 v[70:71], v201 offset:15360\n"
    "v_mul_f32 v174, v174, v198\n"
    "v_mfma_f32_16x16x32_bf16 v[136:139], v[96:99], a[236:239], v[136:139]\n"
    "v_rcp_f32 v199, v199\n"
    "v_mfma_f32_16x16x32_bf16 v[136:139], v[100:103], a[240:243], v[136:139]\n"
    "ds_read_b64_tr_b16 v[72:73], v201 offset:16384\n"
    "v_mfma_f32_16x16x32_bf16 v[136:139], v[104:107], a[244:247], v[136:139]\n"
    "s_add_u32 m0, s64, 49152\n"
    "ds_read_b64_tr_b16 v[74:75], v201 offset:17408\n"
    "v_mul_f32 v175, v175, v199\n"
    "v_mfma_f32_16x16x32_bf16 v[136:139], v[108:111], a[248:251], v[136:139]\n"
    "buffer_load_dwordx4 v250, s[44:47], s67 offen lds\n"
    "v_mfma_f32_16x16x32_bf16 v[140:143], v[76:79], a[252:255], v[140:143]\n"
    "s_add_u32 s67, s67, s68\n"
    "v_cvt_pk_bf16_f32 v190, v172, v173\n"
    "v_mfma_f32_16x16x32_bf16 v[140:143], v[80:83], v[8:11], v[140:143]\n"
    "s_add_u32 m0, s64, 53248\n"
    "v_cvt_pk_bf16_f32 v191, v174, v175\n"
    "v_mfma_f32_16x16x32_bf16 v[140:143], v[84:87], v[12:15], v[140:143]\n"
    "buffer_load_dwordx4 v250, s[44:47], s67 offen lds\n"
    "v_mfma_f32_16x16x32_bf16 v[140:143], v[88:91], v[16:19], v[140:143]\n"
    "s_add_u32 s67, s67, s68\n"
    "s_add_u32 m0, s64, 57344\n"
    "v_mfma_f32_16x16x32_bf16 v[140:143], v[92:95], v[20:23], v[140:143]\n"
    "buffer_load_dwordx4 v250, s[44:47], s67 offen lds\n"
    "v_mfma_f32_16x16x32_bf16 v[140:143], v[96:99], v[24:27], v[140:143]\n"
    "s_add_u32 s67, s67, s68\n"
    "v_mfma_f32_16x16x32_bf16 v[140:143], v[100:103], v[28:31], v[140:143]\n"
    "v_mfma_f32_16x16x32_bf16 v[140:143], v[104:107], v[32:35], v[140:143]\n"
    "buffer_store_dwordx4 v[188:191], v252, s[48:51], s76 offen sc1\n"
    "v_mfma_f32_16x16x32_bf16 v[140:143], v[108:111], v[36:39], v[140:143]\n"
    "s_add_u32 s76, s76, 1024\n"
    "s_waitcnt lgkmcnt(0)\n"
    "s_cmp_eq_u32 s71, 4\n"
    "s_cbranch_scc1 L_fhalf_%=\n"
    "L_fhalf_ret_%=:\n"
    "v_mfma_f32_16x16x32_bf16 v[144:147], v[40:43], a[0:3], 0\n"
    "ds_read_b64_tr_b16 v[76:77], v201 offset:18432\n"
    "v_mul_f32 v192, v236, v112\n"
    "v_mfma_f32_16x16x32_bf16 v[144:147], v[44:47], a[4:7], v[144:147]\n"
    "ds_read_b64_tr_b16 v[78:79], v201 offset:19456\n"
    "v_mul_f32 v193, v236, v113\n"
    "v_mfma_f32_16x16x32_bf16 v[144:147], v[48:51], a[8:11], v[144:147]\n"
    "v_exp_f32 v192, v192\n"
    "v_mfma_f32_16x16x32_bf16 v[144:147], v[52:55], a[12:15], v[144:147]\n"
    "ds_read_b64_tr_b16 v[80:81], v201 offset:20480\n"
    "v_mul_f32 v194, v236, v114\n"
    "v_mfma_f32_16x16x32_bf16 v[144:147], v[56:59], a[16:19], v[144:147]\n"
    "ds_read_b64_tr_b16 v[82:83], v201 offset:21504\n"
    "v_mul_f32 v195, v236, v115\n"
    "v_mfma_f32_16x16x32_bf16 v[144:147], v[60:63], a[20:23], v[144:147]\n"
    "v_exp_f32 v193, v193\n"
    "v_mfma_f32_16x16x32_bf16 v[144:147], v[64:67], a[24:27], v[144:147]\n"
    "ds_read_b64_tr_b16 v[84:85], v201 offset:22528\n"
    "v_fma_f32 v192, v192, v240, v240\n"
    "v_mfma_f32_16x16x32_bf16 v[144:147], v[68:71], a[28:31], v[144:147]\n"
    "ds_read_b64_tr_b16 v[86:87], v201 offset:23552\n"
    "v_mul_f32 v128, v112, v128\n"
    "v_mfma_f32_16x16x32_bf16 v[144:147], v[72:75], a[32:35], v[144:147]\n"
    "v_exp_f32 v194, v194\n"
    "v_mfma_f32_16x16x32_bf16 v[148:151], v[40:43], a[36:39], 0\n"
    "ds_read_b64_tr_b16 v[88:89], v201 offset:24576\n"
    "v_fma_f32 v193, v193, v240, v240\n"
    "v_mfma_f32_16x16x32_bf16 v[148:151], v[44:47], a[40:43], v[148:151]\n"
    "ds_read_b64_tr_b16 v[90:91], v201 offset:25600\n"
    "v_mul_f32 v129, v113, v129\n"
    "v_mfma_f32_16x16x32_bf16 v[148:151], v[48:51], a[44:47], v[148:151]\n"
    "v_exp_f32 v195, v195\n"
    "v_mfma_f32_16x16x32_bf16 v[148:151], v[52:55], a[48:51], v[148:151]\n"
    "ds_read_b64_tr_b16 v[92:93], v201 offset:26624\n"
    "v_fma_f32 v194, v194, v240, v240\n"
    "v_mfma_f32_16x16x32_bf16 v[148:151], v[56:59], a[52:55], v[148:151]\n"
    "ds_read_b64_tr_b16 v[94:95], v201 offset:27648\n"
    "v_mul_f32 v130, v114, v130\n"
    "v_mfma_f32_16x16x32_bf16 v[148:151], v[60:63], a[56:59], v[148:151]\n"
    "v_rcp_f32 v192, v192\n"
    "v_mfma_f32_16x16x32_bf16 v[148:151], v[64:67], a[60:63], v[148:151]\n"
    "ds_read_b64_tr_b16 v[96:97], v201 offset:28672\n"
    "v_fma_f32 v195, v195, v240, v240\n"
    "v_mfma_f32_16x16x32_bf16 v[148:151], v[68:71], a[64:67], v[148:151]\n"
    "ds_read_b64_tr_b16 v[98:99], v201 offset:29696\n"
    "v_mul_f32 v131, v115, v131\n"
    "v_mfma_f32_16x16x32_bf16 v[148:151], v[72:75], a[68:71], v[148:151]\n"
    "v_rcp_f32 v193, v193\n"
    "v_mfma_f32_16x16x32_bf16 v[152:155], v[40:43], a[72:75], 0\n"
    "ds_read_b64_tr_b16 v[100:101], v201 offset:30720\n"
    "v_mul_f32 v128, v128, v192\n"
    "v_mfma_f32_16x16x32_bf16 v[152:155], v[44:47], a[76:79], v[152:155]\n"
    "ds_read_b64_tr_b16 v[102:103], v201 offset:31744\n"
    "v_mfma_f32_16x16x32_bf16 v[152:155], v[48:51], a[80:83], v[152:155]\n"
    "v_rcp_f32 v194, v194\n"
    "v_mfma_f32_16x16x32_bf16 v[152:155], v[52:55], a[84:87], v[152:155]\n"
    "ds_read_b64_tr_b16 v[104:105], v201 offset:32768\n"
    "v_mul_f32 v129, v129, v193\n"
    "v_mfma_f32_16x16x32_bf16 v[152:155], v[56:59], a[88:91], v[152:155]\n"
    "ds_read_b64_tr_b16 v[106:107], v201 offset:33792\n"
    "v_mul_f32 v130, v130, v194\n"
    "v_mfma_f32_16x16x32_bf16 v[152:155], v[60:63], a[92:95], v[152:155]\n"
    "v_rcp_f32 v195, v195\n"
    "v_mfma_f32_16x16x32_bf16 v[152:155], v[64:67], a[96:99], v[152:155]\n"
    "ds_read_b64_tr_b16 v[108:109], v201 offset:34816\n"
    "v_mfma_f32_16x16x32_bf16 v[152:155], v[68:71], a[100:103], v[152:155]\n"
    "s_add_u32 m0, s64, 61440\n"
    "ds_read_b64_tr_b16 v[110:111], v201 offset:35840\n"
    "v_mul_f32 v131, v131, v195\n"
    "v_mfma_f32_16x16x32_bf16 v[152:155], v[72:75], a[104:107], v[152:155]\n"
    "buffer_load_dwordx4 v250, s[44:47], s67 offen lds\n"
    "v_mfma_f32_16x16x32_bf16 v[156:159], v[40:43], a[108:111], 0\n"
    "s_add_u32 s67, s67, s68\n"
    "v_cvt_pk_bf16_f32 v176, v128, v129\n"
    "v_mfma_f32_16x16x32_bf16 v[156:159], v[44:47], a[112:115], v[156:159]\n"
    "s_add_u32 m0, s64, 65536\n"
    "v_cvt_pk_bf16_f32 v177, v130, v131\n"
    "v_mfma_f32_16x16x32_bf16 v[156:159], v[48:51], a[116:119], v[156:159]\n"
    "buffer_load_dwordx4 v250, s[44:47], s67 offen lds\n"
    "v_mfma_f32_16x16x32_bf16 v[156:159], v[52:55], a[120:123], v[156:159]\n"
    "s_add_u32 s67, s67, s68\n"
    "s_add_u32 m0, s64, 69632\n"
    "v_mfma_f32_16x16x32_bf16 v[156:159], v[56:59], a[124:127], v[156:159]\n"
    "buffer_load_dwordx4 v250, s[44:47], s67 offen lds\n"
    "v_mfma_f32_16x16x32_bf16 v[156:159], v[60:63], a[128:131], v[156:159]\n"
    "s_add_u32 s67, s67, s68\n"
    "v_mfma_f32_16x16x32_bf16 v[156:159], v[64:67], a[132:135], v[156:159]\n"
    "v_mfma_f32_16x16x32_bf16 v[156:159], v[68:71], a[136:139], v[156:159]\n"
    "v_mfma_f32_16x16x32_bf16 v[156:159], v[72:75], a[140:143], v[156:159]\n"
    "s_waitcnt lgkmcnt(0)\n"
    "v_mfma_f32_16x16x32_bf16 v[144:147], v[76:79], a[144:147], v[144:147]\n"
    "ds_read_b64_tr_b16 v[40:41], v203 offset:0\n"
    "v_mul_f32 v196, v237, v116\n"
    "v_mfma_f32_16x16x32_bf16 v[144:147], v[80:83], a[148:151], v[144:147]\n"
    "ds_read_b64_tr_b16 v[42:43], v203 offset:1024\n"
    "v_mul_f32 v197, v237, v117\n"
    "v_mfma_f32_16x16x32_bf16 v[144:147], v[84:87], a[152:155], v[144:147]\n"
    "v_exp_f32 v196, v196\n"
    "v_mfma_f32_16x16x32_bf16 v[144:147], v[88:91], a[156:159], v[144:147]\n"
    "ds_read_b64_tr_b16 v[44:45], v203 offset:2048\n"
    "v_mul_f32 v198, v237, v118\n"
    "v_mfma_f32_16x16x32_bf16 v[144:147], v[92:95], a[160:163], v[144:147]\n"
    "ds_read_b64_tr_b16 v[46:47], v203 offset:3072\n"
    "v_mul_f32 v199, v237, v119\n"
    "v_mfma_f32_16x16x32_bf16 v[144:147], v[96:99], a[164:167], v[144:147]\n"
    "v_exp_f32 v197, v197\n"
    "v_mfma_f32_16x16x32_bf16 v[144:147], v[100:103], a[168:171], v[144:147]\n"
    "ds_read_b64_tr_b16 v[48:49], v203 offset:4096\n"
    "v_fma_f32 v196, v196, v241, v241\n"
    "v_mfma_f32_16x16x32_bf16 v[144:147], v[104:107], a[172:175], v[144:147]\n"
    "ds_read_b64_tr_b16 v[50:51], v203 offset:5120\n"
    "v_mul_f32 v132, v116, v132\n"
    "v_mfma_f32_16x16x32_bf16 v[144:147], v[108:111], a[176:179], v[144:147]\n"
    "v_exp_f32 v198, v198\n"
    "v_mfma_f32_16x16x32_bf16 v[148:151], v[76:79], a[180:183], v[148:151]\n"
    "ds_read_b64_tr_b16 v[52:53], v203 offset:6144\n"
    "v_fma_f32 v197, v197, v241, v241\n"
    "v_mfma_f32_16x16x32_bf16 v[148:151], v[80:83], a[184:187], v[148:151]\n"
    "ds_read_b64_tr_b16 v[54:55], v203 offset:7168\n"
    "v_mul_f32 v133, v117, v133\n"
    "v_mfma_f32_16x16x32_bf16 v[148:151], v[84:87], a[188:191], v[148:151]\n"
    "v_exp_f32 v199, v199\n"
    "v_mfma_f32_16x16x32_bf16 v[148:151], v[88:91], a[192:195], v[148:151]\n"
    "ds_read_b64_tr_b16 v[56:57], v203 offset:8192\n"
    "v_fma_f32 v198, v198, v241, v241\n"
    "v_mfma_f32_16x16x32_bf16 v[148:151], v[92:95], a[196:199], v[148:151]\n"
    "ds_read_b64_tr_b16 v[58:59], v203 offset:9216\n"
    "v_mul_f32 v134, v118, v134\n"
    "v_mfma_f32_16x16x32_bf16 v[148:151], v[96:99], a[200:203], v[148:151]\n"
    "v_rcp_f32 v196, v196\n"
    "v_mfma_f32_16x16x32_bf16 v[148:151], v[100:103], a[204:207], v[148:151]\n"
    "ds_read_b64_tr_b16 v[60:61], v203 offset:10240\n"
    "v_fma_f32 v199, v199, v241, v241\n"
    "v_mfma_f32_16x16x32_bf16 v[148:151], v[104:107], a[208:211], v[148:151]\n"
    "ds_read_b64_tr_b16 v[62:63], v203 offset:11264\n"
    "v_mul_f32 v135, v119, v135\n"
    "v_mfma_f32_16x16x32_bf16 v[148:151], v[108:111], a[212:215], v[148:151]\n"
    "v_rcp_f32 v197, v197\n"
    "v_mfma_f32_16x16x32_bf16 v[152:155], v[76:79], a[216:219], v[152:155]\n"
    "ds_read_b64_tr_b16 v[64:65], v203 offset:12288\n"
    "v_mul_f32 v132, v132, v196\n"
    "v_mfma_f32_16x16x32_bf16 v[152:155], v[80:83], a[220:223], v[152:155]\n"
    "s_add_u32 s64, s64, s69\n"
    "s_sub_u32 s69, 0, s69\n"
    "ds_read_b64_tr_b16 v[66:67], v203 offset:13312\n"
    "v_mfma_f32_16x16x32_bf16 v[152:155], v[84:87], a[224:227], v[152:155]\n"
    "v_rcp_f32 v198, v198\n"
    "v_mfma_f32_16x16x32_bf16 v[152:155], v[88:91], a[228:231], v[152:155]\n"
    "s_cmp_eq_u32 s71, 1\n"
    "s_cselect_b32 s96, 128, 64\n"
    "ds_read_b64_tr_b16 v[68:69], v203 offset:14336\n"
    "v_mul_f32 v133, v133, v197\n"
    "v_mfma_f32_16x16x32_bf16 v[152:155], v[92:95], a[232:235], v[152:155]\n"
    "ds_read_b64_tr_b16 v[70:71], v203 offset:15360\n"
    "v_mul_f32 v134, v134, v198\n"
    "v_mfma_f32_16x16x32_bf16 v[152:155], v[96:99], a[236:239], v[152:155]\n"
    "v_rcp_f32 v199, v199\n"
    "v_mfma_f32_16x16x32_bf16 v[152:155], v[100:103], a[240:243], v[152:155]\n"
    "s_add_u32 s65, s65, s96\n"
    "s_add_u32 s66, s66, s96\n"
    "ds_read_b64_tr_b16 v[72:73], v203 offset:16384\n"
    "v_mfma_f32_16x16x32_bf16 v[152:155], v[104:107], a[244:247], v[152:155]\n"
    "ds_read_b64_tr_b16 v[74:75], v203 offset:17408\n"
    "v_mul_f32 v135, v135, v199\n"
    "v_mfma_f32_16x16x32_bf16 v[152:155], v[108:111], a[248:251], v[152:155]\n"
    "v_mfma_f32_16x16x32_bf16 v[156:159], v[76:79], a[252:255], v[156:159]\n"
    "v_cvt_pk_bf16_f32 v180, v132, v133\n"
    "v_mfma_f32_16x16x32_bf16 v[156:159], v[80:83], v[8:11], v[156:159]\n"
    "v_cvt_pk_bf16_f32 v181, v134, v135\n"
    "v_mfma_f32_16x16x32_bf16 v[156:159], v[84:87], v[12:15], v[156:159]\n"
    "v_mfma_f32_16x16x32_bf16 v[156:159], v[88:91], v[16:19], v[156:159]\n"
    "s_mov_b32 s50, s72\n"
    "v_mfma_f32_16x16x32_bf16 v[156:159], v[92:95], v[20:23], v[156:159]\n"
    "s_cmp_eq_u32 s71, 2\n"
    "s_cbranch_scc0 L_noswitch_%=\n"
    "s_mov_b32 s40, s26\n"
    "s_and_b32 s41, s27, 0xffff\n"
    "s_mov_b32 s42, 4718592\n"
    "s_mov_b32 s44, s26\n"
    "s_and_b32 s45, s27, 0xffff\n"
    "s_mov_b32 s46, 4718592\n"
    "s_mov_b32 s68, 524288\n"
    "s_mov_b32 s65, s89\n"
    "s_add_u32 s66, s89, 64\n"
    "v_mov_b32 v250, v251\n"
    "v_mov_b32 v211, v212\n"
    "s_mov_b32 s81, s2\n"
    "L_noswitch_%=:\n"
    "v_mfma_f32_16x16x32_bf16 v[156:159], v[96:99], v[24:27], v[156:159]\n"
    "v_mfma_f32_16x16x32_bf16 v[156:159], v[100:103], v[28:31], v[156:159]\n"
    "v_mfma_f32_16x16x32_bf16 v[156:159], v[104:107], v[32:35], v[156:159]\n"
    "v_mfma_f32_16x16x32_bf16 v[156:159], v[108:111], v[36:39], v[156:159]\n"
    "s_waitcnt lgkmcnt(0)\n"
    "v_mfma_f32_16x16x32_bf16 v[160:163], v[40:43], a[0:3], 0\n"
    "ds_read_b64_tr_b16 v[76:77], v203 offset:18432\n"
    "v_mul_f32 v192, v238, v120\n"
    "v_mfma_f32_16x16x32_bf16 v[160:163], v[44:47], a[4:7], v[160:163]\n"
    "ds_read_b64_tr_b16 v[78:79], v203 offset:19456\n"
    "v_mul_f32 v193, v238, v121\n"
    "v_mfma_f32_16x16x32_bf16 v[160:163], v[48:51], a[8:11], v[160:163]\n"
    "v_exp_f32 v192, v192\n"
    "v_mfma_f32_16x16x32_bf16 v[160:163], v[52:55], a[12:15], v[160:163]\n"
    "ds_read_b64_tr_b16 v[80:81], v203 offset:20480\n"
    "v_mul_f32 v194, v238, v122\n"
    "v_mfma_f32_16x16x32_bf16 v[160:163], v[56:59], a[16:19], v[160:163]\n"
    "ds_read_b64_tr_b16 v[82:83], v203 offset:21504\n"
    "v_mul_f32 v195, v238, v123\n"
    "v_mfma_f32_16x16x32_bf16 v[160:163], v[60:63], a[20:23], v[160:163]\n"
    "v_exp_f32 v193, v193\n"
    "v_mfma_f32_16x16x32_bf16 v[160:163], v[64:67], a[24:27], v[160:163]\n"
    "ds_read_b64_tr_b16 v[84:85], v203 offset:22528\n"
    "v_fma_f32 v192, v192, v242, v242\n"
    "v_mfma_f32_16x16x32_bf16 v[160:163], v[68:71], a[28:31], v[160:163]\n"
    "ds_read_b64_tr_b16 v[86:87], v203 offset:23552\n"
    "v_mul_f32 v136, v120, v136\n"
    "v_mfma_f32_16x16x32_bf16 v[160:163], v[72:75], a[32:35], v[160:163]\n"
    "v_exp_f32 v194, v194\n"
    "v_mfma_f32_16x16x32_bf16 v[164:167], v[40:43], a[36:39], 0\n"
    "ds_read_b64_tr_b16 v[88:89], v203 offset:24576\n"
    "v_fma_f32 v193, v193, v242, v242\n"
    "v_mfma_f32_16x16x32_bf16 v[164:167], v[44:47], a[40:43], v[164:167]\n"
    "ds_read_b64_tr_b16 v[90:91], v203 offset:25600\n"
    "v_mul_f32 v137, v121, v137\n"
    "v_mfma_f32_16x16x32_bf16 v[164:167], v[48:51], a[44:47], v[164:167]\n"
    "v_exp_f32 v195, v195\n"
    "v_mfma_f32_16x16x32_bf16 v[164:167], v[52:55], a[48:51], v[164:167]\n"
    "ds_read_b64_tr_b16 v[92:93], v203 offset:26624\n"
    "v_fma_f32 v194, v194, v242, v242\n"
    "v_mfma_f32_16x16x32_bf16 v[164:167], v[56:59], a[52:55], v[164:167]\n"
    "ds_read_b64_tr_b16 v[94:95], v203 offset:27648\n"
    "v_mul_f32 v138, v122, v138\n"
    "v_mfma_f32_16x16x32_bf16 v[164:167], v[60:63], a[56:59], v[164:167]\n"
    "v_rcp_f32 v192, v192\n"
    "v_mfma_f32_16x16x32_bf16 v[164:167], v[64:67], a[60:63], v[164:167]\n"
    "ds_read_b64_tr_b16 v[96:97], v203 offset:28672\n"
    "v_fma_f32 v195, v195, v242, v242\n"
    "v_mfma_f32_16x16x32_bf16 v[164:167], v[68:71], a[64:67], v[164:167]\n"
    "ds_read_b64_tr_b16 v[98:99], v203 offset:29696\n"
    "v_mul_f32 v139, v123, v139\n"
    "v_mfma_f32_16x16x32_bf16 v[164:167], v[72:75], a[68:71], v[164:167]\n"
    "v_rcp_f32 v193, v193\n"
    "v_mfma_f32_16x16x32_bf16 v[168:171], v[40:43], a[72:75], 0\n"
    "ds_read_b64_tr_b16 v[100:101], v203 offset:30720\n"
    "v_mul_f32 v136, v136, v192\n"
    "v_mfma_f32_16x16x32_bf16 v[168:171], v[44:47], a[76:79], v[168:171]\n"
    "ds_read_b64_tr_b16 v[102:103], v203 offset:31744\n"
    "v_mfma_f32_16x16x32_bf16 v[168:171], v[48:51], a[80:83], v[168:171]\n"
    "v_rcp_f32 v194, v194\n"
    "v_mfma_f32_16x16x32_bf16 v[168:171], v[52:55], a[84:87], v[168:171]\n"
    "ds_read_b64_tr_b16 v[104:105], v203 offset:32768\n"
    "v_mul_f32 v137, v137, v193\n"
    "v_mfma_f32_16x16x32_bf16 v[168:171], v[56:59], a[88:91], v[168:171]\n"
    "ds_read_b64_tr_b16 v[106:107], v203 offset:33792\n"
    "v_mul_f32 v138, v138, v194\n"
    "v_mfma_f32_16x16x32_bf16 v[168:171], v[60:63], a[92:95], v[168:171]\n"
    "v_rcp_f32 v195, v195\n"
    "v_mfma_f32_16x16x32_bf16 v[168:171], v[64:67], a[96:99], v[168:171]\n"
    "ds_read_b64_tr_b16 v[108:109], v203 offset:34816\n"
    "v_mfma_f32_16x16x32_bf16 v[168:171], v[68:71], a[100:103], v[168:171]\n"
    "ds_read_b64_tr_b16 v[110:111], v203 offset:35840\n"
    "v_mul_f32 v139, v139, v195\n"
    "v_mfma_f32_16x16x32_bf16 v[168:171], v[72:75], a[104:107], v[168:171]\n"
    "s_add_u32 m0, s85, 163328\n"
    "s_sub_u32 s96, s65, s81\n"
    "buffer_load_dword v211, s[40:43], s96 offen lds\n"
    "v_mfma_f32_16x16x32_bf16 v[172:175], v[40:43], a[108:111], 0\n"
    "v_cvt_pk_bf16_f32 v184, v136, v137\n"
    "v_mfma_f32_16x16x32_bf16 v[172:175], v[44:47], a[112:115], v[172:175]\n"
    "v_cvt_pk_bf16_f32 v185, v138, v139\n"
    "v_mfma_f32_16x16x32_bf16 v[172:175], v[48:51], a[116:119], v[172:175]\n"
    "s_sub_u32 s96, s66, s81\n"
    "buffer_load_dword v211, s[44:47], s96 offen lds\n"
    "v_mfma_f32_16x16x32_bf16 v[172:175], v[52:55], a[120:123], v[172:175]\n"
    "v_mfma_f32_16x16x32_bf16 v[172:175], v[56:59], a[124:127], v[172:175]\n"
    "v_mfma_f32_16x16x32_bf16 v[172:175], v[60:63], a[128:131], v[172:175]\n"
    "v_mfma_f32_16x16x32_bf16 v[172:175], v[64:67], a[132:135], v[172:175]\n"
    "v_mfma_f32_16x16x32_bf16 v[172:175], v[68:71], a[136:139], v[172:175]\n"
    "v_mfma_f32_16x16x32_bf16 v[172:175], v[72:75], a[140:143], v[172:175]\n"
    "s_waitcnt vmcnt(2) lgkmcnt(0)\n"
    "s_barrier\n"
    "v_add_u32 v200, s70, v200\n"
    "v_add_u32 v201, s70, v201\n"
    "v_add_u32 v202, s70, v202\n"
    "v_add_u32 v203, s70, v203\n"
    "s_sub_u32 s70, 0, s70\n"
    "v_mfma_f32_16x16x32_bf16 v[160:163], v[76:79], a[144:147], v[160:163]\n"
    "ds_read_b64_tr_b16 v[40:41], v200 offset:0\n"
    "v_mul_f32 v196, v239, v124\n"
    "v_mfma_f32_16x16x32_bf16 v[160:163], v[80:83], a[148:151], v[160:163]\n"
    "ds_read_b64_tr_b16 v[42:43], v200 offset:1024\n"
    "v_mul_f32 v197, v239, v125\n"
    "v_mfma_f32_16x16x32_bf16 v[160:163], v[84:87], a[152:155], v[160:163]\n"
    "v_exp_f32 v196, v196\n"
    "v_mfma_f32_16x16x32_bf16 v[160:163], v[88:91], a[156:159], v[160:163]\n"
    "ds_read_b64_tr_b16 v[44:45], v200 offset:2048\n"
    "v_mul_f32 v198, v239, v126\n"
    "v_mfma_f32_16x16x32_bf16 v[160:163], v[92:95], a[160:163], v[160:163]\n"
    "ds_read_b64_tr_b16 v[46:47], v200 offset:3072\n"
    "v_mul_f32 v199, v239, v127\n"
    "v_mfma_f32_16x16x32_bf16 v[160:163], v[96:99], a[164:167], v[160:163]\n"
    "v_exp_f32 v197, v197\n"
    "v_mfma_f32_16x16x32_bf16 v[160:163], v[100:103], a[168:171], v[160:163]\n"
    "ds_read_b64_tr_b16 v[48:49], v200 offset:4096\n"
    "v_fma_f32 v196, v196, v243, v243\n"
    "v_mfma_f32_16x16x32_bf16 v[160:163], v[104:107], a[172:175], v[160:163]\n"
    "ds_read_b64_tr_b16 v[50:51], v200 offset:5120\n"
    "v_mul_f32 v140, v124, v140\n"
    "v_mfma_f32_16x16x32_bf16 v[160:163], v[108:111], a[176:179], v[160:163]\n"
    "v_exp_f32 v198, v198\n"
    "v_mfma_f32_16x16x32_bf16 v[164:167], v[76:79], a[180:183], v[164:167]\n"
    "ds_read_b64_tr_b16 v[52:53], v200 offset:6144\n"
    "v_fma_f32 v197, v197, v243, v243\n"
    "v_mfma_f32_16x16x32_bf16 v[164:167], v[80:83], a[184:187], v[164:167]\n"
    "ds_read_b64_tr_b16 v[54:55], v200 offset:7168\n"
    "v_mul_f32 v141, v125, v141\n"
    "v_mfma_f32_16x16x32_bf16 v[164:167], v[84:87], a[188:191], v[164:167]\n"
    "v_exp_f32 v199, v199\n"
    "v_mfma_f32_16x16x32_bf16 v[164:167], v[88:91], a[192:195], v[164:167]\n"
    "ds_read_b64_tr_b16 v[56:57], v200 offset:8192\n"
    "v_fma_f32 v198, v198, v243, v243\n"
    "v_mfma_f32_16x16x32_bf16 v[164:167], v[92:95], a[196:199], v[164:167]\n"
    "ds_read_b64_tr_b16 v[58:59], v200 offset:9216\n"
    "v_mul_f32 v142, v126, v142\n"
    "v_mfma_f32_16x16x32_bf16 v[164:167], v[96:99], a[200:203], v[164:167]\n"
    "v_rcp_f32 v196, v196\n"
    "v_mfma_f32_16x16x32_bf16 v[164:167], v[100:103], a[204:207], v[164:167]\n"
    "ds_read_b64_tr_b16 v[60:61], v200 offset:10240\n"
    "v_fma_f32 v199, v199, v243, v243\n"
    "v_mfma_f32_16x16x32_bf16 v[164:167], v[104:107], a[208:211], v[164:167]\n"
    "ds_read_b64_tr_b16 v[62:63], v200 offset:11264\n"
    "v_mul_f32 v143, v127, v143\n"
    "v_mfma_f32_16x16x32_bf16 v[164:167], v[108:111], a[212:215], v[164:167]\n"
    "v_rcp_f32 v197, v197\n"
    "v_mfma_f32_16x16x32_bf16 v[168:171], v[76:79], a[216:219], v[168:171]\n"
    "ds_read_b64_tr_b16 v[64:65], v200 offset:12288\n"
    "v_mul_f32 v140, v140, v196\n"
    "v_mfma_f32_16x16x32_bf16 v[168:171], v[80:83], a[220:223], v[168:171]\n"
    "ds_read_b64_tr_b16 v[66:67], v200 offset:13312\n"
    "v_mfma_f32_16x16x32_bf16 v[168:171], v[84:87], a[224:227], v[168:171]\n"
    "v_rcp_f32 v198, v198\n"
    "v_mfma_f32_16x16x32_bf16 v[168:171], v[88:91], a[228:231], v[168:171]\n"
    "ds_read_b64_tr_b16 v[68:69], v200 offset:14336\n"
    "v_mul_f32 v141, v141, v197\n"
    "v_mfma_f32_16x16x32_bf16 v[168:171], v[92:95], a[232:235], v[168:171]\n"
    "ds_read_b64_tr_b16 v[70:71], v200 offset:15360\n"
    "v_mul_f32 v142, v142, v198\n"
    "v_mfma_f32_16x16x32_bf16 v[168:171], v[96:99], a[236:239], v[168:171]\n"
    "v_rcp_f32 v199, v199\n"
    "v_mfma_f32_16x16x32_bf16 v[168:171], v[100:103], a[240:243], v[168:171]\n"
    "ds_read_b64_tr_b16 v[72:73], v200 offset:16384\n"
    "v_mfma_f32_16x16x32_bf16 v[168:171], v[104:107], a[244:247], v[168:171]\n"
    "ds_read_b64_tr_b16 v[74:75], v200 offset:17408\n"
    "v_mul_f32 v143, v143, v199\n"
    "v_mfma_f32_16x16x32_bf16 v[168:171], v[108:111], a[248:251], v[168:171]\n"
    "v_mfma_f32_16x16x32_bf16 v[172:175], v[76:79], a[252:255], v[172:175]\n"
    "v_cvt_pk_bf16_f32 v188, v140, v141\n"
    "v_mfma_f32_16x16x32_bf16 v[172:175], v[80:83], v[8:11], v[172:175]\n"
    "v_cvt_pk_bf16_f32 v189, v142, v143\n"
    "v_mfma_f32_16x16x32_bf16 v[172:175], v[84:87], v[12:15], v[172:175]\n"
    "v_mfma_f32_16x16x32_bf16 v[172:175], v[88:91], v[16:19], v[172:175]\n"
    "v_mfma_f32_16x16x32_bf16 v[172:175], v[92:95], v[20:23], v[172:175]\n"
    "v_mfma_f32_16x16x32_bf16 v[172:175], v[96:99], v[24:27], v[172:175]\n"
    "v_mfma_f32_16x16x32_bf16 v[172:175], v[100:103], v[28:31], v[172:175]\n"
    "v_mfma_f32_16x16x32_bf16 v[172:175], v[104:107], v[32:35], v[172:175]\n"
    "v_mfma_f32_16x16x32_bf16 v[172:175], v[108:111], v[36:39], v[172:175]\n"
    "s_sub_u32 s71, s71, 1\n"
    "s_cmp_lg_u32 s71, 0\n"
    "s_cbranch_scc1 L_f_loop_%=\n"
    "s_lshr_b32 s96, s87, 1\n"
    "s_lshl_b32 s96, s96, 19\n"
    "s_add_u32 s56, s28, s96\n"
    "s_addc_u32 s57, s29, 0\n"
    "s_and_b32 s57, s57, 0xffff\n"
    "s_mov_b32 s58, 524288\n"
    "s_mov_b32 s59, 0x20000\n"
    "s_add_u32 s60, s30, s96\n"
    "s_addc_u32 s61, s31, 0\n"
    "s_and_b32 s61, s61, 0xffff\n"
    "s_mov_b32 s62, 524288\n"
    "s_mov_b32 s63, 0x20000\n"
    "v_and_b32 v2, 63, v1\n"
    "v_lshrrev_b32 v5, 4, v2\n"
    "v_and_b32 v4, 15, v2\n"
    "s_lshl_b32 s86, s39, 12\n"
    "v_lshlrev_b32 v7, 8, v5\n"
    "v_add_u32 v7, s86, v7\n"
    "v_or_b32 v6, 0, v5\n"
    "v_xor_b32 v6, v6, v4\n"
    "v_lshl_add_u32 v244, v6, 4, v7\n"
    "v_or_b32 v6, 4, v5\n"
    "v_xor_b32 v6, v6, v4\n"
    "v_lshl_add_u32 v245, v6, 4, v7\n"
    "v_or_b32 v6, 8, v5\n"
    "v_xor_b32 v6, v6, v4\n"
    "v_lshl_add_u32 v246, v6, 4, v7\n"
    "v_or_b32 v6, 12, v5\n"
    "v_xor_b32 v6, v6, v4\n"
    "v_lshl_add_u32 v247, v6, 4, v7\n"
    "v_or_b32 v6, 0, v5\n"
    "v_lshlrev_b32 v6, 1, v6\n"
    "v_xor_b32 v6, v6, v4\n"
    "v_lshl_add_u32 v248, v6, 4, v7\n"
    "v_or_b32 v6, 4, v5\n"
    "v_lshlrev_b32 v6, 1, v6\n"
    "v_xor_b32 v6, v6, v4\n"
    "v_lshl_add_u32 v249, v6, 4, v7\n"
    "s_add_u32 s98, s85, 73728\n"
    "s_add_u32 s84, s98, s86\n"
    "s_mov_b32 s82, 0\n"
    "s_mov_b32 s83, 0\n"
    "s_waitcnt lgkmcnt(0)\n"
    "v_mfma_f32_16x16x32_bf16 v[112:115], v[40:43], a[0:3], 0\n"
    "ds_read_b64_tr_b16 v[76:77], v200 offset:18432\n"
    "v_mul_f32 v192, v236, v144\n"
    "v_mfma_f32_16x16x32_bf16 v[112:115], v[44:47], a[4:7], v[112:115]\n"
    "ds_read_b64_tr_b16 v[78:79], v200 offset:19456\n"
    "v_mul_f32 v193, v236, v145\n"
    "v_mfma_f32_16x16x32_bf16 v[112:115], v[48:51], a[8:11], v[112:115]\n"
    "v_exp_f32 v192, v192\n"
    "v_mfma_f32_16x16x32_bf16 v[112:115], v[52:55], a[12:15], v[112:115]\n"
    "ds_read_b64_tr_b16 v[80:81], v200 offset:20480\n"
    "v_mul_f32 v194, v236, v146\n"
    "v_mfma_f32_16x16x32_bf16 v[112:115], v[56:59], a[16:19], v[112:115]\n"
    "ds_read_b64_tr_b16 v[82:83], v200 offset:21504\n"
    "v_mul_f32 v195, v236, v147\n"
    "v_mfma_f32_16x16x32_bf16 v[112:115], v[60:63], a[20:23], v[112:115]\n"
    "v_exp_f32 v193, v193\n"
    "v_mfma_f32_16x16x32_bf16 v[112:115], v[64:67], a[24:27], v[112:115]\n"
    "ds_read_b64_tr_b16 v[84:85], v200 offset:22528\n"
    "v_fma_f32 v192, v192, v240, v240\n"
    "v_mfma_f32_16x16x32_bf16 v[112:115], v[68:71], a[28:31], v[112:115]\n"
    "ds_read_b64_tr_b16 v[86:87], v200 offset:23552\n"
    "v_mul_f32 v160, v144, v160\n"
    "v_mfma_f32_16x16x32_bf16 v[112:115], v[72:75], a[32:35], v[112:115]\n"
    "v_exp_f32 v194, v194\n"
    "v_mfma_f32_16x16x32_bf16 v[116:119], v[40:43], a[36:39], 0\n"
    "ds_read_b64_tr_b16 v[88:89], v200 offset:24576\n"
    "v_fma_f32 v193, v193, v240, v240\n"
    "v_mfma_f32_16x16x32_bf16 v[116:119], v[44:47], a[40:43], v[116:119]\n"
    "ds_read_b64_tr_b16 v[90:91], v200 offset:25600\n"
    "v_mul_f32 v161, v145, v161\n"
    "v_mfma_f32_16x16x32_bf16 v[116:119], v[48:51], a[44:47], v[116:119]\n"
    "v_exp_f32 v195, v195\n"
    "v_mfma_f32_16x16x32_bf16 v[116:119], v[52:55], a[48:51], v[116:119]\n"
    "ds_read_b64_tr_b16 v[92:93], v200 offset:26624\n"
    "v_fma_f32 v194, v194, v240, v240\n"
    "v_mfma_f32_16x16x32_bf16 v[116:119], v[56:59], a[52:55], v[116:119]\n"
    "ds_read_b64_tr_b16 v[94:95], v200 offset:27648\n"
    "v_mul_f32 v162, v146, v162\n"
    "v_mfma_f32_16x16x32_bf16 v[116:119], v[60:63], a[56:59], v[116:119]\n"
    "v_rcp_f32 v192, v192\n"
    "v_mfma_f32_16x16x32_bf16 v[116:119], v[64:67], a[60:63], v[116:119]\n"
    "ds_read_b64_tr_b16 v[96:97], v200 offset:28672\n"
    "v_fma_f32 v195, v195, v240, v240\n"
    "v_mfma_f32_16x16x32_bf16 v[116:119], v[68:71], a[64:67], v[116:119]\n"
    "ds_read_b64_tr_b16 v[98:99], v200 offset:29696\n"
    "v_mul_f32 v163, v147, v163\n"
    "v_mfma_f32_16x16x32_bf16 v[116:119], v[72:75], a[68:71], v[116:119]\n"
    "v_rcp_f32 v193, v193\n"
    "v_mfma_f32_16x16x32_bf16 v[120:123], v[40:43], a[72:75], 0\n"
    "ds_read_b64_tr_b16 v[100:101], v200 offset:30720\n"
    "v_mul_f32 v160, v160, v192\n"
    "v_mfma_f32_16x16x32_bf16 v[120:123], v[44:47], a[76:79], v[120:123]\n"
    "ds_read_b64_tr_b16 v[102:103], v200 offset:31744\n"
    "v_mfma_f32_16x16x32_bf16 v[120:123], v[48:51], a[80:83], v[120:123]\n"
    "v_rcp_f32 v194, v194\n"
    "v_mfma_f32_16x16x32_bf16 v[120:123], v[52:55], a[84:87], v[120:123]\n"
    "ds_read_b64_tr_b16 v[104:105], v200 offset:32768\n"
    "v_mul_f32 v161, v161, v193\n"
    "v_mfma_f32_16x16x32_bf16 v[120:123], v[56:59], a[88:91], v[120:123]\n"
    "ds_read_b64_tr_b16 v[106:107], v200 offset:33792\n"
    "v_mul_f32 v162, v162, v194\n"
    "v_mfma_f32_16x16x32_bf16 v[120:123], v[60:63], a[92:95], v[120:123]\n"
    "v_rcp_f32 v195, v195\n"
    "v_mfma_f32_16x16x32_bf16 v[120:123], v[64:67], a[96:99], v[120:123]\n"
    "ds_read_b64_tr_b16 v[108:109], v200 offset:34816\n"
    "v_mfma_f32_16x16x32_bf16 v[120:123], v[68:71], a[100:103], v[120:123]\n"
    "s_mov_b32 s67, s65\n"
    "s_add_u32 m0, s64, 0\n"
    "ds_read_b64_tr_b16 v[110:111], v200 offset:35840\n"
    "v_mul_f32 v163, v163, v195\n"
    "v_mfma_f32_16x16x32_bf16 v[120:123], v[72:75], a[104:107], v[120:123]\n"
    "buffer_load_dwordx4 v250, s[40:43], s67 offen lds\n"
    "v_mfma_f32_16x16x32_bf16 v[124:127], v[40:43], a[108:111], 0\n"
    "s_add_u32 s67, s67, s68\n"
    "v_cvt_pk_bf16_f32 v178, v160, v161\n"
    "v_mfma_f32_16x16x32_bf16 v[124:127], v[44:47], a[112:115], v[124:127]\n"
    "s_add_u32 m0, s64, 4096\n"
    "v_cvt_pk_bf16_f32 v179, v162, v163\n"
    "v_mfma_f32_16x16x32_bf16 v[124:127], v[48:51], a[116:119], v[124:127]\n"
    "buffer_load_dwordx4 v250, s[40:43], s67 offen lds\n"
    "v_mfma_f32_16x16x32_bf16 v[124:127], v[52:55], a[120:123], v[124:127]\n"
    "s_add_u32 s67, s67, s68\n"
    "s_add_u32 m0, s64, 8192\n"
    "v_mfma_f32_16x16x32_bf16 v[124:127], v[56:59], a[124:127], v[124:127]\n"
    "buffer_load_dwordx4 v250, s[40:43], s67 offen lds\n"
    "v_mfma_f32_16x16x32_bf16 v[124:127], v[60:63], a[128:131], v[124:127]\n"
    "s_add_u32 s67, s67, s68\n"
    "s_add_u32 m0, s64, 12288\n"
    "v_mfma_f32_16x16x32_bf16 v[124:127], v[64:67], a[132:135], v[124:127]\n"
    "buffer_load_dwordx4 v250, s[40:43], s67 offen lds\n"
    "v_mfma_f32_16x16x32_bf16 v[124:127], v[68:71], a[136:139], v[124:127]\n"
    "s_add_u32 s67, s67, s68\n"
    "buffer_store_dwordx4 v[176:179], v252, s[48:51], s73 offen sc1\n"
    "v_mfma_f32_16x16x32_bf16 v[124:127], v[72:75], a[140:143], v[124:127]\n"
    "s_waitcnt lgkmcnt(0)\n"
    "v_mfma_f32_16x16x32_bf16 v[112:115], v[76:79], a[144:147], v[112:115]\n"
    "ds_read_b64_tr_b16 v[40:41], v201 offset:0\n"
    "v_mul_f32 v196, v237, v148\n"
    "v_mfma_f32_16x16x32_bf16 v[112:115], v[80:83], a[148:151], v[112:115]\n"
    "ds_read_b64_tr_b16 v[42:43], v201 offset:1024\n"
    "v_mul_f32 v197, v237, v149\n"
    "v_mfma_f32_16x16x32_bf16 v[112:115], v[84:87], a[152:155], v[112:115]\n"
    "v_exp_f32 v196, v196\n"
    "v_mfma_f32_16x16x32_bf16 v[112:115], v[88:91], a[156:159], v[112:115]\n"
    "ds_read_b64_tr_b16 v[44:45], v201 offset:2048\n"
    "v_mul_f32 v198, v237, v150\n"
    "v_mfma_f32_16x16x32_bf16 v[112:115], v[92:95], a[160:163], v[112:115]\n"
    "ds_read_b64_tr_b16 v[46:47], v201 offset:3072\n"
    "v_mul_f32 v199, v237, v151\n"
    "v_mfma_f32_16x16x32_bf16 v[112:115], v[96:99], a[164:167], v[112:115]\n"
    "v_exp_f32 v197, v197\n"
    "v_mfma_f32_16x16x32_bf16 v[112:115], v[100:103], a[168:171], v[112:115]\n"
    "ds_read_b64_tr_b16 v[48:49], v201 offset:4096\n"
    "v_fma_f32 v196, v196, v241, v241\n"
    "v_mfma_f32_16x16x32_bf16 v[112:115], v[104:107], a[172:175], v[112:115]\n"
    "ds_read_b64_tr_b16 v[50:51], v201 offset:5120\n"
    "v_mul_f32 v164, v148, v164\n"
    "v_mfma_f32_16x16x32_bf16 v[112:115], v[108:111], a[176:179], v[112:115]\n"
    "v_exp_f32 v198, v198\n"
    "v_mfma_f32_16x16x32_bf16 v[116:119], v[76:79], a[180:183], v[116:119]\n"
    "ds_read_b64_tr_b16 v[52:53], v201 offset:6144\n"
    "v_fma_f32 v197, v197, v241, v241\n"
    "v_mfma_f32_16x16x32_bf16 v[116:119], v[80:83], a[184:187], v[116:119]\n"
    "ds_read_b64_tr_b16 v[54:55], v201 offset:7168\n"
    "v_mul_f32 v165, v149, v165\n"
    "v_mfma_f32_16x16x32_bf16 v[116:119], v[84:87], a[188:191], v[116:119]\n"
    "v_exp_f32 v199, v199\n"
    "v_mfma_f32_16x16x32_bf16 v[116:119], v[88:91], a[192:195], v[116:119]\n"
    "ds_read_b64_tr_b16 v[56:57], v201 offset:8192\n"
    "v_fma_f32 v198, v198, v241, v241\n"
    "v_mfma_f32_16x16x32_bf16 v[116:119], v[92:95], a[196:199], v[116:119]\n"
    "ds_read_b64_tr_b16 v[58:59], v201 offset:9216\n"
    "v_mul_f32 v166, v150, v166\n"
    "v_mfma_f32_16x16x32_bf16 v[116:119], v[96:99], a[200:203], v[116:119]\n"
    "v_rcp_f32 v196, v196\n"
    "v_mfma_f32_16x16x32_bf16 v[116:119], v[100:103], a[204:207], v[116:119]\n"
    "ds_read_b64_tr_b16 v[60:61], v201 offset:10240\n"
    "v_fma_f32 v199, v199, v241, v241\n"
    "v_mfma_f32_16x16x32_bf16 v[116:119], v[104:107], a[208:211], v[116:119]\n"
    "ds_read_b64_tr_b16 v[62:63], v201 offset:11264\n"
    "v_mul_f32 v167, v151, v167\n"
    "v_mfma_f32_16x16x32_bf16 v[116:119], v[108:111], a[212:215], v[116:119]\n"
    "v_rcp_f32 v197, v197\n"
    "v_mfma_f32_16x16x32_bf16 v[120:123], v[76:79], a[216:219], v[120:123]\n"
    "ds_read_b64_tr_b16 v[64:65], v201 offset:12288\n"
    "v_mul_f32 v164, v164, v196\n"
    "v_mfma_f32_16x16x32_bf16 v[120:123], v[80:83], a[220:223], v[120:123]\n"
    "ds_read_b64_tr_b16 v[66:67], v201 offset:13312\n"
    "v_mfma_f32_16x16x32_bf16 v[120:123], v[84:87], a[224:227], v[120:123]\n"
    "v_rcp_f32 v198, v198\n"
    "v_mfma_f32_16x16x32_bf16 v[120:123], v[88:91], a[228:231], v[120:123]\n"
    "ds_read_b64_tr_b16 v[68:69], v201 offset:14336\n"
    "v_mul_f32 v165, v165, v197\n"
    "v_mfma_f32_16x16x32_bf16 v[120:123], v[92:95], a[232:235], v[120:123]\n"
    "ds_read_b64_tr_b16 v[70:71], v201 offset:15360\n"
    "v_mul_f32 v166, v166, v198\n"
    "v_mfma_f32_16x16x32_bf16 v[120:123], v[96:99], a[236:239], v[120:123]\n"
    "v_rcp_f32 v199, v199\n"
    "v_mfma_f32_16x16x32_bf16 v[120:123], v[100:103], a[240:243], v[120:123]\n"
    "ds_read_b64_tr_b16 v[72:73], v201 offset:16384\n"
    "v_mfma_f32_16x16x32_bf16 v[120:123], v[104:107], a[244:247], v[120:123]\n"
    "s_add_u32 m0, s64, 16384\n"
    "ds_read_b64_tr_b16 v[74:75], v201 offset:17408\n"
    "v_mul_f32 v167, v167, v199\n"
    "v_mfma_f32_16x16x32_bf16 v[120:123], v[108:111], a[248:251], v[120:123]\n"
    "buffer_load_dwordx4 v250, s[40:43], s67 offen lds\n"
    "v_mfma_f32_16x16x32_bf16 v[124:127], v[76:79], a[252:255], v[124:127]\n"
    "s_add_u32 s67, s67, s68\n"
    "v_cvt_pk_bf16_f32 v182, v164, v165\n"
    "v_mfma_f32_16x16x32_bf16 v[124:127], v[80:83], v[8:11], v[124:127]\n"
    "s_add_u32 m0, s64, 20480\n"
    "v_cvt_pk_bf16_f32 v183, v166, v167\n"
    "v_mfma_f32_16x16x32_bf16 v[124:127], v[84:87], v[12:15], v[124:127]\n"
    "buffer_load_dwordx4 v250, s[40:43], s67 offen lds\n"
    "v_mfma_f32_16x16x32_bf16 v[124:127], v[88:91], v[16:19], v[124:127]\n"
    "s_add_u32 s67, s67, s68\n"
    "s_add_u32 m0, s64, 24576\n"
    "v_mfma_f32_16x16x32_bf16 v[124:127], v[92:95], v[20:23], v[124:127]\n"
    "buffer_load_dwordx4 v250, s[40:43], s67 offen lds\n"
    "v_mfma_f32_16x16x32_bf16 v[124:127], v[96:99], v[24:27], v[124:127]\n"
    "s_add_u32 s67, s67, s68\n"
    "s_add_u32 m0, s64, 28672\n"
    "v_mfma_f32_16x16x32_bf16 v[124:127], v[100:103], v[28:31], v[124:127]\n"
    "buffer_load_dwordx4 v250, s[40:43], s67 offen lds\n"
    "v_mfma_f32_16x16x32_bf16 v[124:127], v[104:107], v[32:35], v[124:127]\n"
    "s_add_u32 s67, s67, s68\n"
    "buffer_store_dwordx4 v[180:183], v252, s[48:51], s74 offen sc1\n"
    "v_mfma_f32_16x16x32_bf16 v[124:127], v[108:111], v[36:39], v[124:127]\n"
    "s_waitcnt lgkmcnt(0)\n"
    "v_mfma_f32_16x16x32_bf16 v[128:131], v[40:43], a[0:3], 0\n"
    "ds_read_b64_tr_b16 v[76:77], v201 offset:18432\n"
    "v_mul_f32 v192, v238, v152\n"
    "v_mfma_f32_16x16x32_bf16 v[128:131], v[44:47], a[4:7], v[128:131]\n"
    "ds_read_b64_tr_b16 v[78:79], v201 offset:19456\n"
    "v_mul_f32 v193, v238, v153\n"
    "v_mfma_f32_16x16x32_bf16 v[128:131], v[48:51], a[8:11], v[128:131]\n"
    "v_exp_f32 v192, v192\n"
    "v_mfma_f32_16x16x32_bf16 v[128:131], v[52:55], a[12:15], v[128:131]\n"
    "ds_read_b64_tr_b16 v[80:81], v201 offset:20480\n"
    "v_mul_f32 v194, v238, v154\n"
    "v_mfma_f32_16x16x32_bf16 v[128:131], v[56:59], a[16:19], v[128:131]\n"
    "ds_read_b64_tr_b16 v[82:83], v201 offset:21504\n"
    "v_mul_f32 v195, v238, v155\n"
    "v_mfma_f32_16x16x32_bf16 v[128:131], v[60:63], a[20:23], v[128:131]\n"
    "v_exp_f32 v193, v193\n"
    "v_mfma_f32_16x16x32_bf16 v[128:131], v[64:67], a[24:27], v[128:131]\n"
    "ds_read_b64_tr_b16 v[84:85], v201 offset:22528\n"
    "v_fma_f32 v192, v192, v242, v242\n"
    "v_mfma_f32_16x16x32_bf16 v[128:131], v[68:71], a[28:31], v[128:131]\n"
    "ds_read_b64_tr_b16 v[86:87], v201 offset:23552\n"
    "v_mul_f32 v168, v152, v168\n"
    "v_mfma_f32_16x16x32_bf16 v[128:131], v[72:75], a[32:35], v[128:131]\n"
    "v_exp_f32 v194, v194\n"
    "v_mfma_f32_16x16x32_bf16 v[132:135], v[40:43], a[36:39], 0\n"
    "ds_read_b64_tr_b16 v[88:89], v201 offset:24576\n"
    "v_fma_f32 v193, v193, v242, v242\n"
    "v_mfma_f32_16x16x32_bf16 v[132:135], v[44:47], a[40:43], v[132:135]\n"
    "ds_read_b64_tr_b16 v[90:91], v201 offset:25600\n"
    "v_mul_f32 v169, v153, v169\n"
    "v_mfma_f32_16x16x32_bf16 v[132:135], v[48:51], a[44:47], v[132:135]\n"
    "v_exp_f32 v195, v195\n"
    "v_mfma_f32_16x16x32_bf16 v[132:135], v[52:55], a[48:51], v[132:135]\n"
    "ds_read_b64_tr_b16 v[92:93], v201 offset:26624\n"
    "v_fma_f32 v194, v194, v242, v242\n"
    "v_mfma_f32_16x16x32_bf16 v[132:135], v[56:59], a[52:55], v[132:135]\n"
    "ds_read_b64_tr_b16 v[94:95], v201 offset:27648\n"
    "v_mul_f32 v170, v154, v170\n"
    "v_mfma_f32_16x16x32_bf16 v[132:135], v[60:63], a[56:59], v[132:135]\n"
    "v_rcp_f32 v192, v192\n"
    "v_mfma_f32_16x16x32_bf16 v[132:135], v[64:67], a[60:63], v[132:135]\n"
    "ds_read_b64_tr_b16 v[96:97], v201 offset:28672\n"
    "v_fma_f32 v195, v195, v242, v242\n"
    "v_mfma_f32_16x16x32_bf16 v[132:135], v[68:71], a[64:67], v[132:135]\n"
    "ds_read_b64_tr_b16 v[98:99], v201 offset:29696\n"
    "v_mul_f32 v171, v155, v171\n"
    "v_mfma_f32_16x16x32_bf16 v[132:135], v[72:75], a[68:71], v[132:135]\n"
    "v_rcp_f32 v193, v193\n"
    "v_mfma_f32_16x16x32_bf16 v[136:139], v[40:43], a[72:75], 0\n"
    "ds_read_b64_tr_b16 v[100:101], v201 offset:30720\n"
    "v_mul_f32 v168, v168, v192\n"
    "v_mfma_f32_16x16x32_bf16 v[136:139], v[44:47], a[76:79], v[136:139]\n"
    "ds_read_b64_tr_b16 v[102:103], v201 offset:31744\n"
    "v_mul_f32 v112, v253, v112\n"
    "v_mfma_f32_16x16x32_bf16 v[136:139], v[48:51], a[80:83], v[136:139]\n"
    "v_rcp_f32 v194, v194\n"
    "v_mfma_f32_16x16x32_bf16 v[136:139], v[52:55], a[84:87], v[136:139]\n"
    "ds_read_b64_tr_b16 v[104:105], v201 offset:32768\n"
    "v_mul_f32 v169, v169, v193\n"
    "v_mfma_f32_16x16x32_bf16 v[136:139], v[56:59], a[88:91], v[136:139]\n"
    "ds_read_b64_tr_b16 v[106:107], v201 offset:33792\n"
    "v_mul_f32 v170, v170, v194\n"
    "v_mfma_f32_16x16x32_bf16 v[136:139], v[60:63], a[92:95], v[136:139]\n"
    "v_rcp_f32 v195, v195\n"
    "v_mfma_f32_16x16x32_bf16 v[136:139], v[64:67], a[96:99], v[136:139]\n"
    "ds_read_b64_tr_b16 v[108:109], v201 offset:34816\n"
    "v_mul_f32 v113, v253, v113\n"
    "v_mfma_f32_16x16x32_bf16 v[136:139], v[68:71], a[100:103], v[136:139]\n"
    "s_add_u32 m0, s64, 32768\n"
    "ds_read_b64_tr_b16 v[110:111], v201 offset:35840\n"
    "v_mul_f32 v171, v171, v195\n"
    "v_mfma_f32_16x16x32_bf16 v[136:139], v[72:75], a[104:107], v[136:139]\n"
    "buffer_load_dwordx4 v250, s[40:43], s67 offen lds\n"
    "v_mfma_f32_16x16x32_bf16 v[140:143], v[40:43], a[108:111], 0\n"
    "s_add_u32 s67, s67, s68\n"
    "v_cvt_pk_bf16_f32 v186, v168, v169\n"
    "v_mul_f32 v114, v253, v114\n"
    "v_mfma_f32_16x16x32_bf16 v[140:143], v[44:47], a[112:115], v[140:143]\n"
    "s_mov_b32 s67, s66\n"
    "s_add_u32 m0, s64, 36864\n"
    "v_cvt_pk_bf16_f32 v187, v170, v171\n"
    "v_mul_f32 v115, v253, v115\n"
    "v_mfma_f32_16x16x32_bf16 v[140:143], v[48:51], a[116:119], v[140:143]\n"
    "buffer_load_dwordx4 v250, s[44:47], s67 offen lds\n"
    "v_mfma_f32_16x16x32_bf16 v[140:143], v[52:55], a[120:123], v[140:143]\n"
    "s_add_u32 s67, s67, s68\n"
    "s_add_u32 m0, s64, 40960\n"
    "v_cvt_pk_bf16_f32 v204, v112, v113\n"
    "v_cvt_pk_bf16_f32 v205, v114, v115\n"
    "v_mfma_f32_16x16x32_bf16 v[140:143], v[56:59], a[124:127], v[140:143]\n"
    "buffer_load_dwordx4 v250, s[44:47], s67 offen lds\n"
    "v_mfma_f32_16x16x32_bf16 v[140:143], v[60:63], a[128:131], v[140:143]\n"
    "s_add_u32 s67, s67, s68\n"
    "s_add_u32 m0, s64, 45056\n"
    "v_mul_f32 v116, v255, v116\n"
    "v_mul_f32 v117, v255, v117\n"
    "v_mfma_f32_16x16x32_bf16 v[140:143], v[64:67], a[132:135], v[140:143]\n"
    "buffer_load_dwordx4 v250, s[44:47], s67 offen lds\n"
    "v_mfma_f32_16x16x32_bf16 v[140:143], v[68:71], a[136:139], v[140:143]\n"
    "s_add_u32 s67, s67, s68\n"
    "buffer_store_dwordx4 v[184:187], v252, s[48:51], s75 offen sc1\n"
    "v_mul_f32 v118, v255, v118\n"
    "v_mfma_f32_16x16x32_bf16 v[140:143], v[72:75], a[140:143], v[140:143]\n"
    "v_mul_f32 v119, v255, v119\n"
    "v_cvt_pk_bf16_f32 v208, v116, v117\n"
    "v_cvt_pk_bf16_f32 v209, v118, v119\n"
    "s_waitcnt lgkmcnt(0)\n"
    "v_mfma_f32_16x16x32_bf16 v[128:131], v[76:79], a[144:147], v[128:131]\n"
    "ds_read_b64_tr_b16 v[40:41], v202 offset:0\n"
    "v_mul_f32 v196, v239, v156\n"
    "v_mfma_f32_16x16x32_bf16 v[128:131], v[80:83], a[148:151], v[128:131]\n"
    "ds_read_b64_tr_b16 v[42:43], v202 offset:1024\n"
    "v_mul_f32 v197, v239, v157\n"
    "v_mfma_f32_16x16x32_bf16 v[128:131], v[84:87], a[152:155], v[128:131]\n"
    "v_exp_f32 v196, v196\n"
    "v_mfma_f32_16x16x32_bf16 v[128:131], v[88:91], a[156:159], v[128:131]\n"
    "ds_read_b64_tr_b16 v[44:45], v202 offset:2048\n"
    "v_mul_f32 v198, v239, v158\n"
    "v_mfma_f32_16x16x32_bf16 v[128:131], v[92:95], a[160:163], v[128:131]\n"
    "ds_read_b64_tr_b16 v[46:47], v202 offset:3072\n"
    "v_mul_f32 v199, v239, v159\n"
    "v_mfma_f32_16x16x32_bf16 v[128:131], v[96:99], a[164:167], v[128:131]\n"
    "v_exp_f32 v197, v197\n"
    "v_mfma_f32_16x16x32_bf16 v[128:131], v[100:103], a[168:171], v[128:131]\n"
    "ds_read_b64_tr_b16 v[48:49], v202 offset:4096\n"
    "v_fma_f32 v196, v196, v243, v243\n"
    "v_mfma_f32_16x16x32_bf16 v[128:131], v[104:107], a[172:175], v[128:131]\n"
    "ds_read_b64_tr_b16 v[50:51], v202 offset:5120\n"
    "v_mul_f32 v172, v156, v172\n"
    "v_mfma_f32_16x16x32_bf16 v[128:131], v[108:111], a[176:179], v[128:131]\n"
    "v_exp_f32 v198, v198\n"
    "v_mfma_f32_16x16x32_bf16 v[132:135], v[76:79], a[180:183], v[132:135]\n"
    "ds_read_b64_tr_b16 v[52:53], v202 offset:6144\n"
    "v_fma_f32 v197, v197, v243, v243\n"
    "v_mfma_f32_16x16x32_bf16 v[132:135], v[80:83], a[184:187], v[132:135]\n"
    "ds_read_b64_tr_b16 v[54:55], v202 offset:7168\n"
    "v_mul_f32 v173, v157, v173\n"
    "v_mfma_f32_16x16x32_bf16 v[132:135], v[84:87], a[188:191], v[132:135]\n"
    "v_exp_f32 v199, v199\n"
    "v_mfma_f32_16x16x32_bf16 v[132:135], v[88:91], a[192:195], v[132:135]\n"
    "ds_read_b64_tr_b16 v[56:57], v202 offset:8192\n"
    "v_fma_f32 v198, v198, v243, v243\n"
    "v_mfma_f32_16x16x32_bf16 v[132:135], v[92:95], a[196:199], v[132:135]\n"
    "ds_read_b64_tr_b16 v[58:59], v202 offset:9216\n"
    "v_mul_f32 v174, v158, v174\n"
    "v_mfma_f32_16x16x32_bf16 v[132:135], v[96:99], a[200:203], v[132:135]\n"
    "v_rcp_f32 v196, v196\n"
    "v_mfma_f32_16x16x32_bf16 v[132:135], v[100:103], a[204:207], v[132:135]\n"
    "ds_read_b64_tr_b16 v[60:61], v202 offset:10240\n"
    "v_fma_f32 v199, v199, v243, v243\n"
    "v_mfma_f32_16x16x32_bf16 v[132:135], v[104:107], a[208:211], v[132:135]\n"
    "ds_read_b64_tr_b16 v[62:63], v202 offset:11264\n"
    "v_mul_f32 v175, v159, v175\n"
    "v_mfma_f32_16x16x32_bf16 v[132:135], v[108:111], a[212:215], v[132:135]\n"
    "v_rcp_f32 v197, v197\n"
    "v_mfma_f32_16x16x32_bf16 v[136:139], v[76:79], a[216:219], v[136:139]\n"
    "ds_read_b64_tr_b16 v[64:65], v202 offset:12288\n"
    "v_mul_f32 v172, v172, v196\n"
    "v_mfma_f32_16x16x32_bf16 v[136:139], v[80:83], a[220:223], v[136:139]\n"
    "ds_read_b64_tr_b16 v[66:67], v202 offset:13312\n"
    "v_mul_f32 v120, v3, v120\n"
    "v_mfma_f32_16x16x32_bf16 v[136:139], v[84:87], a[224:227], v[136:139]\n"
    "v_rcp_f32 v198, v198\n"
    "v_mfma_f32_16x16x32_bf16 v[136:139], v[88:91], a[228:231], v[136:139]\n"
    "ds_read_b64_tr_b16 v[68:69], v202 offset:14336\n"
    "v_mul_f32 v173, v173, v197\n"
    "v_mfma_f32_16x16x32_bf16 v[136:139], v[92:95], a[232:235], v[136:139]\n"
    "ds_read_b64_tr_b16 v[70:71], v202 offset:15360\n"
    "v_mul_f32 v174, v174, v198\n"
    "v_mfma_f32_16x16x32_bf16 v[136:139], v[96:99], a[236:239], v[136:139]\n"
    "v_rcp_f32 v199, v199\n"
    "v_mfma_f32_16x16x32_bf16 v[136:139], v[100:103], a[240:243], v[136:139]\n"
    "ds_read_b64_tr_b16 v[72:73], v202 offset:16384\n"
    "v_mul_f32 v121, v3, v121\n"
    "v_mfma_f32_16x16x32_bf16 v[136:139], v[104:107], a[244:247], v[136:139]\n"
    "s_add_u32 m0, s64, 49152\n"
    "ds_read_b64_tr_b16 v[74:75], v202 offset:17408\n"
    "v_mul_f32 v175, v175, v199\n"
    "v_mfma_f32_16x16x32_bf16 v[136:139], v[108:111], a[248:251], v[136:139]\n"
    "buffer_load_dwordx4 v250, s[44:47], s67 offen lds\n"
    "v_mfma_f32_16x16x32_bf16 v[140:143], v[76:79], a[252:255], v[140:143]\n"
    "s_add_u32 s67, s67, s68\n"
    "v_cvt_pk_bf16_f32 v190, v172, v173\n"
    "v_mul_f32 v122, v3, v122\n"
    "v_mfma_f32_16x16x32_bf16 v[140:143], v[80:83], v[8:11], v[140:143]\n"
    "s_add_u32 m0, s64, 53248\n"
    "v_cvt_pk_bf16_f32 v191, v174, v175\n"
    "v_mul_f32 v123, v3, v123\n"
    "v_mfma_f32_16x16x32_bf16 v[140:143], v[84:87], v[12:15], v[140:143]\n"
    "buffer_load_dwordx4 v250, s[44:47], s67 offen lds\n"
    "v_mfma_f32_16x16x32_bf16 v[140:143], v[88:91], v[16:19], v[140:143]\n"
    "s_add_u32 s67, s67, s68\n"
    "s_add_u32 m0, s64, 57344\n"
    "v_cvt_pk_bf16_f32 v212, v120, v121\n"
    "v_cvt_pk_bf16_f32 v213, v122, v123\n"
    "v_mfma_f32_16x16x32_bf16 v[140:143], v[92:95], v[20:23], v[140:143]\n"
    "buffer_load_dwordx4 v250, s[44:47], s67 offen lds\n"
    "v_mfma_f32_16x16x32_bf16 v[140:143], v[96:99], v[24:27], v[140:143]\n"
    "s_add_u32 s67, s67, s68\n"
    "v_mul_f32 v124, v0, v124\n"
    "v_mul_f32 v125, v0, v125\n"
    "v_mfma_f32_16x16x32_bf16 v[140:143], v[100:103], v[28:31], v[140:143]\n"
    "v_mfma_f32_16x16x32_bf16 v[140:143], v[104:107], v[32:35], v[140:143]\n"
    "buffer_store_dwordx4 v[188:191], v252, s[48:51], s76 offen sc1\n"
    "v_mul_f32 v126, v0, v126\n"
    "v_mfma_f32_16x16x32_bf16 v[140:143], v[108:111], v[36:39], v[140:143]\n"
    "v_mul_f32 v127, v0, v127\n"
    "v_cvt_pk_bf16_f32 v216, v124, v125\n"
    "v_cvt_pk_bf16_f32 v217, v126, v127\n"
    "s_waitcnt lgkmcnt(0)\n"
    "v_mfma_f32_16x16x32_bf16 v[112:115], v[40:43], a[0:3], 0\n"
    "ds_read_b64_tr_b16 v[76:77], v202 offset:18432\n"
    "v_mul_f32 v128, v253, v128\n"
    "v_mfma_f32_16x16x32_bf16 v[112:115], v[44:47], a[4:7], v[112:115]\n"
    "ds_read_b64_tr_b16 v[78:79], v202 offset:19456\n"
    "v_mul_f32 v129, v253, v129\n"
    "v_mfma_f32_16x16x32_bf16 v[112:115], v[48:51], a[8:11], v[112:115]\n"
    "v_mfma_f32_16x16x32_bf16 v[112:115], v[52:55], a[12:15], v[112:115]\n"
    "ds_read_b64_tr_b16 v[80:81], v202 offset:20480\n"
    "v_mul_f32 v130, v253, v130\n"
    "v_mfma_f32_16x16x32_bf16 v[112:115], v[56:59], a[16:19], v[112:115]\n"
    "ds_read_b64_tr_b16 v[82:83], v202 offset:21504\n"
    "v_mul_f32 v131, v253, v131\n"
    "v_mfma_f32_16x16x32_bf16 v[112:115], v[60:63], a[20:23], v[112:115]\n"
    "v_mfma_f32_16x16x32_bf16 v[112:115], v[64:67], a[24:27], v[112:115]\n"
    "ds_read_b64_tr_b16 v[84:85], v202 offset:22528\n"
    "v_cvt_pk_bf16_f32 v206, v128, v129\n"
    "v_mfma_f32_16x16x32_bf16 v[112:115], v[68:71], a[28:31], v[112:115]\n"
    "ds_read_b64_tr_b16 v[86:87], v202 offset:23552\n"
    "v_cvt_pk_bf16_f32 v207, v130, v131\n"
    "v_mfma_f32_16x16x32_bf16 v[112:115], v[72:75], a[32:35], v[112:115]\n"
    "v_mfma_f32_16x16x32_bf16 v[116:119], v[40:43], a[36:39], 0\n"
    "ds_read_b64_tr_b16 v[88:89], v202 offset:24576\n"
    "v_mul_f32 v132, v255, v132\n"
    "v_mfma_f32_16x16x32_bf16 v[116:119], v[44:47], a[40:43], v[116:119]\n"
    "ds_read_b64_tr_b16 v[90:91], v202 offset:25600\n"
    "v_mul_f32 v133, v255, v133\n"
    "v_mfma_f32_16x16x32_bf16 v[116:119], v[48:51], a[44:47], v[116:119]\n"
    "v_mfma_f32_16x16x32_bf16 v[116:119], v[52:55], a[48:51], v[116:119]\n"
    "ds_read_b64_tr_b16 v[92:93], v202 offset:26624\n"
    "v_mul_f32 v134, v255, v134\n"
    "v_mfma_f32_16x16x32_bf16 v[116:119], v[56:59], a[52:55], v[116:119]\n"
    "ds_read_b64_tr_b16 v[94:95], v202 offset:27648\n"
    "v_mul_f32 v135, v255, v135\n"
    "v_mfma_f32_16x16x32_bf16 v[116:119], v[60:63], a[56:59], v[116:119]\n"
    "v_mfma_f32_16x16x32_bf16 v[116:119], v[64:67], a[60:63], v[116:119]\n"
    "ds_read_b64_tr_b16 v[96:97], v202 offset:28672\n"
    "v_cvt_pk_bf16_f32 v210, v132, v133\n"
    "v_mfma_f32_16x16x32_bf16 v[116:119], v[68:71], a[64:67], v[116:119]\n"
    "ds_read_b64_tr_b16 v[98:99], v202 offset:29696\n"
    "v_cvt_pk_bf16_f32 v211, v134, v135\n"
    "v_mfma_f32_16x16x32_bf16 v[116:119], v[72:75], a[68:71], v[116:119]\n"
    "v_mfma_f32_16x16x32_bf16 v[120:123], v[40:43], a[72:75], 0\n"
    "ds_read_b64_tr_b16 v[100:101], v202 offset:30720\n"
    "v_mfma_f32_16x16x32_bf16 v[120:123], v[44:47], a[76:79], v[120:123]\n"
    "ds_read_b64_tr_b16 v[102:103], v202 offset:31744\n"
    "v_mfma_f32_16x16x32_bf16 v[120:123], v[48:51], a[80:83], v[120:123]\n"
    "v_permlane32_swap_b32 v204, v206\n"
    "v_mfma_f32_16x16x32_bf16 v[120:123], v[52:55], a[84:87], v[120:123]\n"
    "ds_read_b64_tr_b16 v[104:105], v202 offset:32768\n"
    "v_mfma_f32_16x16x32_bf16 v[120:123], v[56:59], a[88:91], v[120:123]\n"
    "ds_read_b64_tr_b16 v[106:107], v202 offset:33792\n"
    "v_mfma_f32_16x16x32_bf16 v[120:123], v[60:63], a[92:95], v[120:123]\n"
    "v_permlane32_swap_b32 v205, v207\n"
    "v_mfma_f32_16x16x32_bf16 v[120:123], v[64:67], a[96:99], v[120:123]\n"
    "ds_read_b64_tr_b16 v[108:109], v202 offset:34816\n"
    "v_mfma_f32_16x16x32_bf16 v[120:123], v[68:71], a[100:103], v[120:123]\n"
    "s_add_u32 m0, s64, 61440\n"
    "ds_read_b64_tr_b16 v[110:111], v202 offset:35840\n"
    "v_mfma_f32_16x16x32_bf16 v[120:123], v[72:75], a[104:107], v[120:123]\n"
    "buffer_load_dwordx4 v250, s[44:47], s67 offen lds\n"
    "v_permlane32_swap_b32 v208, v210\n"
    "v_mfma_f32_16x16x32_bf16 v[124:127], v[40:43], a[108:111], 0\n"
    "s_add_u32 s67, s67, s68\n"
    "v_permlane32_swap_b32 v209, v211\n"
    "v_mfma_f32_16x16x32_bf16 v[124:127], v[44:47], a[112:115], v[124:127]\n"
    "s_add_u32 m0, s64, 65536\n"
    "v_permlane16_swap_b32 v204, v206\n"
    "v_mfma_f32_16x16x32_bf16 v[124:127], v[48:51], a[116:119], v[124:127]\n"
    "buffer_load_dwordx4 v250, s[44:47], s67 offen lds\n"
    "v_permlane16_swap_b32 v205, v207\n"
    "v_mfma_f32_16x16x32_bf16 v[124:127], v[52:55], a[120:123], v[124:127]\n"
    "s_add_u32 s67, s67, s68\n"
    "s_add_u32 m0, s64, 69632\n"
    "v_permlane16_swap_b32 v208, v210\n"
    "v_mfma_f32_16x16x32_bf16 v[124:127], v[56:59], a[124:127], v[124:127]\n"
    "buffer_load_dwordx4 v250, s[44:47], s67 offen lds\n"
    "v_permlane16_swap_b32 v209, v211\n"
    "v_mfma_f32_16x16x32_bf16 v[124:127], v[60:63], a[128:131], v[124:127]\n"
    "s_add_u32 s67, s67, s68\n"
    "v_mfma_f32_16x16x32_bf16 v[124:127], v[64:67], a[132:135], v[124:127]\n"
    "v_mfma_f32_16x16x32_bf16 v[124:127], v[68:71], a[136:139], v[124:127]\n"
    "v_mfma_f32_16x16x32_bf16 v[124:127], v[72:75], a[140:143], v[124:127]\n"
    "s_waitcnt lgkmcnt(0)\n"
    "v_mfma_f32_16x16x32_bf16 v[112:115], v[76:79], a[144:147], v[112:115]\n"
    "ds_read_b64_tr_b16 v[40:41], v203 offset:0\n"
    "v_mul_f32 v136, v3, v136\n"
    "v_mfma_f32_16x16x32_bf16 v[112:115], v[80:83], a[148:151], v[112:115]\n"
    "ds_read_b64_tr_b16 v[42:43], v203 offset:1024\n"
    "v_mul_f32 v137, v3, v137\n"
    "v_mfma_f32_16x16x32_bf16 v[112:115], v[84:87], a[152:155], v[112:115]\n"
    "s_and_b32 s96, s87, 1\n"
    "s_lshl_b32 s96, s96, 3\n"
    "s_add_u32 s96, s96, s88\n"
    "v_mfma_f32_16x16x32_bf16 v[112:115], v[88:91], a[156:159], v[112:115]\n"
    "ds_read_b64_tr_b16 v[44:45], v203 offset:2048\n"
    "v_mul_f32 v138, v3, v138\n"
    "v_mfma_f32_16x16x32_bf16 v[112:115], v[92:95], a[160:163], v[112:115]\n"
    "ds_read_b64_tr_b16 v[46:47], v203 offset:3072\n"
    "v_mul_f32 v139, v3, v139\n"
    "v_mfma_f32_16x16x32_bf16 v[112:115], v[96:99], a[164:167], v[112:115]\n"
    "s_lshl_b32 s96, s96, 2\n"
    "s_add_u32 s96, s96, s39\n"
    "s_lshl_b32 s96, s96, 11\n"
    "v_mfma_f32_16x16x32_bf16 v[112:115], v[100:103], a[168:171], v[112:115]\n"
    "ds_read_b64_tr_b16 v[48:49], v203 offset:4096\n"
    "v_cvt_pk_bf16_f32 v214, v136, v137\n"
    "v_mfma_f32_16x16x32_bf16 v[112:115], v[104:107], a[172:175], v[112:115]\n"
    "ds_read_b64_tr_b16 v[50:51], v203 offset:5120\n"
    "v_cvt_pk_bf16_f32 v215, v138, v139\n"
    "v_mfma_f32_16x16x32_bf16 v[112:115], v[108:111], a[176:179], v[112:115]\n"
    "v_mfma_f32_16x16x32_bf16 v[116:119], v[76:79], a[180:183], v[116:119]\n"
    "ds_read_b64_tr_b16 v[52:53], v203 offset:6144\n"
    "v_mul_f32 v140, v0, v140\n"
    "v_mfma_f32_16x16x32_bf16 v[116:119], v[80:83], a[184:187], v[116:119]\n"
    "ds_read_b64_tr_b16 v[54:55], v203 offset:7168\n"
    "v_mul_f32 v141, v0, v141\n"
    "v_mfma_f32_16x16x32_bf16 v[116:119], v[84:87], a[188:191], v[116:119]\n"
    "v_and_b32 v2, 15, v1\n"
    "v_lshlrev_b32 v2, 7, v2\n"
    "v_mfma_f32_16x16x32_bf16 v[116:119], v[88:91], a[192:195], v[116:119]\n"
    "ds_read_b64_tr_b16 v[56:57], v203 offset:8192\n"
    "v_mul_f32 v142, v0, v142\n"
    "v_mfma_f32_16x16x32_bf16 v[116:119], v[92:95], a[196:199], v[116:119]\n"
    "ds_read_b64_tr_b16 v[58:59], v203 offset:9216\n"
    "v_mul_f32 v143, v0, v143\n"
    "v_mfma_f32_16x16x32_bf16 v[116:119], v[96:99], a[200:203], v[116:119]\n"
    "s_add_u32 m0, s85, 163328\n"
    "s_nop 0\n"
    "buffer_load_dword v2, s[56:59], s96 offen lds\n"
    "v_mfma_f32_16x16x32_bf16 v[116:119], v[100:103], a[204:207], v[116:119]\n"
    "ds_read_b64_tr_b16 v[60:61], v203 offset:10240\n"
    "v_cvt_pk_bf16_f32 v218, v140, v141\n"
    "v_mfma_f32_16x16x32_bf16 v[116:119], v[104:107], a[208:211], v[116:119]\n"
    "ds_read_b64_tr_b16 v[62:63], v203 offset:11264\n"
    "v_cvt_pk_bf16_f32 v219, v142, v143\n"
    "v_mfma_f32_16x16x32_bf16 v[116:119], v[108:111], a[212:215], v[116:119]\n"
    "buffer_load_dword v2, s[60:63], s96 offen lds\n"
    "v_mfma_f32_16x16x32_bf16 v[120:123], v[76:79], a[216:219], v[120:123]\n"
    "ds_read_b64_tr_b16 v[64:65], v203 offset:12288\n"
    "v_mfma_f32_16x16x32_bf16 v[120:123], v[80:83], a[220:223], v[120:123]\n"
    "ds_read_b64_tr_b16 v[66:67], v203 offset:13312\n"
    "v_mfma_f32_16x16x32_bf16 v[120:123], v[84:87], a[224:227], v[120:123]\n"
    "v_permlane32_swap_b32 v212, v214\n"
    "v_mfma_f32_16x16x32_bf16 v[120:123], v[88:91], a[228:231], v[120:123]\n"
    "ds_read_b64_tr_b16 v[68:69], v203 offset:14336\n"
    "v_mfma_f32_16x16x32_bf16 v[120:123], v[92:95], a[232:235], v[120:123]\n"
    "ds_read_b64_tr_b16 v[70:71], v203 offset:15360\n"
    "v_mfma_f32_16x16x32_bf16 v[120:123], v[96:99], a[236:239], v[120:123]\n"
    "v_permlane32_swap_b32 v213, v215\n"
    "v_mfma_f32_16x16x32_bf16 v[120:123], v[100:103], a[240:243], v[120:123]\n"
    "ds_read_b64_tr_b16 v[72:73], v203 offset:16384\n"
    "v_mfma_f32_16x16x32_bf16 v[120:123], v[104:107], a[244:247], v[120:123]\n"
    "ds_read_b64_tr_b16 v[74:75], v203 offset:17408\n"
    "v_mfma_f32_16x16x32_bf16 v[120:123], v[108:111], a[248:251], v[120:123]\n"
    "v_permlane32_swap_b32 v216, v218\n"
    "v_mfma_f32_16x16x32_bf16 v[124:127], v[76:79], a[252:255], v[124:127]\n"
    "v_permlane32_swap_b32 v217, v219\n"
    "v_mfma_f32_16x16x32_bf16 v[124:127], v[80:83], v[8:11], v[124:127]\n"
    "v_permlane16_swap_b32 v212, v214\n"
    "v_mfma_f32_16x16x32_bf16 v[124:127], v[84:87], v[12:15], v[124:127]\n"
    "v_permlane16_swap_b32 v213, v215\n"
    "v_mfma_f32_16x16x32_bf16 v[124:127], v[88:91], v[16:19], v[124:127]\n"
    "v_permlane16_swap_b32 v216, v218\n"
    "v_mfma_f32_16x16x32_bf16 v[124:127], v[92:95], v[20:23], v[124:127]\n"
    "v_permlane16_swap_b32 v217, v219\n"
    "v_mfma_f32_16x16x32_bf16 v[124:127], v[96:99], v[24:27], v[124:127]\n"
    "v_mfma_f32_16x16x32_bf16 v[124:127], v[100:103], v[28:31], v[124:127]\n"
    "v_mfma_f32_16x16x32_bf16 v[124:127], v[104:107], v[32:35], v[124:127]\n"
    "v_mfma_f32_16x16x32_bf16 v[124:127], v[108:111], v[36:39], v[124:127]\n"
    "s_waitcnt lgkmcnt(0)\n"
    "v_mfma_f32_16x16x32_bf16 v[128:131], v[40:43], a[0:3], 0\n"
    "ds_read_b64_tr_b16 v[76:77], v203 offset:18432\n"
    "v_mul_f32 v112, v253, v112\n"
    "v_mfma_f32_16x16x32_bf16 v[128:131], v[44:47], a[4:7], v[128:131]\n"
    "ds_read_b64_tr_b16 v[78:79], v203 offset:19456\n"
    "v_mul_f32 v113, v253, v113\n"
    "v_mfma_f32_16x16x32_bf16 v[128:131], v[48:51], a[8:11], v[128:131]\n"
    "s_cmp_lt_u32 s38, 144\n"
    "s_cselect_b32 s64, s8, s10\n"
    "s_cselect_b32 s65, s9, s11\n"
    "v_and_b32 v2, 63, v1\n"
    "v_mfma_f32_16x16x32_bf16 v[128:131], v[52:55], a[12:15], v[128:131]\n"
    "ds_read_b64_tr_b16 v[80:81], v203 offset:20480\n"
    "v_mul_f32 v114, v253, v114\n"
    "v_mfma_f32_16x16x32_bf16 v[128:131], v[56:59], a[16:19], v[128:131]\n"
    "ds_read_b64_tr_b16 v[82:83], v203 offset:21504\n"
    "v_mul_f32 v115, v253, v115\n"
    "v_mfma_f32_16x16x32_bf16 v[128:131], v[60:63], a[20:23], v[128:131]\n"
    "s_cselect_b32 s96, 0, 144\n"
    "s_sub_u32 s96, s38, s96\n"
    "s_mul_i32 s96, s96, 36864\n"
    "v_and_b32 v4, 15, v2\n"
    "v_mfma_f32_16x16x32_bf16 v[128:131], v[64:67], a[24:27], v[128:131]\n"
    "ds_read_b64_tr_b16 v[84:85], v203 offset:22528\n"
    "v_cvt_pk_bf16_f32 v176, v112, v113\n"
    "v_mfma_f32_16x16x32_bf16 v[128:131], v[68:71], a[28:31], v[128:131]\n"
    "ds_read_b64_tr_b16 v[86:87], v203 offset:23552\n"
    "v_cvt_pk_bf16_f32 v177, v114, v115\n"
    "v_mfma_f32_16x16x32_bf16 v[128:131], v[72:75], a[32:35], v[128:131]\n"
    "s_add_u32 s64, s64, s96\n"
    "s_addc_u32 s65, s65, 0\n"
    "s_and_b32 s65, s65, 0xffff\n"
    "v_lshrrev_b32 v5, 4, v2\n"
    "v_mfma_f32_16x16x32_bf16 v[132:135], v[40:43], a[36:39], 0\n"
    "ds_read_b64_tr_b16 v[88:89], v203 offset:24576\n"
    "v_mul_f32 v116, v255, v116\n"
    "v_mfma_f32_16x16x32_bf16 v[132:135], v[44:47], a[40:43], v[132:135]\n"
    "ds_read_b64_tr_b16 v[90:91], v203 offset:25600\n"
    "v_mul_f32 v117, v255, v117\n"
    "v_mfma_f32_16x16x32_bf16 v[132:135], v[48:51], a[44:47], v[132:135]\n"
    "s_mov_b32 s66, 36864\n"
    "s_mov_b32 s67, 0x20000\n"
    "s_mul_i32 s96, s39, 9216\n"
    "v_or_b32 v6, 0, v5\n"
    "v_mfma_f32_16x16x32_bf16 v[132:135], v[52:55], a[48:51], v[132:135]\n"
    "ds_read_b64_tr_b16 v[92:93], v203 offset:26624\n"
    "v_mul_f32 v118, v255, v118\n"
    "v_mfma_f32_16x16x32_bf16 v[132:135], v[56:59], a[52:55], v[132:135]\n"
    "ds_read_b64_tr_b16 v[94:95], v203 offset:27648\n"
    "v_mul_f32 v119, v255, v119\n"
    "v_mfma_f32_16x16x32_bf16 v[132:135], v[60:63], a[56:59], v[132:135]\n"
    "v_and_b32 v220, 63, v1\n"
    "v_lshlrev_b32 v220, 7, v220\n"
    "v_xor_b32 v6, v6, v4\n"
    "v_mfma_f32_16x16x32_bf16 v[132:135], v[64:67], a[60:63], v[132:135]\n"
    "ds_read_b64_tr_b16 v[96:97], v203 offset:28672\n"
    "v_cvt_pk_bf16_f32 v180, v116, v117\n"
    "v_mfma_f32_16x16x32_bf16 v[132:135], v[68:71], a[64:67], v[132:135]\n"
    "ds_read_b64_tr_b16 v[98:99], v203 offset:29696\n"
    "v_cvt_pk_bf16_f32 v181, v118, v119\n"
    "v_mfma_f32_16x16x32_bf16 v[132:135], v[72:75], a[68:71], v[132:135]\n"
    "s_add_u32 m0, s85, 163328\n"
    "s_nop 0\n"
    "buffer_load_dword v220, s[64:67], s96 offen lds\n"
    "v_lshlrev_b32 v6, 4, v6\n"
    "v_mfma_f32_16x16x32_bf16 v[136:139], v[40:43], a[72:75], 0\n"
    "ds_read_b64_tr_b16 v[100:101], v203 offset:30720\n"
    "v_mfma_f32_16x16x32_bf16 v[136:139], v[44:47], a[76:79], v[136:139]\n"
    "ds_read_b64_tr_b16 v[102:103], v203 offset:31744\n"
    "v_mfma_f32_16x16x32_bf16 v[136:139], v[48:51], a[80:83], v[136:139]\n"
    "v_mfma_f32_16x16x32_bf16 v[136:139], v[52:55], a[84:87], v[136:139]\n"
    "ds_read_b64_tr_b16 v[104:105], v203 offset:32768\n"
    "v_mfma_f32_16x16x32_bf16 v[136:139], v[56:59], a[88:91], v[136:139]\n"
    "ds_read_b64_tr_b16 v[106:107], v203 offset:33792\n"
    "v_mfma_f32_16x16x32_bf16 v[136:139], v[60:63], a[92:95], v[136:139]\n"
    "v_mfma_f32_16x16x32_bf16 v[136:139], v[64:67], a[96:99], v[136:139]\n"
    "ds_read_b64_tr_b16 v[108:109], v203 offset:34816\n"
    "v_mfma_f32_16x16x32_bf16 v[136:139], v[68:71], a[100:103], v[136:139]\n"
    "ds_read_b64_tr_b16 v[110:111], v203 offset:35840\n"
    "v_mfma_f32_16x16x32_bf16 v[136:139], v[72:75], a[104:107], v[136:139]\n"
    "v_mfma_f32_16x16x32_bf16 v[140:143], v[40:43], a[108:111], 0\n"
    "v_mfma_f32_16x16x32_bf16 v[140:143], v[44:47], a[112:115], v[140:143]\n"
    "v_mfma_f32_16x16x32_bf16 v[140:143], v[48:51], a[116:119], v[140:143]\n"
    "v_mfma_f32_16x16x32_bf16 v[140:143], v[52:55], a[120:123], v[140:143]\n"
    "v_mfma_f32_16x16x32_bf16 v[140:143], v[56:59], a[124:127], v[140:143]\n"
    "v_mfma_f32_16x16x32_bf16 v[140:143], v[60:63], a[128:131], v[140:143]\n"
    "v_mfma_f32_16x16x32_bf16 v[140:143], v[64:67], a[132:135], v[140:143]\n"
    "v_lshl_add_u32 v232, v4, 8, v6\n"
    "v_mfma_f32_16x16x32_bf16 v[140:143], v[68:71], a[136:139], v[140:143]\n"
    "v_mfma_f32_16x16x32_bf16 v[140:143], v[72:75], a[140:143], v[140:143]\n"
    "s_waitcnt vmcnt(3) lgkmcnt(0)\n"
    "s_barrier\n"
    "v_add_u32 v200, s70, v200\n"
    "v_add_u32 v201, s70, v201\n"
    "v_add_u32 v202, s70, v202\n"
    "v_add_u32 v203, s70, v203\n"
    "s_sub_u32 s70, 0, s70\n"
    "v_mfma_f32_16x16x32_bf16 v[128:131], v[76:79], a[144:147], v[128:131]\n"
    "ds_read_b64_tr_b16 v[40:41], v200 offset:0\n"
    "v_mul_f32 v120, v3, v120\n"
    "v_mfma_f32_16x16x32_bf16 v[128:131], v[80:83], a[148:151], v[128:131]\n"
    "ds_read_b64_tr_b16 v[42:43], v200 offset:1024\n"
    "v_mul_f32 v121, v3, v121\n"
    "v_mfma_f32_16x16x32_bf16 v[128:131], v[84:87], a[152:155], v[128:131]\n"
    "v_add_u32 v232, s98, v232\n"
    "v_mfma_f32_16x16x32_bf16 v[128:131], v[88:91], a[156:159], v[128:131]\n"
    "ds_read_b64_tr_b16 v[44:45], v200 offset:2048\n"
    "v_mul_f32 v122, v3, v122\n"
    "v_mfma_f32_16x16x32_bf16 v[128:131], v[92:95], a[160:163], v[128:131]\n"
    "ds_read_b64_tr_b16 v[46:47], v200 offset:3072\n"
    "v_mul_f32 v123, v3, v123\n"
    "v_mfma_f32_16x16x32_bf16 v[128:131], v[96:99], a[164:167], v[128:131]\n"
    "v_or_b32 v6, 4, v5\n"
    "v_mfma_f32_16x16x32_bf16 v[128:131], v[100:103], a[168:171], v[128:131]\n"
    "ds_read_b64_tr_b16 v[48:49], v200 offset:4096\n"
    "v_cvt_pk_bf16_f32 v184, v120, v121\n"
    "v_mfma_f32_16x16x32_bf16 v[128:131], v[104:107], a[172:175], v[128:131]\n"
    "ds_read_b64_tr_b16 v[50:51], v200 offset:5120\n"
    "v_cvt_pk_bf16_f32 v185, v122, v123\n"
    "v_mfma_f32_16x16x32_bf16 v[128:131], v[108:111], a[176:179], v[128:131]\n"
    "v_xor_b32 v6, v6, v4\n"
    "v_mfma_f32_16x16x32_bf16 v[132:135], v[76:79], a[180:183], v[132:135]\n"
    "ds_read_b64_tr_b16 v[52:53], v200 offset:6144\n"
    "v_mul_f32 v124, v0, v124\n"
    "v_mfma_f32_16x16x32_bf16 v[132:135], v[80:83], a[184:187], v[132:135]\n"
    "ds_read_b64_tr_b16 v[54:55], v200 offset:7168\n"
    "v_mul_f32 v125, v0, v125\n"
    "v_mfma_f32_16x16x32_bf16 v[132:135], v[84:87], a[188:191], v[132:135]\n"
    "v_lshlrev_b32 v6, 4, v6\n"
    "v_mfma_f32_16x16x32_bf16 v[132:135], v[88:91], a[192:195], v[132:135]\n"
    "ds_read_b64_tr_b16 v[56:57], v200 offset:8192\n"
    "v_mul_f32 v126, v0, v126\n"
    "v_mfma_f32_16x16x32_bf16 v[132:135], v[92:95], a[196:199], v[132:135]\n"
    "ds_read_b64_tr_b16 v[58:59], v200 offset:9216\n"
    "v_mul_f32 v127, v0, v127\n"
    "v_mfma_f32_16x16x32_bf16 v[132:135], v[96:99], a[200:203], v[132:135]\n"
    "v_and_b32 v220, 7, v1\n"
    "v_lshlrev_b32 v220, 7, v220\n"
    "s_mul_i32 s96, s39, 9216\n"
    "v_lshl_add_u32 v233, v4, 8, v6\n"
    "v_mfma_f32_16x16x32_bf16 v[132:135], v[100:103], a[204:207], v[132:135]\n"
    "ds_read_b64_tr_b16 v[60:61], v200 offset:10240\n"
    "v_cvt_pk_bf16_f32 v188, v124, v125\n"
    "v_mfma_f32_16x16x32_bf16 v[132:135], v[104:107], a[208:211], v[132:135]\n"
    "ds_read_b64_tr_b16 v[62:63], v200 offset:11264\n"
    "v_cvt_pk_bf16_f32 v189, v126, v127\n"
    "v_mfma_f32_16x16x32_bf16 v[132:135], v[108:111], a[212:215], v[132:135]\n"
    "s_add_u32 m0, s85, 163328\n"
    "s_add_u32 s96, s96, 8192\n"
    "buffer_load_dword v220, s[64:67], s96 offen lds\n"
    "v_add_u32 v233, s98, v233\n"
    "v_mfma_f32_16x16x32_bf16 v[136:139], v[76:79], a[216:219], v[136:139]\n"
    "ds_read_b64_tr_b16 v[64:65], v200 offset:12288\n"
    "v_mfma_f32_16x16x32_bf16 v[136:139], v[80:83], a[220:223], v[136:139]\n"
    "ds_read_b64_tr_b16 v[66:67], v200 offset:13312\n"
    "v_mfma_f32_16x16x32_bf16 v[136:139], v[84:87], a[224:227], v[136:139]\n"
    "v_mfma_f32_16x16x32_bf16 v[136:139], v[88:91], a[228:231], v[136:139]\n"
    "ds_read_b64_tr_b16 v[68:69], v200 offset:14336\n"
    "v_mfma_f32_16x16x32_bf16 v[136:139], v[92:95], a[232:235], v[136:139]\n"
    "ds_read_b64_tr_b16 v[70:71], v200 offset:15360\n"
    "v_mfma_f32_16x16x32_bf16 v[136:139], v[96:99], a[236:239], v[136:139]\n"
    "v_mfma_f32_16x16x32_bf16 v[136:139], v[100:103], a[240:243], v[136:139]\n"
    "ds_read_b64_tr_b16 v[72:73], v200 offset:16384\n"
    "v_mfma_f32_16x16x32_bf16 v[136:139], v[104:107], a[244:247], v[136:139]\n"
    "ds_read_b64_tr_b16 v[74:75], v200 offset:17408\n"
    "v_mfma_f32_16x16x32_bf16 v[136:139], v[108:111], a[248:251], v[136:139]\n"
    "v_mfma_f32_16x16x32_bf16 v[140:143], v[76:79], a[252:255], v[140:143]\n"
    "v_mfma_f32_16x16x32_bf16 v[140:143], v[80:83], v[8:11], v[140:143]\n"
    "v_mfma_f32_16x16x32_bf16 v[140:143], v[84:87], v[12:15], v[140:143]\n"
    "v_mfma_f32_16x16x32_bf16 v[140:143], v[88:91], v[16:19], v[140:143]\n"
    "v_mfma_f32_16x16x32_bf16 v[140:143], v[92:95], v[20:23], v[140:143]\n"
    "v_mfma_f32_16x16x32_bf16 v[140:143], v[96:99], v[24:27], v[140:143]\n"
    "v_mfma_f32_16x16x32_bf16 v[140:143], v[100:103], v[28:31], v[140:143]\n"
    "v_mfma_f32_16x16x32_bf16 v[140:143], v[104:107], v[32:35], v[140:143]\n"
    "v_mfma_f32_16x16x32_bf16 v[140:143], v[108:111], v[36:39], v[140:143]\n"
    "s_waitcnt lgkmcnt(0)\n"
    "v_mfma_f32_16x16x32_bf16 v[112:115], v[40:43], a[0:3], 0\n"
    "ds_read_b64_tr_b16 v[76:77], v200 offset:18432\n"
    "v_mul_f32 v128, v253, v128\n"
    "v_mfma_f32_16x16x32_bf16 v[112:115], v[44:47], a[4:7], v[112:115]\n"
    "ds_read_b64_tr_b16 v[78:79], v200 offset:19456\n"
    "v_mul_f32 v129, v253, v129\n"
    "v_mfma_f32_16x16x32_bf16 v[112:115], v[48:51], a[8:11], v[112:115]\n"
    "v_or_b32 v6, 8, v5\n"
    "v_mfma_f32_16x16x32_bf16 v[112:115], v[52:55], a[12:15], v[112:115]\n"
    "ds_read_b64_tr_b16 v[80:81], v200 offset:20480\n"
    "v_mul_f32 v130, v253, v130\n"
    "v_mfma_f32_16x16x32_bf16 v[112:115], v[56:59], a[16:19], v[112:115]\n"
    "ds_read_b64_tr_b16 v[82:83], v200 offset:21504\n"
    "v_mul_f32 v131, v253, v131\n"
    "v_mfma_f32_16x16x32_bf16 v[112:115], v[60:63], a[20:23], v[112:115]\n"
    "v_xor_b32 v6, v6, v4\n"
    "v_mfma_f32_16x16x32_bf16 v[112:115], v[64:67], a[24:27], v[112:115]\n"
    "ds_read_b64_tr_b16 v[84:85], v200 offset:22528\n"
    "v_cvt_pk_bf16_f32 v178, v128, v129\n"
    "v_mfma_f32_16x16x32_bf16 v[112:115], v[68:71], a[28:31], v[112:115]\n"
    "ds_read_b64_tr_b16 v[86:87], v200 offset:23552\n"
    "v_cvt_pk_bf16_f32 v179, v130, v131\n"
    "v_mfma_f32_16x16x32_bf16 v[112:115], v[72:75], a[32:35], v[112:115]\n"
    "v_lshlrev_b32 v6, 4, v6\n"
    "v_mfma_f32_16x16x32_bf16 v[116:119], v[40:43], a[36:39], 0\n"
    "ds_read_b64_tr_b16 v[88:89], v200 offset:24576\n"
    "v_mul_f32 v132, v255, v132\n"
    "v_mfma_f32_16x16x32_bf16 v[116:119], v[44:47], a[40:43], v[116:119]\n"
    "ds_read_b64_tr_b16 v[90:91], v200 offset:25600\n"
    "v_mul_f32 v133, v255, v133\n"
    "v_mfma_f32_16x16x32_bf16 v[116:119], v[48:51], a[44:47], v[116:119]\n"
    "v_lshl_add_u32 v234, v4, 8, v6\n"
    "v_mfma_f32_16x16x32_bf16 v[116:119], v[52:55], a[48:51], v[116:119]\n"
    "ds_read_b64_tr_b16 v[92:93], v200 offset:26624\n"
    "v_mul_f32 v134, v255, v134\n"
    "v_mfma_f32_16x16x32_bf16 v[116:119], v[56:59], a[52:55], v[116:119]\n"
    "ds_read_b64_tr_b16 v[94:95], v200 offset:27648\n"
    "v_mul_f32 v135, v255, v135\n"
    "v_mfma_f32_16x16x32_bf16 v[116:119], v[60:63], a[56:59], v[116:119]\n"
    "v_add_u32 v234, s98, v234\n"
    "v_mfma_f32_16x16x32_bf16 v[116:119], v[64:67], a[60:63], v[116:119]\n"
    "ds_read_b64_tr_b16 v[96:97], v200 offset:28672\n"
    "v_cvt_pk_bf16_f32 v182, v132, v133\n"
    "v_mfma_f32_16x16x32_bf16 v[116:119], v[68:71], a[64:67], v[116:119]\n"
    "ds_read_b64_tr_b16 v[98:99], v200 offset:29696\n"
    "v_cvt_pk_bf16_f32 v183, v134, v135\n"
    "v_mfma_f32_16x16x32_bf16 v[116:119], v[72:75], a[68:71], v[116:119]\n"
    "v_or_b32 v6, 12, v5\n"
    "v_mfma_f32_16x16x32_bf16 v[120:123], v[40:43], a[72:75], 0\n"
    "ds_read_b64_tr_b16 v[100:101], v200 offset:30720\n"
    "v_mfma_f32_16x16x32_bf16 v[120:123], v[44:47], a[76:79], v[120:123]\n"
    "ds_read_b64_tr_b16 v[102:103], v200 offset:31744\n"
    "v_mfma_f32_16x16x32_bf16 v[120:123], v[48:51], a[80:83], v[120:123]\n"
    "v_permlane32_swap_b32 v176, v178\n"
    "v_mfma_f32_16x16x32_bf16 v[120:123], v[52:55], a[84:87], v[120:123]\n"
    "ds_read_b64_tr_b16 v[104:105], v200 offset:32768\n"
    "v_mfma_f32_16x16x32_bf16 v[120:123], v[56:59], a[88:91], v[120:123]\n"
    "ds_read_b64_tr_b16 v[106:107], v200 offset:33792\n"
    "v_mfma_f32_16x16x32_bf16 v[120:123], v[60:63], a[92:95], v[120:123]\n"
    "v_permlane32_swap_b32 v177, v179\n"
    "v_mfma_f32_16x16x32_bf16 v[120:123], v[64:67], a[96:99], v[120:123]\n"
    "ds_read_b64_tr_b16 v[108:109], v200 offset:34816\n"
    "v_mfma_f32_16x16x32_bf16 v[120:123], v[68:71], a[100:103], v[120:123]\n"
    "ds_read_b64_tr_b16 v[110:111], v200 offset:35840\n"
    "v_mfma_f32_16x16x32_bf16 v[120:123], v[72:75], a[104:107], v[120:123]\n"
    "v_permlane32_swap_b32 v180, v182\n"
    "v_mfma_f32_16x16x32_bf16 v[124:127], v[40:43], a[108:111], 0\n"
    "v_permlane32_swap_b32 v181, v183\n"
    "v_mfma_f32_16x16x32_bf16 v[124:127], v[44:47], a[112:115], v[124:127]\n"
    "v_permlane16_swap_b32 v176, v178\n"
    "v_mfma_f32_16x16x32_bf16 v[124:127], v[48:51], a[116:119], v[124:127]\n"
    "v_permlane16_swap_b32 v177, v179\n"
    "v_mfma_f32_16x16x32_bf16 v[124:127], v[52:55], a[120:123], v[124:127]\n"
    "v_permlane16_swap_b32 v180, v182\n"
    "v_mfma_f32_16x16x32_bf16 v[124:127], v[56:59], a[124:127], v[124:127]\n"
    "v_permlane16_swap_b32 v181, v183\n"
    "v_mfma_f32_16x16x32_bf16 v[124:127], v[60:63], a[128:131], v[124:127]\n"
    "v_mfma_f32_16x16x32_bf16 v[124:127], v[64:67], a[132:135], v[124:127]\n"
    "v_mfma_f32_16x16x32_bf16 v[124:127], v[68:71], a[136:139], v[124:127]\n"
    "v_mfma_f32_16x16x32_bf16 v[124:127], v[72:75], a[140:143], v[124:127]\n"
    "s_waitcnt lgkmcnt(0)\n"
    "v_mfma_f32_16x16x32_bf16 v[112:115], v[76:79], a[144:147], v[112:115]\n"
    "ds_read_b64_tr_b16 v[40:41], v201 offset:0\n"
    "v_mul_f32 v136, v3, v136\n"
    "v_mfma_f32_16x16x32_bf16 v[112:115], v[80:83], a[148:151], v[112:115]\n"
    "ds_read_b64_tr_b16 v[42:43], v201 offset:1024\n"
    "v_mul_f32 v137, v3, v137\n"
    "v_mfma_f32_16x16x32_bf16 v[112:115], v[84:87], a[152:155], v[112:115]\n"
    "v_xor_b32 v6, v6, v4\n"
    "v_mfma_f32_16x16x32_bf16 v[112:115], v[88:91], a[156:159], v[112:115]\n"
    "ds_read_b64_tr_b16 v[44:45], v201 offset:2048\n"
    "v_mul_f32 v138, v3, v138\n"
    "v_mfma_f32_16x16x32_bf16 v[112:115], v[92:95], a[160:163], v[112:115]\n"
    "ds_read_b64_tr_b16 v[46:47], v201 offset:3072\n"
    "v_mul_f32 v139, v3, v139\n"
    "v_mfma_f32_16x16x32_bf16 v[112:115], v[96:99], a[164:167], v[112:115]\n"
    "v_lshlrev_b32 v6, 4, v6\n"
    "v_mfma_f32_16x16x32_bf16 v[112:115], v[100:103], a[168:171], v[112:115]\n"
    "ds_read_b64_tr_b16 v[48:49], v201 offset:4096\n"
    "v_cvt_pk_bf16_f32 v186, v136, v137\n"
    "v_mfma_f32_16x16x32_bf16 v[112:115], v[104:107], a[172:175], v[112:115]\n"
    "ds_read_b64_tr_b16 v[50:51], v201 offset:5120\n"
    "v_cvt_pk_bf16_f32 v187, v138, v139\n"
    "v_mfma_f32_16x16x32_bf16 v[112:115], v[108:111], a[176:179], v[112:115]\n"
    "v_lshl_add_u32 v235, v4, 8, v6\n"
    "v_mfma_f32_16x16x32_bf16 v[116:119], v[76:79], a[180:183], v[116:119]\n"
    "ds_read_b64_tr_b16 v[52:53], v201 offset:6144\n"
    "v_mul_f32 v140, v0, v140\n"
    "v_mfma_f32_16x16x32_bf16 v[116:119], v[80:83], a[184:187], v[116:119]\n"
    "ds_read_b64_tr_b16 v[54:55], v201 offset:7168\n"
    "v_mul_f32 v141, v0, v141\n"
    "v_mfma_f32_16x16x32_bf16 v[116:119], v[84:87], a[188:191], v[116:119]\n"
    "v_add_u32 v235, s98, v235\n"
    "v_mfma_f32_16x16x32_bf16 v[116:119], v[88:91], a[192:195], v[116:119]\n"
    "ds_read_b64_tr_b16 v[56:57], v201 offset:8192\n"
    "v_mul_f32 v142, v0, v142\n"
    "v_mfma_f32_16x16x32_bf16 v[116:119], v[92:95], a[196:199], v[116:119]\n"
    "ds_read_b64_tr_b16 v[58:59], v201 offset:9216\n"
    "v_mul_f32 v143, v0, v143\n"
    "v_mfma_f32_16x16x32_bf16 v[116:119], v[96:99], a[200:203], v[116:119]\n"
    "v_lshrrev_b32 v4, 2, v2\n"
    "v_mfma_f32_16x16x32_bf16 v[116:119], v[100:103], a[204:207], v[116:119]\n"
    "ds_read_b64_tr_b16 v[60:61], v201 offset:10240\n"
    "v_cvt_pk_bf16_f32 v190, v140, v141\n"
    "v_mfma_f32_16x16x32_bf16 v[116:119], v[104:107], a[208:211], v[116:119]\n"
    "ds_read_b64_tr_b16 v[62:63], v201 offset:11264\n"
    "v_cvt_pk_bf16_f32 v191, v142, v143\n"
    "v_mfma_f32_16x16x32_bf16 v[116:119], v[108:111], a[212:215], v[116:119]\n"
    "v_and_b32 v6, 7, v4\n"
    "v_mfma_f32_16x16x32_bf16 v[120:123], v[76:79], a[216:219], v[120:123]\n"
    "ds_read_b64_tr_b16 v[64:65], v201 offset:12288\n"
    "v_mfma_f32_16x16x32_bf16 v[120:123], v[80:83], a[220:223], v[120:123]\n"
    "ds_read_b64_tr_b16 v[66:67], v201 offset:13312\n"
    "v_mfma_f32_16x16x32_bf16 v[120:123], v[84:87], a[224:227], v[120:123]\n"
    "v_permlane32_swap_b32 v184, v186\n"
    "v_mfma_f32_16x16x32_bf16 v[120:123], v[88:91], a[228:231], v[120:123]\n"
    "ds_read_b64_tr_b16 v[68:69], v201 offset:14336\n"
    "v_mfma_f32_16x16x32_bf16 v[120:123], v[92:95], a[232:235], v[120:123]\n"
    "ds_read_b64_tr_b16 v[70:71], v201 offset:15360\n"
    "v_mfma_f32_16x16x32_bf16 v[120:123], v[96:99], a[236:239], v[120:123]\n"
    "v_permlane32_swap_b32 v185, v187\n"
    "v_mfma_f32_16x16x32_bf16 v[120:123], v[100:103], a[240:243], v[120:123]\n"
    "ds_read_b64_tr_b16 v[72:73], v201 offset:16384\n"
    "v_mfma_f32_16x16x32_bf16 v[120:123], v[104:107], a[244:247], v[120:123]\n"
    "ds_read_b64_tr_b16 v[74:75], v201 offset:17408\n"
    "v_mfma_f32_16x16x32_bf16 v[120:123], v[108:111], a[248:251], v[120:123]\n"
    "s_add_u32 m0, s84, 0\n"
    "v_permlane32_swap_b32 v188, v190\n"
    "v_mfma_f32_16x16x32_bf16 v[124:127], v[76:79], a[252:255], v[124:127]\n"
    "buffer_load_dwordx4 v244, s[56:59], s82 offen lds\n"
    "v_permlane32_swap_b32 v189, v191\n"
    "v_mfma_f32_16x16x32_bf16 v[124:127], v[80:83], v[8:11], v[124:127]\n"
    "v_permlane16_swap_b32 v184, v186\n"
    "v_mfma_f32_16x16x32_bf16 v[124:127], v[84:87], v[12:15], v[124:127]\n"
    "buffer_load_dwordx4 v245, s[56:59], s82 offen offset:1024 lds\n"
    "v_permlane16_swap_b32 v185, v187\n"
    "v_mfma_f32_16x16x32_bf16 v[124:127], v[88:91], v[16:19], v[124:127]\n"
    "v_permlane16_swap_b32 v188, v190\n"
    "v_mfma_f32_16x16x32_bf16 v[124:127], v[92:95], v[20:23], v[124:127]\n"
    "buffer_load_dwordx4 v246, s[56:59], s82 offen offset:2048 lds\n"
    "v_permlane16_swap_b32 v189, v191\n"
    "v_mfma_f32_16x16x32_bf16 v[124:127], v[96:99], v[24:27], v[124:127]\n"
    "v_mfma_f32_16x16x32_bf16 v[124:127], v[100:103], v[28:31], v[124:127]\n"
    "buffer_load_dwordx4 v247, s[56:59], s82 offen offset:3072 lds\n"
    "v_mfma_f32_16x16x32_bf16 v[124:127], v[104:107], v[32:35], v[124:127]\n"
    "v_mfma_f32_16x16x32_bf16 v[124:127], v[108:111], v[36:39], v[124:127]\n"
    "s_add_u32 s82, s82, 16384\n"
    "s_waitcnt lgkmcnt(0)\n"
    "v_mfma_f32_16x16x32_bf16 v[128:131], v[40:43], a[0:3], 0\n"
    "ds_read_b64_tr_b16 v[76:77], v201 offset:18432\n"
    "v_mul_f32 v112, v253, v112\n"
    "v_mfma_f32_16x16x32_bf16 v[128:131], v[44:47], a[4:7], v[128:131]\n"
    "ds_read_b64_tr_b16 v[78:79], v201 offset:19456\n"
    "v_mul_f32 v113, v253, v113\n"
    "v_mfma_f32_16x16x32_bf16 v[128:131], v[48:51], a[8:11], v[128:131]\n"
    "v_and_b32 v7, 3, v2\n"
    "v_mfma_f32_16x16x32_bf16 v[128:131], v[52:55], a[12:15], v[128:131]\n"
    "ds_read_b64_tr_b16 v[80:81], v201 offset:20480\n"
    "v_mul_f32 v114, v253, v114\n"
    "v_mfma_f32_16x16x32_bf16 v[128:131], v[56:59], a[16:19], v[128:131]\n"
    "ds_read_b64_tr_b16 v[82:83], v201 offset:21504\n"
    "v_mul_f32 v115, v253, v115\n"
    "v_mfma_f32_16x16x32_bf16 v[128:131], v[60:63], a[20:23], v[128:131]\n"
    "v_lshlrev_b32 v7, 3, v7\n"
    "v_mfma_f32_16x16x32_bf16 v[128:131], v[64:67], a[24:27], v[128:131]\n"
    "ds_read_b64_tr_b16 v[84:85], v201 offset:22528\n"
    "v_cvt_pk_bf16_f32 v144, v112, v113\n"
    "v_mfma_f32_16x16x32_bf16 v[128:131], v[68:71], a[28:31], v[128:131]\n"
    "ds_read_b64_tr_b16 v[86:87], v201 offset:23552\n"
    "v_cvt_pk_bf16_f32 v145, v114, v115\n"
    "v_mfma_f32_16x16x32_bf16 v[128:131], v[72:75], a[32:35], v[128:131]\n"
    "v_lshl_add_u32 v7, v4, 8, v7\n"
    "v_mfma_f32_16x16x32_bf16 v[132:135], v[40:43], a[36:39], 0\n"
    "ds_read_b64_tr_b16 v[88:89], v201 offset:24576\n"
    "v_mul_f32 v116, v255, v116\n"
    "v_mfma_f32_16x16x32_bf16 v[132:135], v[44:47], a[40:43], v[132:135]\n"
    "ds_read_b64_tr_b16 v[90:91], v201 offset:25600\n"
    "v_mul_f32 v117, v255, v117\n"
    "v_mfma_f32_16x16x32_bf16 v[132:135], v[48:51], a[44:47], v[132:135]\n"
    "v_add_u32 v7, s98, v7\n"
    "v_mfma_f32_16x16x32_bf16 v[132:135], v[52:55], a[48:51], v[132:135]\n"
    "ds_read_b64_tr_b16 v[92:93], v201 offset:26624\n"
    "v_mul_f32 v118, v255, v118\n"
    "v_mfma_f32_16x16x32_bf16 v[132:135], v[56:59], a[52:55], v[132:135]\n"
    "ds_read_b64_tr_b16 v[94:95], v201 offset:27648\n"
    "v_mul_f32 v119, v255, v119\n"
    "v_mfma_f32_16x16x32_bf16 v[132:135], v[60:63], a[56:59], v[132:135]\n"
    "v_xor_b32 v4, 0, v6\n"
    "v_mfma_f32_16x16x32_bf16 v[132:135], v[64:67], a[60:63], v[132:135]\n"
    "ds_read_b64_tr_b16 v[96:97], v201 offset:28672\n"
    "v_cvt_pk_bf16_f32 v148, v116, v117\n"
    "v_mfma_f32_16x16x32_bf16 v[132:135], v[68:71], a[64:67], v[132:135]\n"
    "ds_read_b64_tr_b16 v[98:99], v201 offset:29696\n"
    "v_cvt_pk_bf16_f32 v149, v118, v119\n"
    "v_mfma_f32_16x16x32_bf16 v[132:135], v[72:75], a[68:71], v[132:135]\n"
    "v_lshl_add_u32 v236, v4, 5, v7\n"
    "v_mfma_f32_16x16x32_bf16 v[136:139], v[40:43], a[72:75], 0\n"
    "ds_read_b64_tr_b16 v[100:101], v201 offset:30720\n"
    "v_mfma_f32_16x16x32_bf16 v[136:139], v[44:47], a[76:79], v[136:139]\n"
    "ds_read_b64_tr_b16 v[102:103], v201 offset:31744\n"
    "v_mfma_f32_16x16x32_bf16 v[136:139], v[48:51], a[80:83], v[136:139]\n"
    "v_mfma_f32_16x16x32_bf16 v[136:139], v[52:55], a[84:87], v[136:139]\n"
    "ds_read_b64_tr_b16 v[104:105], v201 offset:32768\n"
    "v_mfma_f32_16x16x32_bf16 v[136:139], v[56:59], a[88:91], v[136:139]\n"
    "ds_read_b64_tr_b16 v[106:107], v201 offset:33792\n"
    "v_mfma_f32_16x16x32_bf16 v[136:139], v[60:63], a[92:95], v[136:139]\n"
    "v_mfma_f32_16x16x32_bf16 v[136:139], v[64:67], a[96:99], v[136:139]\n"
    "ds_read_b64_tr_b16 v[108:109], v201 offset:34816\n"
    "v_mfma_f32_16x16x32_bf16 v[136:139], v[68:71], a[100:103], v[136:139]\n"
    "ds_read_b64_tr_b16 v[110:111], v201 offset:35840\n"
    "v_mfma_f32_16x16x32_bf16 v[136:139], v[72:75], a[104:107], v[136:139]\n"
    "s_add_u32 m0, s84, 32768\n"
    "v_mfma_f32_16x16x32_bf16 v[140:143], v[40:43], a[108:111], 0\n"
    "buffer_load_dwordx4 v248, s[60:63], s83 offen lds\n"
    "v_mfma_f32_16x16x32_bf16 v[140:143], v[44:47], a[112:115], v[140:143]\n"
    "v_mfma_f32_16x16x32_bf16 v[140:143], v[48:51], a[116:119], v[140:143]\n"
    "buffer_load_dwordx4 v249, s[60:63], s83 offen offset:1024 lds\n"
    "v_mfma_f32_16x16x32_bf16 v[140:143], v[52:55], a[120:123], v[140:143]\n"
    "v_mfma_f32_16x16x32_bf16 v[140:143], v[56:59], a[124:127], v[140:143]\n"
    "buffer_load_dwordx4 v248, s[60:63], s83 offen offset:2048 lds\n"
    "v_mfma_f32_16x16x32_bf16 v[140:143], v[60:63], a[128:131], v[140:143]\n"
    "v_mfma_f32_16x16x32_bf16 v[140:143], v[64:67], a[132:135], v[140:143]\n"
    "buffer_load_dwordx4 v249, s[60:63], s83 offen offset:3072 lds\n"
    "v_mfma_f32_16x16x32_bf16 v[140:143], v[68:71], a[136:139], v[140:143]\n"
    "v_mfma_f32_16x16x32_bf16 v[140:143], v[72:75], a[140:143], v[140:143]\n"
    "s_add_u32 s83, s83, 16384\n"
    "s_waitcnt lgkmcnt(0)\n"
    "v_mfma_f32_16x16x32_bf16 v[128:131], v[76:79], a[144:147], v[128:131]\n"
    "ds_read_b64_tr_b16 v[40:41], v202 offset:0\n"
    "v_mul_f32 v120, v3, v120\n"
    "v_mfma_f32_16x16x32_bf16 v[128:131], v[80:83], a[148:151], v[128:131]\n"
    "ds_read_b64_tr_b16 v[42:43], v202 offset:1024\n"
    "v_mul_f32 v121, v3, v121\n"
    "v_mfma_f32_16x16x32_bf16 v[128:131], v[84:87], a[152:155], v[128:131]\n"
    "v_xor_b32 v4, 1, v6\n"
    "v_mfma_f32_16x16x32_bf16 v[128:131], v[88:91], a[156:159], v[128:131]\n"
    "ds_read_b64_tr_b16 v[44:45], v202 offset:2048\n"
    "v_mul_f32 v122, v3, v122\n"
    "v_mfma_f32_16x16x32_bf16 v[128:131], v[92:95], a[160:163], v[128:131]\n"
    "ds_read_b64_tr_b16 v[46:47], v202 offset:3072\n"
    "v_mul_f32 v123, v3, v123\n"
    "v_mfma_f32_16x16x32_bf16 v[128:131], v[96:99], a[164:167], v[128:131]\n"
    "v_lshl_add_u32 v237, v4, 5, v7\n"
    "v_mfma_f32_16x16x32_bf16 v[128:131], v[100:103], a[168:171], v[128:131]\n"
    "ds_read_b64_tr_b16 v[48:49], v202 offset:4096\n"
    "v_cvt_pk_bf16_f32 v152, v120, v121\n"
    "v_mfma_f32_16x16x32_bf16 v[128:131], v[104:107], a[172:175], v[128:131]\n"
    "ds_read_b64_tr_b16 v[50:51], v202 offset:5120\n"
    "v_cvt_pk_bf16_f32 v153, v122, v123\n"
    "v_mfma_f32_16x16x32_bf16 v[128:131], v[108:111], a[176:179], v[128:131]\n"
    "v_xor_b32 v4, 2, v6\n"
    "v_mfma_f32_16x16x32_bf16 v[132:135], v[76:79], a[180:183], v[132:135]\n"
    "ds_read_b64_tr_b16 v[52:53], v202 offset:6144\n"
    "v_mul_f32 v124, v0, v124\n"
    "v_mfma_f32_16x16x32_bf16 v[132:135], v[80:83], a[184:187], v[132:135]\n"
    "ds_read_b64_tr_b16 v[54:55], v202 offset:7168\n"
    "v_mul_f32 v125, v0, v125\n"
    "v_mfma_f32_16x16x32_bf16 v[132:135], v[84:87], a[188:191], v[132:135]\n"
    "v_lshl_add_u32 v238, v4, 5, v7\n"
    "v_mfma_f32_16x16x32_bf16 v[132:135], v[88:91], a[192:195], v[132:135]\n"
    "ds_read_b64_tr_b16 v[56:57], v202 offset:8192\n"
    "v_mul_f32 v126, v0, v126\n"
    "v_mfma_f32_16x16x32_bf16 v[132:135], v[92:95], a[196:199], v[132:135]\n"
    "ds_read_b64_tr_b16 v[58:59], v202 offset:9216\n"
    "v_mul_f32 v127, v0, v127\n"
    "v_mfma_f32_16x16x32_bf16 v[132:135], v[96:99], a[200:203], v[132:135]\n"
    "v_xor_b32 v4, 3, v6\n"
    "v_mfma_f32_16x16x32_bf16 v[132:135], v[100:103], a[204:207], v[132:135]\n"
    "ds_read_b64_tr_b16 v[60:61], v202 offset:10240\n"
    "v_cvt_pk_bf16_f32 v156, v124, v125\n"
    "v_mfma_f32_16x16x32_bf16 v[132:135], v[104:107], a[208:211], v[132:135]\n"
    "ds_read_b64_tr_b16 v[62:63], v202 offset:11264\n"
    "v_cvt_pk_bf16_f32 v157, v126, v127\n"
    "v_mfma_f32_16x16x32_bf16 v[132:135], v[108:111], a[212:215], v[132:135]\n"
    "v_lshl_add_u32 v239, v4, 5, v7\n"
    "v_mfma_f32_16x16x32_bf16 v[136:139], v[76:79], a[216:219], v[136:139]\n"
    "ds_read_b64_tr_b16 v[64:65], v202 offset:12288\n"
    "v_mfma_f32_16x16x32_bf16 v[136:139], v[80:83], a[220:223], v[136:139]\n"
    "ds_read_b64_tr_b16 v[66:67], v202 offset:13312\n"
    "v_mfma_f32_16x16x32_bf16 v[136:139], v[84:87], a[224:227], v[136:139]\n"
    "v_mfma_f32_16x16x32_bf16 v[136:139], v[88:91], a[228:231], v[136:139]\n"
    "ds_read_b64_tr_b16 v[68:69], v202 offset:14336\n"
    "v_mfma_f32_16x16x32_bf16 v[136:139], v[92:95], a[232:235], v[136:139]\n"
    "ds_read_b64_tr_b16 v[70:71], v202 offset:15360\n"
    "v_mfma_f32_16x16x32_bf16 v[136:139], v[96:99], a[236:239], v[136:139]\n"
    "v_mfma_f32_16x16x32_bf16 v[136:139], v[100:103], a[240:243], v[136:139]\n"
    "ds_read_b64_tr_b16 v[72:73], v202 offset:16384\n"
    "v_mfma_f32_16x16x32_bf16 v[136:139], v[104:107], a[244:247], v[136:139]\n"
    "ds_read_b64_tr_b16 v[74:75], v202 offset:17408\n"
    "v_mfma_f32_16x16x32_bf16 v[136:139], v[108:111], a[248:251], v[136:139]\n"
    "s_add_u32 m0, s84, 16384\n"
    "v_mfma_f32_16x16x32_bf16 v[140:143], v[76:79], a[252:255], v[140:143]\n"
    "buffer_load_dwordx4 v244, s[56:59], s82 offen lds\n"
    "v_mfma_f32_16x16x32_bf16 v[140:143], v[80:83], v[8:11], v[140:143]\n"
    "v_mfma_f32_16x16x32_bf16 v[140:143], v[84:87], v[12:15], v[140:143]\n"
    "buffer_load_dwordx4 v245, s[56:59], s82 offen offset:1024 lds\n"
    "v_mfma_f32_16x16x32_bf16 v[140:143], v[88:91], v[16:19], v[140:143]\n"
    "v_mfma_f32_16x16x32_bf16 v[140:143], v[92:95], v[20:23], v[140:143]\n"
    "buffer_load_dwordx4 v246, s[56:59], s82 offen offset:2048 lds\n"
    "v_mfma_f32_16x16x32_bf16 v[140:143], v[96:99], v[24:27], v[140:143]\n"
    "v_mfma_f32_16x16x32_bf16 v[140:143], v[100:103], v[28:31], v[140:143]\n"
    "buffer_load_dwordx4 v247, s[56:59], s82 offen offset:3072 lds\n"
    "v_mfma_f32_16x16x32_bf16 v[140:143], v[104:107], v[32:35], v[140:143]\n"
    "v_mfma_f32_16x16x32_bf16 v[140:143], v[108:111], v[36:39], v[140:143]\n"
    "s_add_u32 s82, s82, 16384\n"
    "s_waitcnt lgkmcnt(0)\n"
    "v_mfma_f32_16x16x32_bf16 v[112:115], v[40:43], a[0:3], 0\n"
    "ds_read_b64_tr_b16 v[76:77], v202 offset:18432\n"
    "v_mul_f32 v128, v253, v128\n"
    "v_mfma_f32_16x16x32_bf16 v[112:115], v[44:47], a[4:7], v[112:115]\n"
    "ds_read_b64_tr_b16 v[78:79], v202 offset:19456\n"
    "v_mul_f32 v129, v253, v129\n"
    "v_mfma_f32_16x16x32_bf16 v[112:115], v[48:51], a[8:11], v[112:115]\n"
    "v_xor_b32 v4, 4, v6\n"
    "v_mfma_f32_16x16x32_bf16 v[112:115], v[52:55], a[12:15], v[112:115]\n"
    "ds_read_b64_tr_b16 v[80:81], v202 offset:20480\n"
    "v_mul_f32 v130, v253, v130\n"
    "v_mfma_f32_16x16x32_bf16 v[112:115], v[56:59], a[16:19], v[112:115]\n"
    "ds_read_b64_tr_b16 v[82:83], v202 offset:21504\n"
    "v_mul_f32 v131, v253, v131\n"
    "v_mfma_f32_16x16x32_bf16 v[112:115], v[60:63], a[20:23], v[112:115]\n"
    "v_lshl_add_u32 v240, v4, 5, v7\n"
    "v_mfma_f32_16x16x32_bf16 v[112:115], v[64:67], a[24:27], v[112:115]\n"
    "ds_read_b64_tr_b16 v[84:85], v202 offset:22528\n"
    "v_cvt_pk_bf16_f32 v146, v128, v129\n"
    "v_mfma_f32_16x16x32_bf16 v[112:115], v[68:71], a[28:31], v[112:115]\n"
    "ds_read_b64_tr_b16 v[86:87], v202 offset:23552\n"
    "v_cvt_pk_bf16_f32 v147, v130, v131\n"
    "v_mfma_f32_16x16x32_bf16 v[112:115], v[72:75], a[32:35], v[112:115]\n"
    "v_xor_b32 v4, 5, v6\n"
    "v_mfma_f32_16x16x32_bf16 v[116:119], v[40:43], a[36:39], 0\n"
    "ds_read_b64_tr_b16 v[88:89], v202 offset:24576\n"
    "v_mul_f32 v132, v255, v132\n"
    "v_mfma_f32_16x16x32_bf16 v[116:119], v[44:47], a[40:43], v[116:119]\n"
    "ds_read_b64_tr_b16 v[90:91], v202 offset:25600\n"
    "v_mul_f32 v133, v255, v133\n"
    "v_mfma_f32_16x16x32_bf16 v[116:119], v[48:51], a[44:47], v[116:119]\n"
    "v_lshl_add_u32 v241, v4, 5, v7\n"
    "v_mfma_f32_16x16x32_bf16 v[116:119], v[52:55], a[48:51], v[116:119]\n"
    "ds_read_b64_tr_b16 v[92:93], v202 offset:26624\n"
    "v_mul_f32 v134, v255, v134\n"
    "v_mfma_f32_16x16x32_bf16 v[116:119], v[56:59], a[52:55], v[116:119]\n"
    "ds_read_b64_tr_b16 v[94:95], v202 offset:27648\n"
    "v_mul_f32 v135, v255, v135\n"
    "v_mfma_f32_16x16x32_bf16 v[116:119], v[60:63], a[56:59], v[116:119]\n"
    "v_xor_b32 v4, 6, v6\n"
    "v_mfma_f32_16x16x32_bf16 v[116:119], v[64:67], a[60:63], v[116:119]\n"
    "ds_read_b64_tr_b16 v[96:97], v202 offset:28672\n"
    "v_cvt_pk_bf16_f32 v150, v132, v133\n"
    "v_mfma_f32_16x16x32_bf16 v[116:119], v[68:71], a[64:67], v[116:119]\n"
    "ds_read_b64_tr_b16 v[98:99], v202 offset:29696\n"
    "v_cvt_pk_bf16_f32 v151, v134, v135\n"
    "v_mfma_f32_16x16x32_bf16 v[116:119], v[72:75], a[68:71], v[116:119]\n"
    "v_lshl_add_u32 v242, v4, 5, v7\n"
    "v_mfma_f32_16x16x32_bf16 v[120:123], v[40:43], a[72:75], 0\n"
    "ds_read_b64_tr_b16 v[100:101], v202 offset:30720\n"
    "v_mfma_f32_16x16x32_bf16 v[120:123], v[44:47], a[76:79], v[120:123]\n"
    "ds_read_b64_tr_b16 v[102:103], v202 offset:31744\n"
    "v_mfma_f32_16x16x32_bf16 v[120:123], v[48:51], a[80:83], v[120:123]\n"
    "v_permlane32_swap_b32 v144, v146\n"
    "v_mfma_f32_16x16x32_bf16 v[120:123], v[52:55], a[84:87], v[120:123]\n"
    "ds_read_b64_tr_b16 v[104:105], v202 offset:32768\n"
    "v_mfma_f32_16x16x32_bf16 v[120:123], v[56:59], a[88:91], v[120:123]\n"
    "ds_read_b64_tr_b16 v[106:107], v202 offset:33792\n"
    "v_mfma_f32_16x16x32_bf16 v[120:123], v[60:63], a[92:95], v[120:123]\n"
    "v_permlane32_swap_b32 v145, v147\n"
    "v_mfma_f32_16x16x32_bf16 v[120:123], v[64:67], a[96:99], v[120:123]\n"
    "ds_read_b64_tr_b16 v[108:109], v202 offset:34816\n"
    "v_mfma_f32_16x16x32_bf16 v[120:123], v[68:71], a[100:103], v[120:123]\n"
    "ds_read_b64_tr_b16 v[110:111], v202 offset:35840\n"
    "v_mfma_f32_16x16x32_bf16 v[120:123], v[72:75], a[104:107], v[120:123]\n"
    "v_permlane32_swap_b32 v148, v150\n"
    "v_mfma_f32_16x16x32_bf16 v[124:127], v[40:43], a[108:111], 0\n"
    "v_permlane32_swap_b32 v149, v151\n"
    "v_mfma_f32_16x16x32_bf16 v[124:127], v[44:47], a[112:115], v[124:127]\n"
    "v_permlane16_swap_b32 v144, v146\n"
    "v_mfma_f32_16x16x32_bf16 v[124:127], v[48:51], a[116:119], v[124:127]\n"
    "v_permlane16_swap_b32 v145, v147\n"
    "v_mfma_f32_16x16x32_bf16 v[124:127], v[52:55], a[120:123], v[124:127]\n"
    "v_permlane16_swap_b32 v148, v150\n"
    "v_mfma_f32_16x16x32_bf16 v[124:127], v[56:59], a[124:127], v[124:127]\n"
    "v_permlane16_swap_b32 v149, v151\n"
    "v_mfma_f32_16x16x32_bf16 v[124:127], v[60:63], a[128:131], v[124:127]\n"
    "v_mfma_f32_16x16x32_bf16 v[124:127], v[64:67], a[132:135], v[124:127]\n"
    "v_mfma_f32_16x16x32_bf16 v[124:127], v[68:71], a[136:139], v[124:127]\n"
    "v_mfma_f32_16x16x32_bf16 v[124:127], v[72:75], a[140:143], v[124:127]\n"
    "s_waitcnt lgkmcnt(0)\n"
    "v_mfma_f32_16x16x32_bf16 v[112:115], v[76:79], a[144:147], v[112:115]\n"
    "ds_read_b64_tr_b16 v[40:41], v203 offset:0\n"
    "v_mul_f32 v136, v3, v136\n"
    "v_mfma_f32_16x16x32_bf16 v[112:115], v[80:83], a[148:151], v[112:115]\n"
    "ds_read_b64_tr_b16 v[42:43], v203 offset:1024\n"
    "v_mul_f32 v137, v3, v137\n"
    "v_mfma_f32_16x16x32_bf16 v[112:115], v[84:87], a[152:155], v[112:115]\n"
    "v_xor_b32 v4, 7, v6\n"
    "v_mfma_f32_16x16x32_bf16 v[112:115], v[88:91], a[156:159], v[112:115]\n"
    "ds_read_b64_tr_b16 v[44:45], v203 offset:2048\n"
    "v_mul_f32 v138, v3, v138\n"
    "v_mfma_f32_16x16x32_bf16 v[112:115], v[92:95], a[160:163], v[112:115]\n"
    "ds_read_b64_tr_b16 v[46:47], v203 offset:3072\n"
    "v_mul_f32 v139, v3, v139\n"
    "v_mfma_f32_16x16x32_bf16 v[112:115], v[96:99], a[164:167], v[112:115]\n"
    "v_lshl_add_u32 v243, v4, 5, v7\n"
    "v_mfma_f32_16x16x32_bf16 v[112:115], v[100:103], a[168:171], v[112:115]\n"
    "ds_read_b64_tr_b16 v[48:49], v203 offset:4096\n"
    "v_cvt_pk_bf16_f32 v154, v136, v137\n"
    "v_mfma_f32_16x16x32_bf16 v[112:115], v[104:107], a[172:175], v[112:115]\n"
    "ds_read_b64_tr_b16 v[50:51], v203 offset:5120\n"
    "v_cvt_pk_bf16_f32 v155, v138, v139\n"
    "v_mfma_f32_16x16x32_bf16 v[112:115], v[108:111], a[176:179], v[112:115]\n"
    "v_mov_b32 v4, 0\n"
    "v_mfma_f32_16x16x32_bf16 v[116:119], v[76:79], a[180:183], v[116:119]\n"
    "ds_read_b64_tr_b16 v[52:53], v203 offset:6144\n"
    "v_mul_f32 v140, v0, v140\n"
    "v_mfma_f32_16x16x32_bf16 v[116:119], v[80:83], a[184:187], v[116:119]\n"
    "ds_read_b64_tr_b16 v[54:55], v203 offset:7168\n"
    "v_mul_f32 v141, v0, v141\n"
    "v_mfma_f32_16x16x32_bf16 v[116:119], v[84:87], a[188:191], v[116:119]\n"
    "v_mov_b32 v5, s85\n"
    "v_mfma_f32_16x16x32_bf16 v[116:119], v[88:91], a[192:195], v[116:119]\n"
    "ds_read_b64_tr_b16 v[56:57], v203 offset:8192\n"
    "v_mul_f32 v142, v0, v142\n"
    "v_mfma_f32_16x16x32_bf16 v[116:119], v[92:95], a[196:199], v[116:119]\n"
    "ds_read_b64_tr_b16 v[58:59], v203 offset:9216\n"
    "v_mul_f32 v143, v0, v143\n"
    "v_mfma_f32_16x16x32_bf16 v[116:119], v[96:99], a[200:203], v[116:119]\n"
    "v_add_u32 v5, 162816, v5\n"
    "v_mfma_f32_16x16x32_bf16 v[116:119], v[100:103], a[204:207], v[116:119]\n"
    "ds_read_b64_tr_b16 v[60:61], v203 offset:10240\n"
    "v_cvt_pk_bf16_f32 v158, v140, v141\n"
    "v_mfma_f32_16x16x32_bf16 v[116:119], v[104:107], a[208:211], v[116:119]\n"
    "ds_read_b64_tr_b16 v[62:63], v203 offset:11264\n"
    "v_cvt_pk_bf16_f32 v159, v142, v143\n"
    "v_mfma_f32_16x16x32_bf16 v[116:119], v[108:111], a[212:215], v[116:119]\n"
    "ds_write_b32 v5, v4\n"
    "v_mfma_f32_16x16x32_bf16 v[120:123], v[76:79], a[216:219], v[120:123]\n"
    "ds_read_b64_tr_b16 v[64:65], v203 offset:12288\n"
    "v_mfma_f32_16x16x32_bf16 v[120:123], v[80:83], a[220:223], v[120:123]\n"
    "ds_read_b64_tr_b16 v[66:67], v203 offset:13312\n"
    "v_mfma_f32_16x16x32_bf16 v[120:123], v[84:87], a[224:227], v[120:123]\n"
    "v_permlane32_swap_b32 v152, v154\n"
    "v_mfma_f32_16x16x32_bf16 v[120:123], v[88:91], a[228:231], v[120:123]\n"
    "ds_read_b64_tr_b16 v[68:69], v203 offset:14336\n"
    "v_mfma_f32_16x16x32_bf16 v[120:123], v[92:95], a[232:235], v[120:123]\n"
    "ds_read_b64_tr_b16 v[70:71], v203 offset:15360\n"
    "v_mfma_f32_16x16x32_bf16 v[120:123], v[96:99], a[236:239], v[120:123]\n"
    "v_permlane32_swap_b32 v153, v155\n"
    "v_mfma_f32_16x16x32_bf16 v[120:123], v[100:103], a[240:243], v[120:123]\n"
    "ds_read_b64_tr_b16 v[72:73], v203 offset:16384\n"
    "v_mfma_f32_16x16x32_bf16 v[120:123], v[104:107], a[244:247], v[120:123]\n"
    "ds_read_b64_tr_b16 v[74:75], v203 offset:17408\n"
    "v_mfma_f32_16x16x32_bf16 v[120:123], v[108:111], a[248:251], v[120:123]\n"
    "v_permlane32_swap_b32 v156, v158\n"
    "v_mfma_f32_16x16x32_bf16 v[124:127], v[76:79], a[252:255], v[124:127]\n"
    "v_permlane32_swap_b32 v157, v159\n"
    "v_mfma_f32_16x16x32_bf16 v[124:127], v[80:83], v[8:11], v[124:127]\n"
    "v_permlane16_swap_b32 v152, v154\n"
    "v_mfma_f32_16x16x32_bf16 v[124:127], v[84:87], v[12:15], v[124:127]\n"
    "v_permlane16_swap_b32 v153, v155\n"
    "v_mfma_f32_16x16x32_bf16 v[124:127], v[88:91], v[16:19], v[124:127]\n"
    "v_permlane16_swap_b32 v156, v158\n"
    "v_mfma_f32_16x16x32_bf16 v[124:127], v[92:95], v[20:23], v[124:127]\n"
    "v_permlane16_swap_b32 v157, v159\n"
    "v_mfma_f32_16x16x32_bf16 v[124:127], v[96:99], v[24:27], v[124:127]\n"
    "v_mfma_f32_16x16x32_bf16 v[124:127], v[100:103], v[28:31], v[124:127]\n"
    "v_mfma_f32_16x16x32_bf16 v[124:127], v[104:107], v[32:35], v[124:127]\n"
    "v_mfma_f32_16x16x32_bf16 v[124:127], v[108:111], v[36:39], v[124:127]\n"
    "s_waitcnt lgkmcnt(0)\n"
    "v_mfma_f32_16x16x32_bf16 v[128:131], v[40:43], a[0:3], 0\n"
    "ds_read_b64_tr_b16 v[76:77], v203 offset:18432\n"
    "v_mul_f32 v112, v253, v112\n"
    "v_mfma_f32_16x16x32_bf16 v[128:131], v[44:47], a[4:7], v[128:131]\n"
    "ds_read_b64_tr_b16 v[78:79], v203 offset:19456\n"
    "v_mul_f32 v113, v253, v113\n"
    "v_mfma_f32_16x16x32_bf16 v[128:131], v[48:51], a[8:11], v[128:131]\n"
    "v_mfma_f32_16x16x32_bf16 v[128:131], v[52:55], a[12:15], v[128:131]\n"
    "ds_read_b64_tr_b16 v[80:81], v203 offset:20480\n"
    "v_mul_f32 v114, v253, v114\n"
    "v_mfma_f32_16x16x32_bf16 v[128:131], v[56:59], a[16:19], v[128:131]\n"
    "ds_read_b64_tr_b16 v[82:83], v203 offset:21504\n"
    "v_mul_f32 v115, v253, v115\n"
    "v_mfma_f32_16x16x32_bf16 v[128:131], v[60:63], a[20:23], v[128:131]\n"
    "v_mfma_f32_16x16x32_bf16 v[128:131], v[64:67], a[24:27], v[128:131]\n"
    "ds_read_b64_tr_b16 v[84:85], v203 offset:22528\n"
    "v_cvt_pk_bf16_f32 v160, v112, v113\n"
    "v_mfma_f32_16x16x32_bf16 v[128:131], v[68:71], a[28:31], v[128:131]\n"
    "ds_read_b64_tr_b16 v[86:87], v203 offset:23552\n"
    "v_cvt_pk_bf16_f32 v161, v114, v115\n"
    "v_mfma_f32_16x16x32_bf16 v[128:131], v[72:75], a[32:35], v[128:131]\n"
    "v_mfma_f32_16x16x32_bf16 v[132:135], v[40:43], a[36:39], 0\n"
    "ds_read_b64_tr_b16 v[88:89], v203 offset:24576\n"
    "v_mul_f32 v116, v255, v116\n"
    "v_mfma_f32_16x16x32_bf16 v[132:135], v[44:47], a[40:43], v[132:135]\n"
    "ds_read_b64_tr_b16 v[90:91], v203 offset:25600\n"
    "v_mul_f32 v117, v255, v117\n"
    "v_mfma_f32_16x16x32_bf16 v[132:135], v[48:51], a[44:47], v[132:135]\n"
    "v_mfma_f32_16x16x32_bf16 v[132:135], v[52:55], a[48:51], v[132:135]\n"
    "ds_read_b64_tr_b16 v[92:93], v203 offset:26624\n"
    "v_mul_f32 v118, v255, v118\n"
    "v_mfma_f32_16x16x32_bf16 v[132:135], v[56:59], a[52:55], v[132:135]\n"
    "ds_read_b64_tr_b16 v[94:95], v203 offset:27648\n"
    "v_mul_f32 v119, v255, v119\n"
    "v_mfma_f32_16x16x32_bf16 v[132:135], v[60:63], a[56:59], v[132:135]\n"
    "v_mfma_f32_16x16x32_bf16 v[132:135], v[64:67], a[60:63], v[132:135]\n"
    "ds_read_b64_tr_b16 v[96:97], v203 offset:28672\n"
    "v_cvt_pk_bf16_f32 v164, v116, v117\n"
    "v_mfma_f32_16x16x32_bf16 v[132:135], v[68:71], a[64:67], v[132:135]\n"
    "ds_read_b64_tr_b16 v[98:99], v203 offset:29696\n"
    "v_cvt_pk_bf16_f32 v165, v118, v119\n"
    "v_mfma_f32_16x16x32_bf16 v[132:135], v[72:75], a[68:71], v[132:135]\n"
    "v_mfma_f32_16x16x32_bf16 v[136:139], v[40:43], a[72:75], 0\n"
    "ds_read_b64_tr_b16 v[100:101], v203 offset:30720\n"
    "v_mfma_f32_16x16x32_bf16 v[136:139], v[44:47], a[76:79], v[136:139]\n"
    "ds_read_b64_tr_b16 v[102:103], v203 offset:31744\n"
    "v_mfma_f32_16x16x32_bf16 v[136:139], v[48:51], a[80:83], v[136:139]\n"
    "v_mfma_f32_16x16x32_bf16 v[136:139], v[52:55], a[84:87], v[136:139]\n"
    "ds_read_b64_tr_b16 v[104:105], v203 offset:32768\n"
    "v_mfma_f32_16x16x32_bf16 v[136:139], v[56:59], a[88:91], v[136:139]\n"
    "ds_read_b64_tr_b16 v[106:107], v203 offset:33792\n"
    "v_mfma_f32_16x16x32_bf16 v[136:139], v[60:63], a[92:95], v[136:139]\n"
    "v_mfma_f32_16x16x32_bf16 v[136:139], v[64:67], a[96:99], v[136:139]\n"
    "ds_read_b64_tr_b16 v[108:109], v203 offset:34816\n"
    "v_mfma_f32_16x16x32_bf16 v[136:139], v[68:71], a[100:103], v[136:139]\n"
    "ds_read_b64_tr_b16 v[110:111], v203 offset:35840\n"
    "v_mfma_f32_16x16x32_bf16 v[136:139], v[72:75], a[104:107], v[136:139]\n"
    "v_mfma_f32_16x16x32_bf16 v[140:143], v[40:43], a[108:111], 0\n"
    "v_mfma_f32_16x16x32_bf16 v[140:143], v[44:47], a[112:115], v[140:143]\n"
    "v_mfma_f32_16x16x32_bf16 v[140:143], v[48:51], a[116:119], v[140:143]\n"
    "v_mfma_f32_16x16x32_bf16 v[140:143], v[52:55], a[120:123], v[140:143]\n"
    "v_mfma_f32_16x16x32_bf16 v[140:143], v[56:59], a[124:127], v[140:143]\n"
    "v_mfma_f32_16x16x32_bf16 v[140:143], v[60:63], a[128:131], v[140:143]\n"
    "v_mfma_f32_16x16x32_bf16 v[140:143], v[64:67], a[132:135], v[140:143]\n"
    "v_mfma_f32_16x16x32_bf16 v[140:143], v[68:71], a[136:139], v[140:143]\n"
    "v_mfma_f32_16x16x32_bf16 v[140:143], v[72:75], a[140:143], v[140:143]\n"
    "s_waitcnt vmcnt(0) lgkmcnt(0)\n"
    "s_barrier\n"
    "v_mfma_f32_16x16x32_bf16 v[128:131], v[76:79], a[144:147], v[128:131]\n"
    "ds_read_b128 a[80:83], v232 offset:0\n"
    "v_mul_f32 v120, v3, v120\n"
    "v_mfma_f32_16x16x32_bf16 v[128:131], v[80:83], a[148:151], v[128:131]\n"
    "ds_read_b128 a[84:87], v233 offset:0\n"
    "v_mul_f32 v121, v3, v121\n"
    "v_mfma_f32_16x16x32_bf16 v[128:131], v[84:87], a[152:155], v[128:131]\n"
    "v_accvgpr_write_b32 a16, v204\n"
    "v_accvgpr_write_b32 a17, v205\n"
    "v_mfma_f32_16x16x32_bf16 v[128:131], v[88:91], a[156:159], v[128:131]\n"
    "ds_read_b128 a[88:91], v234 offset:0\n"
    "v_mul_f32 v122, v3, v122\n"
    "v_mfma_f32_16x16x32_bf16 v[128:131], v[92:95], a[160:163], v[128:131]\n"
    "ds_read_b128 a[92:95], v235 offset:0\n"
    "v_mul_f32 v123, v3, v123\n"
    "v_mfma_f32_16x16x32_bf16 v[128:131], v[96:99], a[164:167], v[128:131]\n"
    "v_accvgpr_write_b32 a18, v206\n"
    "v_accvgpr_write_b32 a19, v207\n"
    "v_mfma_f32_16x16x32_bf16 v[128:131], v[100:103], a[168:171], v[128:131]\n"
    "ds_read_b128 a[96:99], v232 offset:4096\n"
    "v_cvt_pk_bf16_f32 v168, v120, v121\n"
    "v_mfma_f32_16x16x32_bf16 v[128:131], v[104:107], a[172:175], v[128:131]\n"
    "ds_read_b128 a[100:103], v233 offset:4096\n"
    "v_cvt_pk_bf16_f32 v169, v122, v123\n"
    "v_mfma_f32_16x16x32_bf16 v[128:131], v[108:111], a[176:179], v[128:131]\n"
    "v_accvgpr_write_b32 a32, v208\n"
    "v_accvgpr_write_b32 a33, v209\n"
    "v_mfma_f32_16x16x32_bf16 v[132:135], v[76:79], a[180:183], v[132:135]\n"
    "ds_read_b128 a[104:107], v234 offset:4096\n"
    "v_mul_f32 v124, v0, v124\n"
    "v_mfma_f32_16x16x32_bf16 v[132:135], v[80:83], a[184:187], v[132:135]\n"
    "ds_read_b128 a[108:111], v235 offset:4096\n"
    "v_mul_f32 v125, v0, v125\n"
    "v_mfma_f32_16x16x32_bf16 v[132:135], v[84:87], a[188:191], v[132:135]\n"
    "v_accvgpr_write_b32 a34, v210\n"
    "v_accvgpr_write_b32 a35, v211\n"
    "v_mfma_f32_16x16x32_bf16 v[132:135], v[88:91], a[192:195], v[132:135]\n"
    "ds_read_b128 a[112:115], v232 offset:8192\n"
    "v_mul_f32 v126, v0, v126\n"
    "v_mfma_f32_16x16x32_bf16 v[132:135], v[92:95], a[196:199], v[132:135]\n"
    "ds_read_b128 a[116:119], v233 offset:8192\n"
    "v_mul_f32 v127, v0, v127\n"
    "v_mfma_f32_16x16x32_bf16 v[132:135], v[96:99], a[200:203], v[132:135]\n"
    "v_accvgpr_write_b32 a48, v212\n"
    "v_accvgpr_write_b32 a49, v213\n"
    "v_mfma_f32_16x16x32_bf16 v[132:135], v[100:103], a[204:207], v[132:135]\n"
    "ds_read_b128 a[120:123], v234 offset:8192\n"
    "v_cvt_pk_bf16_f32 v172, v124, v125\n"
    "v_mfma_f32_16x16x32_bf16 v[132:135], v[104:107], a[208:211], v[132:135]\n"
    "ds_read_b128 a[124:127], v235 offset:8192\n"
    "v_cvt_pk_bf16_f32 v173, v126, v127\n"
    "v_mfma_f32_16x16x32_bf16 v[132:135], v[108:111], a[212:215], v[132:135]\n"
    "v_accvgpr_write_b32 a50, v214\n"
    "v_accvgpr_write_b32 a51, v215\n"
    "v_mfma_f32_16x16x32_bf16 v[136:139], v[76:79], a[216:219], v[136:139]\n"
    "ds_read_b128 a[128:131], v232 offset:12288\n"
    "v_accvgpr_write_b32 a64, v216\n"
    "v_mfma_f32_16x16x32_bf16 v[136:139], v[80:83], a[220:223], v[136:139]\n"
    "ds_read_b128 a[132:135], v233 offset:12288\n"
    "v_accvgpr_write_b32 a65, v217\n"
    "v_mfma_f32_16x16x32_bf16 v[136:139], v[84:87], a[224:227], v[136:139]\n"
    "v_accvgpr_write_b32 a66, v218\n"
    "v_accvgpr_write_b32 a67, v219\n"
    "v_mfma_f32_16x16x32_bf16 v[136:139], v[88:91], a[228:231], v[136:139]\n"
    "ds_read_b128 a[136:139], v234 offset:12288\n"
    "v_accvgpr_write_b32 a20, v176\n"
    "v_mfma_f32_16x16x32_bf16 v[136:139], v[92:95], a[232:235], v[136:139]\n"
    "ds_read_b128 a[140:143], v235 offset:12288\n"
    "v_accvgpr_write_b32 a21, v177\n"
    "v_mfma_f32_16x16x32_bf16 v[136:139], v[96:99], a[236:239], v[136:139]\n"
    "v_accvgpr_write_b32 a22, v178\n"
    "v_accvgpr_write_b32 a23, v179\n"
    "v_mfma_f32_16x16x32_bf16 v[136:139], v[100:103], a[240:243], v[136:139]\n"
    "v_accvgpr_write_b32 a36, v180\n"
    "v_mfma_f32_16x16x32_bf16 v[136:139], v[104:107], a[244:247], v[136:139]\n"
    "v_accvgpr_write_b32 a37, v181\n"
    "v_mfma_f32_16x16x32_bf16 v[136:139], v[108:111], a[248:251], v[136:139]\n"
    "v_accvgpr_write_b32 a38, v182\n"
    "v_accvgpr_write_b32 a39, v183\n"
    "v_mfma_f32_16x16x32_bf16 v[140:143], v[76:79], a[252:255], v[140:143]\n"
    "v_accvgpr_write_b32 a52, v184\n"
    "v_accvgpr_write_b32 a53, v185\n"
    "v_mfma_f32_16x16x32_bf16 v[140:143], v[80:83], v[8:11], v[140:143]\n"
    "v_accvgpr_write_b32 a54, v186\n"
    "v_accvgpr_write_b32 a55, v187\n"
    "v_mfma_f32_16x16x32_bf16 v[140:143], v[84:87], v[12:15], v[140:143]\n"
    "v_accvgpr_write_b32 a68, v188\n"
    "v_accvgpr_write_b32 a69, v189\n"
    "v_accvgpr_write_b32 a70, v190\n"
    "v_mfma_f32_16x16x32_bf16 v[140:143], v[88:91], v[16:19], v[140:143]\n"
    "v_accvgpr_write_b32 a71, v191\n"
    "v_accvgpr_write_b32 a24, v144\n"
    "v_accvgpr_write_b32 a25, v145\n"
    "v_mfma_f32_16x16x32_bf16 v[140:143], v[92:95], v[20:23], v[140:143]\n"
    "v_accvgpr_write_b32 a26, v146\n"
    "v_accvgpr_write_b32 a27, v147\n"
    "v_accvgpr_write_b32 a40, v148\n"
    "v_mfma_f32_16x16x32_bf16 v[140:143], v[96:99], v[24:27], v[140:143]\n"
    "v_accvgpr_write_b32 a41, v149\n"
    "v_accvgpr_write_b32 a42, v150\n"
    "v_accvgpr_write_b32 a43, v151\n"
    "v_mfma_f32_16x16x32_bf16 v[140:143], v[100:103], v[28:31], v[140:143]\n"
    "v_accvgpr_write_b32 a56, v152\n"
    "v_accvgpr_write_b32 a57, v153\n"
    "v_accvgpr_write_b32 a58, v154\n"
    "v_mfma_f32_16x16x32_bf16 v[140:143], v[104:107], v[32:35], v[140:143]\n"
    "v_accvgpr_write_b32 a59, v155\n"
    "v_accvgpr_write_b32 a72, v156\n"
    "v_accvgpr_write_b32 a73, v157\n"
    "v_mfma_f32_16x16x32_bf16 v[140:143], v[108:111], v[36:39], v[140:143]\n"
    "v_accvgpr_write_b32 a74, v158\n"
    "v_accvgpr_write_b32 a75, v159\n"
    "s_nop 7\n"
    "v_mul_f32 v128, v253, v128\n"
    "v_mul_f32 v129, v253, v129\n"
    "v_mul_f32 v130, v253, v130\n"
    "v_mul_f32 v131, v253, v131\n"
    "v_cvt_pk_bf16_f32 v162, v128, v129\n"
    "v_cvt_pk_bf16_f32 v163, v130, v131\n"
    "v_mul_f32 v132, v255, v132\n"
    "v_mul_f32 v133, v255, v133\n"
    "v_mul_f32 v134, v255, v134\n"
    "v_mul_f32 v135, v255, v135\n"
    "v_cvt_pk_bf16_f32 v166, v132, v133\n"
    "v_cvt_pk_bf16_f32 v167, v134, v135\n"
    "v_mul_f32 v136, v3, v136\n"
    "v_mul_f32 v137, v3, v137\n"
    "v_mul_f32 v138, v3, v138\n"
    "v_mul_f32 v139, v3, v139\n"
    "v_cvt_pk_bf16_f32 v170, v136, v137\n"
    "v_cvt_pk_bf16_f32 v171, v138, v139\n"
    "v_mul_f32 v140, v0, v140\n"
    "v_mul_f32 v141, v0, v141\n"
    "v_mul_f32 v142, v0, v142\n"
    "v_mul_f32 v143, v0, v143\n"
    "v_cvt_pk_bf16_f32 v174, v140, v141\n"
    "v_cvt_pk_bf16_f32 v175, v142, v143\n"
    "s_nop 1\n"
    "v_permlane32_swap_b32 v160, v162\n"
    "v_permlane32_swap_b32 v164, v166\n"
    "v_permlane32_swap_b32 v168, v170\n"
    "v_permlane32_swap_b32 v172, v174\n"
    "s_nop 1\n"
    "v_permlane32_swap_b32 v161, v163\n"
    "v_permlane32_swap_b32 v165, v167\n"
    "v_permlane32_swap_b32 v169, v171\n"
    "v_permlane32_swap_b32 v173, v175\n"
    "s_nop 1\n"
    "v_permlane16_swap_b32 v160, v162\n"
    "v_permlane16_swap_b32 v164, v166\n"
    "v_permlane16_swap_b32 v168, v170\n"
    "v_permlane16_swap_b32 v172, v174\n"
    "s_nop 1\n"
    "v_permlane16_swap_b32 v161, v163\n"
    "v_permlane16_swap_b32 v165, v167\n"
    "v_permlane16_swap_b32 v169, v171\n"
    "v_permlane16_swap_b32 v173, v175\n"
    "s_nop 1\n"
    "v_accvgpr_write_b32 a28, v160\n"
    "v_accvgpr_write_b32 a29, v161\n"
    "v_accvgpr_write_b32 a30, v162\n"
    "v_accvgpr_write_b32 a31, v163\n"
    "v_accvgpr_write_b32 a44, v164\n"
    "v_accvgpr_write_b32 a45, v165\n"
    "v_accvgpr_write_b32 a46, v166\n"
    "v_accvgpr_write_b32 a47, v167\n"
    "v_accvgpr_write_b32 a60, v168\n"
    "v_accvgpr_write_b32 a61, v169\n"
    "v_accvgpr_write_b32 a62, v170\n"
    "v_accvgpr_write_b32 a63, v171\n"
    "v_accvgpr_write_b32 a76, v172\n"
    "v_accvgpr_write_b32 a77, v173\n"
    "v_accvgpr_write_b32 a78, v174\n"
    "v_accvgpr_write_b32 a79, v175\n"
    "s_waitcnt lgkmcnt(0)\n"
    "v_mov_b32 v152, 0\n"
    "v_mov_b32 v153, 0\n"
    "v_mov_b32 v154, 0\n"
    "v_mov_b32 v155, 0\n"
    "v_mov_b32 v2, 0x3f803f80\n"
    "v_accvgpr_write_b32 a144, v2\n"
    "v_accvgpr_write_b32 a145, v2\n"
    "v_accvgpr_write_b32 a146, v2\n"
    "v_accvgpr_write_b32 a147, v2\n"
    "s_barrier\n"
    "s_add_u32 m0, s84, 0\n"
    "s_nop 0\n"
    "buffer_load_dwordx4 v244, s[56:59], s82 offen lds\n"
    "buffer_load_dwordx4 v245, s[56:59], s82 offen offset:1024 lds\n"
    "buffer_load_dwordx4 v246, s[56:59], s82 offen offset:2048 lds\n"
    "buffer_load_dwordx4 v247, s[56:59], s82 offen offset:3072 lds\n"
    "s_add_u32 m0, s84, 49152\n"
    "s_nop 0\n"
    "buffer_load_dwordx4 v248, s[60:63], s83 offen lds\n"
    "buffer_load_dwordx4 v249, s[60:63], s83 offen offset:1024 lds\n"
    "buffer_load_dwordx4 v248, s[60:63], s83 offen offset:2048 lds\n"
    "buffer_load_dwordx4 v249, s[60:63], s83 offen offset:3072 lds\n"
    "s_add_u32 s82, s82, 16384\n"
    "s_add_u32 s83, s83, 16384\n"
    "v_mfma_f32_16x16x32_bf16 v[200:203], a[80:83], a[16:19], 0\n"
    "v_mfma_f32_16x16x32_bf16 v[200:203], a[84:87], a[20:23], v[200:203]\n"
    "v_mfma_f32_16x16x32_bf16 v[200:203], a[88:91], a[24:27], v[200:203]\n"
    "v_mfma_f32_16x16x32_bf16 v[200:203], a[92:95], a[28:31], v[200:203]\n"
    "v_mfma_f32_16x16x32_bf16 v[204:207], a[96:99], a[16:19], 0\n"
    "v_mfma_f32_16x16x32_bf16 v[204:207], a[100:103], a[20:23], v[204:207]\n"
    "v_mfma_f32_16x16x32_bf16 v[204:207], a[104:107], a[24:27], v[204:207]\n"
    "v_mfma_f32_16x16x32_bf16 v[204:207], a[108:111], a[28:31], v[204:207]\n"
    "v_mfma_f32_16x16x32_bf16 v[208:211], a[112:115], a[16:19], 0\n"
    "v_mfma_f32_16x16x32_bf16 v[208:211], a[116:119], a[20:23], v[208:211]\n"
    "v_mfma_f32_16x16x32_bf16 v[208:211], a[120:123], a[24:27], v[208:211]\n"
    "v_mfma_f32_16x16x32_bf16 v[208:211], a[124:127], a[28:31], v[208:211]\n"
    "v_mfma_f32_16x16x32_bf16 v[212:215], a[128:131], a[16:19], 0\n"
    "v_mfma_f32_16x16x32_bf16 v[212:215], a[132:135], a[20:23], v[212:215]\n"
    "v_mfma_f32_16x16x32_bf16 v[212:215], a[136:139], a[24:27], v[212:215]\n"
    "v_mfma_f32_16x16x32_bf16 v[212:215], a[140:143], a[28:31], v[212:215]\n"
    "v_mfma_f32_16x16x32_bf16 v[216:219], a[80:83], a[32:35], 0\n"
    "v_mfma_f32_16x16x32_bf16 v[216:219], a[84:87], a[36:39], v[216:219]\n"
    "v_mfma_f32_16x16x32_bf16 v[216:219], a[88:91], a[40:43], v[216:219]\n"
    "v_max3_f32 v136, v200, v201, v202\n"
    "v_max3_f32 v136, v136, v203, v204\n"
    "v_max3_f32 v136, v136, v205, v206\n"
    "v_max3_f32 v136, v136, v207, v208\n"
    "v_mfma_f32_16x16x32_bf16 v[216:219], a[92:95], a[44:47], v[216:219]\n"
    "v_max3_f32 v136, v136, v209, v210\n"
    "v_max3_f32 v136, v136, v211, v212\n"
    "v_max3_f32 v136, v136, v213, v214\n"
    "v_max_f32 v136, v136, v215\n"
    "v_mov_b32 v137, v136\n"
    "v_mfma_f32_16x16x32_bf16 v[220:223], a[96:99], a[32:35], 0\n"
    "s_nop 1\n"
    "v_permlane16_swap_b32 v136, v137\n"
    "s_nop 1\n"
    "v_max_f32 v136, v136, v137\n"
    "v_mfma_f32_16x16x32_bf16 v[220:223], a[100:103], a[36:39], v[220:223]\n"
    "v_mov_b32 v137, v136\n"
    "s_nop 1\n"
    "v_permlane32_swap_b32 v136, v137\n"
    "s_nop 1\n"
    "v_max_f32 v136, v136, v137\n"
    "v_mfma_f32_16x16x32_bf16 v[220:223], a[104:107], a[40:43], v[220:223]\n"
    "v_sub_f32 v136, 0, v136\n"
    "v_mov_b32 v137, v136\n"
    "v_mov_b32 v138, v136\n"
    "v_mov_b32 v139, v136\n"
    "v_mfma_f32_16x16x32_bf16 v[220:223], a[108:111], a[44:47], v[220:223]\n"
    "v_add_f32 v200, v200, v136\n"
    "v_add_f32 v201, v201, v136\n"
    "v_add_f32 v202, v202, v136\n"
    "v_add_f32 v203, v203, v136\n"
    "v_add_f32 v204, v204, v136\n"
    "v_mfma_f32_16x16x32_bf16 v[224:227], a[112:115], a[32:35], 0\n"
    "v_add_f32 v205, v205, v136\n"
    "v_add_f32 v206, v206, v136\n"
    "v_add_f32 v207, v207, v136\n"
    "v_add_f32 v208, v208, v136\n"
    "v_mfma_f32_16x16x32_bf16 v[224:227], a[116:119], a[36:39], v[224:227]\n"
    "v_add_f32 v209, v209, v136\n"
    "v_add_f32 v210, v210, v136\n"
    "v_add_f32 v211, v211, v136\n"
    "v_add_f32 v212, v212, v136\n"
    "v_add_f32 v213, v213, v136\n"
    "v_mfma_f32_16x16x32_bf16 v[224:227], a[120:123], a[40:43], v[224:227]\n"
    "v_add_f32 v214, v214, v136\n"
    "v_add_f32 v215, v215, v136\n"
    "v_exp_f32 v200, v200\n"
    "v_exp_f32 v201, v201\n"
    "v_mfma_f32_16x16x32_bf16 v[224:227], a[124:127], a[44:47], v[224:227]\n"
    "v_exp_f32 v202, v202\n"
    "v_exp_f32 v203, v203\n"
    "v_exp_f32 v204, v204\n"
    "v_exp_f32 v205, v205\n"
    "v_exp_f32 v206, v206\n"
    "v_mfma_f32_16x16x32_bf16 v[228:231], a[128:131], a[32:35], 0\n"
    "v_exp_f32 v207, v207\n"
    "v_exp_f32 v208, v208\n"
    "v_exp_f32 v209, v209\n"
    "v_exp_f32 v210, v210\n"
    "v_mfma_f32_16x16x32_bf16 v[228:231], a[132:135], a[36:39], v[228:231]\n"
    "v_exp_f32 v211, v211\n"
    "v_exp_f32 v212, v212\n"
    "v_exp_f32 v213, v213\n"
    "v_exp_f32 v214, v214\n"
    "v_exp_f32 v215, v215\n"
    "v_mfma_f32_16x16x32_bf16 v[228:231], a[136:139], a[40:43], v[228:231]\n"
    "s_nop 0\n"
    "v_cvt_pk_bf16_f32 v168, v200, v201\n"
    "v_cvt_pk_bf16_f32 v169, v202, v203\n"
    "v_cvt_pk_bf16_f32 v170, v204, v205\n"
    "v_mfma_f32_16x16x32_bf16 v[228:231], a[140:143], a[44:47], v[228:231]\n"
    "v_cvt_pk_bf16_f32 v171, v206, v207\n"
    "v_cvt_pk_bf16_f32 v172, v208, v209\n"
    "v_cvt_pk_bf16_f32 v173, v210, v211\n"
    "v_cvt_pk_bf16_f32 v174, v212, v213\n"
    "v_cvt_pk_bf16_f32 v175, v214, v215\n"
    "v_mfma_f32_16x16x32_bf16 v[200:203], a[80:83], a[48:51], 0\n"
    "v_mfma_f32_16x16x32_bf16 v[200:203], a[84:87], a[52:55], v[200:203]\n"
    "v_mfma_f32_16x16x32_bf16 v[200:203], a[88:91], a[56:59], v[200:203]\n"
    "v_max3_f32 v140, v216, v217, v218\n"
    "v_max3_f32 v140, v140, v219, v220\n"
    "v_max3_f32 v140, v140, v221, v222\n"
    "v_max3_f32 v140, v140, v223, v224\n"
    "v_mfma_f32_16x16x32_bf16 v[200:203], a[92:95], a[60:63], v[200:203]\n"
    "v_max3_f32 v140, v140, v225, v226\n"
    "v_max3_f32 v140, v140, v227, v228\n"
    "v_max3_f32 v140, v140, v229, v230\n"
    "v_max_f32 v140, v140, v231\n"
    "v_mov_b32 v141, v140\n"
    "v_mfma_f32_16x16x32_bf16 v[204:207], a[96:99], a[48:51], 0\n"
    "s_nop 1\n"
    "v_permlane16_swap_b32 v140, v141\n"
    "s_nop 1\n"
    "v_max_f32 v140, v140, v141\n"
    "v_mfma_f32_16x16x32_bf16 v[204:207], a[100:103], a[52:55], v[204:207]\n"
    "v_mov_b32 v141, v140\n"
    "s_nop 1\n"
    "v_permlane32_swap_b32 v140, v141\n"
    "s_nop 1\n"
    "v_max_f32 v140, v140, v141\n"
    "v_mfma_f32_16x16x32_bf16 v[204:207], a[104:107], a[56:59], v[204:207]\n"
    "v_sub_f32 v140, 0, v140\n"
    "v_mov_b32 v141, v140\n"
    "v_mov_b32 v142, v140\n"
    "v_mov_b32 v143, v140\n"
    "v_mfma_f32_16x16x32_bf16 v[204:207], a[108:111], a[60:63], v[204:207]\n"
    "v_add_f32 v216, v216, v140\n"
    "v_add_f32 v217, v217, v140\n"
    "v_add_f32 v218, v218, v140\n"
    "v_add_f32 v219, v219, v140\n"
    "v_add_f32 v220, v220, v140\n"
    "v_mfma_f32_16x16x32_bf16 v[208:211], a[112:115], a[48:51], 0\n"
    "v_add_f32 v221, v221, v140\n"
    "v_add_f32 v222, v222, v140\n"
    "v_add_f32 v223, v223, v140\n"
    "v_add_f32 v224, v224, v140\n"
    "v_mfma_f32_16x16x32_bf16 v[208:211], a[116:119], a[52:55], v[208:211]\n"
    "v_add_f32 v225, v225, v140\n"
    "v_add_f32 v226, v226, v140\n"
    "v_add_f32 v227, v227, v140\n"
    "v_add_f32 v228, v228, v140\n"
    "v_add_f32 v229, v229, v140\n"
    "v_mfma_f32_16x16x32_bf16 v[208:211], a[120:123], a[56:59], v[208:211]\n"
    "v_add_f32 v230, v230, v140\n"
    "v_add_f32 v231, v231, v140\n"
    "v_exp_f32 v216, v216\n"
    "v_exp_f32 v217, v217\n"
    "v_mfma_f32_16x16x32_bf16 v[208:211], a[124:127], a[60:63], v[208:211]\n"
    "v_exp_f32 v218, v218\n"
    "v_exp_f32 v219, v219\n"
    "v_exp_f32 v220, v220\n"
    "v_exp_f32 v221, v221\n"
    "v_exp_f32 v222, v222\n"
    "v_mfma_f32_16x16x32_bf16 v[212:215], a[128:131], a[48:51], 0\n"
    "v_exp_f32 v223, v223\n"
    "v_exp_f32 v224, v224\n"
    "v_exp_f32 v225, v225\n"
    "v_exp_f32 v226, v226\n"
    "v_mfma_f32_16x16x32_bf16 v[212:215], a[132:135], a[52:55], v[212:215]\n"
    "v_exp_f32 v227, v227\n"
    "v_exp_f32 v228, v228\n"
    "v_exp_f32 v229, v229\n"
    "v_exp_f32 v230, v230\n"
    "v_exp_f32 v231, v231\n"
    "v_mfma_f32_16x16x32_bf16 v[212:215], a[136:139], a[56:59], v[212:215]\n"
    "s_nop 0\n"
    "v_cvt_pk_bf16_f32 v176, v216, v217\n"
    "v_cvt_pk_bf16_f32 v177, v218, v219\n"
    "v_cvt_pk_bf16_f32 v178, v220, v221\n"
    "v_mfma_f32_16x16x32_bf16 v[212:215], a[140:143], a[60:63], v[212:215]\n"
    "v_cvt_pk_bf16_f32 v179, v222, v223\n"
    "v_cvt_pk_bf16_f32 v180, v224, v225\n"
    "v_cvt_pk_bf16_f32 v181, v226, v227\n"
    "v_cvt_pk_bf16_f32 v182, v228, v229\n"
    "v_cvt_pk_bf16_f32 v183, v230, v231\n"
    "v_mfma_f32_16x16x32_bf16 v[216:219], a[80:83], a[64:67], 0\n"
    "v_mfma_f32_16x16x32_bf16 v[216:219], a[84:87], a[68:71], v[216:219]\n"
    "v_mfma_f32_16x16x32_bf16 v[216:219], a[88:91], a[72:75], v[216:219]\n"
    "v_max3_f32 v144, v200, v201, v202\n"
    "v_max3_f32 v144, v144, v203, v204\n"
    "v_max3_f32 v144, v144, v205, v206\n"
    "v_max3_f32 v144, v144, v207, v208\n"
    "v_mfma_f32_16x16x32_bf16 v[216:219], a[92:95], a[76:79], v[216:219]\n"
    "v_max3_f32 v144, v144, v209, v210\n"
    "v_max3_f32 v144, v144, v211, v212\n"
    "v_max3_f32 v144, v144, v213, v214\n"
    "v_max_f32 v144, v144, v215\n"
    "v_mov_b32 v145, v144\n"
    "v_mfma_f32_16x16x32_bf16 v[220:223], a[96:99], a[64:67], 0\n"
    "s_nop 1\n"
    "v_permlane16_swap_b32 v144, v145\n"
    "s_nop 1\n"
    "v_max_f32 v144, v144, v145\n"
    "v_mfma_f32_16x16x32_bf16 v[220:223], a[100:103], a[68:71], v[220:223]\n"
    "v_mov_b32 v145, v144\n"
    "s_nop 1\n"
    "v_permlane32_swap_b32 v144, v145\n"
    "s_nop 1\n"
    "v_max_f32 v144, v144, v145\n"
    "v_mfma_f32_16x16x32_bf16 v[220:223], a[104:107], a[72:75], v[220:223]\n"
    "v_sub_f32 v144, 0, v144\n"
    "v_mov_b32 v145, v144\n"
    "v_mov_b32 v146, v144\n"
    "v_mov_b32 v147, v144\n"
    "v_mfma_f32_16x16x32_bf16 v[220:223], a[108:111], a[76:79], v[220:223]\n"
    "v_add_f32 v200, v200, v144\n"
    "v_add_f32 v201, v201, v144\n"
    "v_add_f32 v202, v202, v144\n"
    "v_add_f32 v203, v203, v144\n"
    "v_add_f32 v204, v204, v144\n"
    "v_mfma_f32_16x16x32_bf16 v[224:227], a[112:115], a[64:67], 0\n"
    "v_add_f32 v205, v205, v144\n"
    "v_add_f32 v206, v206, v144\n"
    "v_add_f32 v207, v207, v144\n"
    "v_add_f32 v208, v208, v144\n"
    "v_mfma_f32_16x16x32_bf16 v[224:227], a[116:119], a[68:71], v[224:227]\n"
    "v_add_f32 v209, v209, v144\n"
    "v_add_f32 v210, v210, v144\n"
    "v_add_f32 v211, v211, v144\n"
    "v_add_f32 v212, v212, v144\n"
    "v_add_f32 v213, v213, v144\n"
    "v_mfma_f32_16x16x32_bf16 v[224:227], a[120:123], a[72:75], v[224:227]\n"
    "v_add_f32 v214, v214, v144\n"
    "v_add_f32 v215, v215, v144\n"
    "v_exp_f32 v200, v200\n"
    "v_exp_f32 v201, v201\n"
    "v_mfma_f32_16x16x32_bf16 v[224:227], a[124:127], a[76:79], v[224:227]\n"
    "v_exp_f32 v202, v202\n"
    "v_exp_f32 v203, v203\n"
    "v_exp_f32 v204, v204\n"
    "v_exp_f32 v205, v205\n"
    "v_exp_f32 v206, v206\n"
    "v_mfma_f32_16x16x32_bf16 v[228:231], a[128:131], a[64:67], 0\n"
    "v_exp_f32 v207, v207\n"
    "v_exp_f32 v208, v208\n"
    "v_exp_f32 v209, v209\n"
    "v_exp_f32 v210, v210\n"
    "v_mfma_f32_16x16x32_bf16 v[228:231], a[132:135], a[68:71], v[228:231]\n"
    "v_exp_f32 v211, v211\n"
    "v_exp_f32 v212, v212\n"
    "v_exp_f32 v213, v213\n"
    "v_exp_f32 v214, v214\n"
    "v_exp_f32 v215, v215\n"
    "v_mfma_f32_16x16x32_bf16 v[228:231], a[136:139], a[72:75], v[228:231]\n"
    "s_nop 0\n"
    "v_cvt_pk_bf16_f32 v184, v200, v201\n"
    "v_cvt_pk_bf16_f32 v185, v202, v203\n"
    "v_cvt_pk_bf16_f32 v186, v204, v205\n"
    "v_mfma_f32_16x16x32_bf16 v[228:231], a[140:143], a[76:79], v[228:231]\n"
    "v_cvt_pk_bf16_f32 v187, v206, v207\n"
    "v_cvt_pk_bf16_f32 v188, v208, v209\n"
    "v_cvt_pk_bf16_f32 v189, v210, v211\n"
    "v_cvt_pk_bf16_f32 v190, v212, v213\n"
    "v_cvt_pk_bf16_f32 v191, v214, v215\n"
    "ds_read_b128 a[80:83], v232 offset:16384\n"
    "ds_read_b128 a[84:87], v233 offset:16384\n"
    "ds_read_b128 a[88:91], v234 offset:16384\n"
    "ds_read_b128 a[92:95], v235 offset:16384\n"
    "ds_read_b128 a[96:99], v232 offset:20480\n"
    "ds_read_b64_tr_b16 a[112:113], v236 offset:32768\n"
    "ds_read_b64_tr_b16 a[114:115], v236 offset:36864\n"
    "ds_read_b64_tr_b16 a[116:117], v236 offset:40960\n"
    "ds_read_b64_tr_b16 a[118:119], v236 offset:45056\n"
    "ds_read_b64_tr_b16 a[120:121], v237 offset:32768\n"
    "ds_read_b64_tr_b16 a[122:123], v237 offset:36864\n"
    "ds_read_b64_tr_b16 a[124:125], v237 offset:40960\n"
    "ds_read_b64_tr_b16 a[126:127], v237 offset:45056\n"
    "ds_read_b64_tr_b16 a[128:129], v238 offset:32768\n"
    "ds_read_b64_tr_b16 a[130:131], v238 offset:36864\n"
    "s_nop 4\n"
    "v_mfma_f32_32x32x16_bf16 v[8:23], v[152:155], v[152:155], 0\n"
    "v_max3_f32 v148, v216, v217, v218\n"
    "v_max3_f32 v148, v148, v219, v220\n"
    "v_max3_f32 v148, v148, v221, v222\n"
    "v_max3_f32 v148, v148, v223, v224\n"
    "v_max3_f32 v148, v148, v225, v226\n"
    "v_max3_f32 v148, v148, v227, v228\n"
    "v_mfma_f32_32x32x16_bf16 v[24:39], v[152:155], v[152:155], 0\n"
    "v_max3_f32 v148, v148, v229, v230\n"
    "v_max_f32 v148, v148, v231\n"
    "v_mov_b32 v149, v148\n"
    "s_nop 1\n"
    "v_permlane16_swap_b32 v148, v149\n"
    "s_nop 1\n"
    "v_mfma_f32_32x32x16_bf16 v[40:55], v[152:155], v[152:155], 0\n"
    "v_max_f32 v148, v148, v149\n"
    "v_mov_b32 v149, v148\n"
    "s_nop 1\n"
    "v_permlane32_swap_b32 v148, v149\n"
    "s_nop 1\n"
    "v_max_f32 v148, v148, v149\n"
    "v_sub_f32 v148, 0, v148\n"
    "v_mfma_f32_32x32x16_bf16 v[56:71], v[152:155], v[152:155], 0\n"
    "v_mov_b32 v149, v148\n"
    "v_mov_b32 v150, v148\n"
    "v_mov_b32 v151, v148\n"
    "v_add_f32 v216, v216, v148\n"
    "v_add_f32 v217, v217, v148\n"
    "v_add_f32 v218, v218, v148\n"
    "v_mfma_f32_32x32x16_bf16 v[72:87], v[152:155], v[152:155], 0\n"
    "v_add_f32 v219, v219, v148\n"
    "v_add_f32 v220, v220, v148\n"
    "v_add_f32 v221, v221, v148\n"
    "v_add_f32 v222, v222, v148\n"
    "v_add_f32 v223, v223, v148\n"
    "v_add_f32 v224, v224, v148\n"
    "v_mfma_f32_32x32x16_bf16 v[88:103], v[152:155], v[152:155], 0\n"
    "v_add_f32 v225, v225, v148\n"
    "v_add_f32 v226, v226, v148\n"
    "v_add_f32 v227, v227, v148\n"
    "v_add_f32 v164, v228, v148\n"
    "v_add_f32 v165, v229, v148\n"
    "v_add_f32 v166, v230, v148\n"
    "v_add_f32 v167, v231, v148\n"
    "v_mfma_f32_32x32x16_bf16 v[104:119], v[152:155], v[152:155], 0\n"
    "v_exp_f32 v216, v216\n"
    "v_exp_f32 v217, v217\n"
    "v_exp_f32 v218, v218\n"
    "v_exp_f32 v219, v219\n"
    "v_exp_f32 v220, v220\n"
    "v_exp_f32 v221, v221\n"
    "v_mfma_f32_32x32x16_bf16 v[120:135], v[152:155], v[152:155], 0\n"
    "v_exp_f32 v222, v222\n"
    "v_exp_f32 v223, v223\n"
    "v_exp_f32 v224, v224\n"
    "v_exp_f32 v225, v225\n"
    "v_exp_f32 v226, v226\n"
    "v_exp_f32 v227, v227\n"
    "v_mfma_f32_32x32x16_bf16 a[0:15], v[152:155], v[152:155], 0\n"
    "s_nop 0\n"
    "v_cvt_pk_bf16_f32 v192, v216, v217\n"
    "v_cvt_pk_bf16_f32 v193, v218, v219\n"
    "v_cvt_pk_bf16_f32 v194, v220, v221\n"
    "v_cvt_pk_bf16_f32 v195, v222, v223\n"
    "v_cvt_pk_bf16_f32 v196, v224, v225\n"
    "v_cvt_pk_bf16_f32 v197, v226, v227\n"
    "s_waitcnt lgkmcnt(0)\n"
    "s_nop 7\n"
    "s_mov_b32 s18, 0\n"
    "s_mov_b32 s78, 0\n"
    "s_mov_b32 s19, 0x20000\n"
    "s_mov_b32 s79, 0x20000\n"
    "s_mov_b32 s16, s12\n"
    "s_and_b32 s17, s13, 0xffff\n"
    "s_mov_b32 s76, s12\n"
    "s_and_b32 s77, s13, 0xffff\n"
    "s_mov_b32 s14, 0\n"
    "s_mov_b32 s80, 0\n"
    "v_mov_b32 v250, s85\n"
    "v_mov_b32 v2, -1\n"
    "v_accvgpr_write_b32 a216, v2\n"
    "v_accvgpr_write_b32 a217, v2\n"
    "v_accvgpr_write_b32 a218, v2\n"
    "v_accvgpr_write_b32 a219, v2\n"
    "s_and_b32 s96, s87, 1\n"
    "s_lshl_b32 s96, s96, 3\n"
    "s_add_u32 s96, s96, s88\n"
    "s_lshl_b32 s96, s96, 2\n"
    "s_add_u32 s96, s96, s39\n"
    "s_lshl_b32 s96, s96, 9\n"
    "s_add_u32 s5, s96, 131072\n"
    "v_and_b32 v251, 3, v1\n"
    "v_lshlrev_b32 v251, 7, v251\n"
    "s_mov_b32 s15, 0\n"
    "s_mov_b32 s72, 16\n"
    "s_mov_b32 s71, 16\n"
    "s_branch L_attn_mid_%=\n"
    "L_attn_loop_%=:\n"
    "v_mfma_f32_16x16x32_bf16 v[152:155], a[80:83], a[16:19], v[136:139]\n"
    "v_exp_f32 v164, v164\n"
    "v_mfma_f32_16x16x32_bf16 v[152:155], a[84:87], a[20:23], v[152:155]\n"
    "v_exp_f32 v165, v165\n"
    "v_mfma_f32_16x16x32_bf16 v[152:155], a[88:91], a[24:27], v[152:155]\n"
    "v_exp_f32 v166, v166\n"
    "v_mfma_f32_16x16x32_bf16 v[152:155], a[92:95], a[28:31], v[152:155]\n"
    "v_exp_f32 v167, v167\n"
    "v_mfma_f32_16x16x32_bf16 v[8:11], a[112:115], v[200:203], v[8:11]\n"
    "ds_read_b128 a[100:103], v233 offset:4096\n"
    "v_cvt_pk_bf16_f32 v230, v164, v165\n"
    "v_mfma_f32_16x16x32_bf16 v[8:11], a[116:119], v[204:207], v[8:11]\n"
    "ds_read_b64_tr_b16 a[132:133], v238 offset:57344\n"
    "v_cvt_pk_bf16_f32 v231, v166, v167\n"
    "v_mfma_f32_16x16x32_bf16 v[12:15], a[112:115], v[208:211], v[12:15]\n"
    "ds_read_b64_tr_b16 a[134:135], v238 offset:61440\n"
    "v_mfma_f32_16x16x32_bf16 v[12:15], a[116:119], v[212:215], v[12:15]\n"
    "v_mfma_f32_16x16x32_bf16 v[156:159], a[80:83], a[32:35], v[140:143]\n"
    "v_exp_f32 v152, v152\n"
    "v_mfma_f32_16x16x32_bf16 v[156:159], a[84:87], a[36:39], v[156:159]\n"
    "v_exp_f32 v153, v153\n"
    "v_mfma_f32_16x16x32_bf16 v[156:159], a[88:91], a[40:43], v[156:159]\n"
    "v_exp_f32 v154, v154\n"
    "v_mfma_f32_16x16x32_bf16 v[156:159], a[92:95], a[44:47], v[156:159]\n"
    "v_exp_f32 v155, v155\n"
    "v_mfma_f32_16x16x32_bf16 v[16:19], a[112:115], v[216:219], v[16:19]\n"
    "ds_read_b128 a[104:107], v234 offset:4096\n"
    "v_cvt_pk_bf16_f32 v168, v152, v153\n"
    "v_mfma_f32_16x16x32_bf16 v[16:19], a[116:119], v[220:223], v[16:19]\n"
    "ds_read_b64_tr_b16 a[136:137], v239 offset:49152\n"
    "v_cvt_pk_bf16_f32 v169, v154, v155\n"
    "v_mfma_f32_16x16x32_bf16 v[20:23], a[112:115], v[224:227], v[20:23]\n"
    "ds_read_b64_tr_b16 a[138:139], v239 offset:53248\n"
    "v_mfma_f32_16x16x32_bf16 v[20:23], a[116:119], v[228:231], v[20:23]\n"
    "s_waitcnt lgkmcnt(5)\n"
    "v_mfma_f32_16x16x32_bf16 v[160:163], a[80:83], a[48:51], v[144:147]\n"
    "v_exp_f32 v156, v156\n"
    "v_mfma_f32_16x16x32_bf16 v[160:163], a[84:87], a[52:55], v[160:163]\n"
    "v_exp_f32 v157, v157\n"
    "v_mfma_f32_16x16x32_bf16 v[160:163], a[88:91], a[56:59], v[160:163]\n"
    "v_exp_f32 v158, v158\n"
    "v_mfma_f32_16x16x32_bf16 v[160:163], a[92:95], a[60:63], v[160:163]\n"
    "ds_read_b64_tr_b16 a[212:213], v250\n"
    "ds_read_b64_tr_b16 a[214:215], v250 offset:18432\n"
    "v_exp_f32 v159, v159\n"
    "v_mfma_f32_16x16x32_bf16 v[24:27], a[120:123], v[200:203], v[24:27]\n"
    "ds_read_b128 a[108:111], v235 offset:4096\n"
    "v_cvt_pk_bf16_f32 v176, v156, v157\n"
    "v_mfma_f32_16x16x32_bf16 v[24:27], a[124:127], v[204:207], v[24:27]\n"
    "ds_read_b64_tr_b16 a[140:141], v239 offset:57344\n"
    "v_cvt_pk_bf16_f32 v177, v158, v159\n"
    "v_mfma_f32_16x16x32_bf16 v[28:31], a[120:123], v[208:211], v[28:31]\n"
    "ds_read_b64_tr_b16 a[142:143], v239 offset:61440\n"
    "v_mfma_f32_16x16x32_bf16 v[28:31], a[124:127], v[212:215], v[28:31]\n"
    "v_mfma_f32_16x16x32_bf16 v[164:167], a[80:83], a[64:67], v[148:151]\n"
    "v_exp_f32 v160, v160\n"
    "v_mfma_f32_16x16x32_bf16 v[164:167], a[84:87], a[68:71], v[164:167]\n"
    "v_exp_f32 v161, v161\n"
    "v_mfma_f32_16x16x32_bf16 v[164:167], a[88:91], a[72:75], v[164:167]\n"
    "v_exp_f32 v162, v162\n"
    "v_mfma_f32_16x16x32_bf16 v[164:167], a[92:95], a[76:79], v[164:167]\n"
    "v_exp_f32 v163, v163\n"
    "v_mfma_f32_16x16x32_bf16 v[32:35], a[120:123], v[216:219], v[32:35]\n"
    "ds_read_b128 a[80:83], v232 offset:8192\n"
    "v_cvt_pk_bf16_f32 v184, v160, v161\n"
    "v_mfma_f32_16x16x32_bf16 v[32:35], a[124:127], v[220:223], v[32:35]\n"
    "ds_read_b64_tr_b16 a[112:113], v240 offset:49152\n"
    "v_cvt_pk_bf16_f32 v185, v162, v163\n"
    "v_mfma_f32_16x16x32_bf16 v[36:39], a[120:123], v[224:227], v[36:39]\n"
    "ds_read_b64_tr_b16 a[114:115], v240 offset:53248\n"
    "v_mfma_f32_16x16x32_bf16 v[36:39], a[124:127], v[228:231], v[36:39]\n"
    "s_waitcnt lgkmcnt(5)\n"
    "v_mfma_f32_16x16x32_bf16 a[0:3], a[144:147], v[200:203], a[0:3]\n"
    "v_mfma_f32_16x16x32_bf16 a[0:3], a[144:147], v[204:207], a[0:3]\n"
    "v_mfma_f32_16x16x32_bf16 v[152:155], a[96:99], a[16:19], v[136:139]\n"
    "v_exp_f32 v164, v164\n"
    "v_mfma_f32_16x16x32_bf16 v[152:155], a[100:103], a[20:23], v[152:155]\n"
    "v_exp_f32 v165, v165\n"
    "v_mfma_f32_16x16x32_bf16 v[152:155], a[104:107], a[24:27], v[152:155]\n"
    "v_exp_f32 v166, v166\n"
    "v_mfma_f32_16x16x32_bf16 v[152:155], a[108:111], a[28:31], v[152:155]\n"
    "ds_read_b64_tr_b16 a[220:221], v250 offset:128\n"
    "ds_read_b64_tr_b16 a[222:223], v250 offset:18560\n"
    "v_exp_f32 v167, v167\n"
    "v_mfma_f32_16x16x32_bf16 v[40:43], a[128:131], v[200:203], v[40:43]\n"
    "ds_read_b128 a[84:87], v233 offset:8192\n"
    "v_cvt_pk_bf16_f32 v192, v164, v165\n"
    "v_mfma_f32_16x16x32_bf16 v[40:43], a[132:135], v[204:207], v[40:43]\n"
    "ds_read_b64_tr_b16 a[116:117], v240 offset:57344\n"
    "v_cvt_pk_bf16_f32 v193, v166, v167\n"
    "v_mfma_f32_16x16x32_bf16 v[44:47], a[128:131], v[208:211], v[44:47]\n"
    "ds_read_b64_tr_b16 a[118:119], v240 offset:61440\n"
    "v_mfma_f32_16x16x32_bf16 v[44:47], a[132:135], v[212:215], v[44:47]\n"
    "buffer_store_dwordx4 a[212:215], v252, s[16:19], s14 offen sc1\n"
    "s_add_u32 s14, s14, 4096\n"
    "v_mfma_f32_16x16x32_bf16 v[156:159], a[96:99], a[32:35], v[140:143]\n"
    "v_exp_f32 v152, v152\n"
    "v_mfma_f32_16x16x32_bf16 v[156:159], a[100:103], a[36:39], v[156:159]\n"
    "v_exp_f32 v153, v153\n"
    "v_mfma_f32_16x16x32_bf16 v[156:159], a[104:107], a[40:43], v[156:159]\n"
    "v_exp_f32 v154, v154\n"
    "v_mfma_f32_16x16x32_bf16 v[156:159], a[108:111], a[44:47], v[156:159]\n"
    "v_exp_f32 v155, v155\n"
    "v_mfma_f32_16x16x32_bf16 v[48:51], a[128:131], v[216:219], v[48:51]\n"
    "ds_read_b128 a[88:91], v234 offset:8192\n"
    "v_cvt_pk_bf16_f32 v170, v152, v153\n"
    "v_mfma_f32_16x16x32_bf16 v[48:51], a[132:135], v[220:223], v[48:51]\n"
    "ds_read_b64_tr_b16 a[120:121], v241 offset:49152\n"
    "v_cvt_pk_bf16_f32 v171, v154, v155\n"
    "v_mfma_f32_16x16x32_bf16 v[52:55], a[128:131], v[224:227], v[52:55]\n"
    "ds_read_b64_tr_b16 a[122:123], v241 offset:53248\n"
    "v_mfma_f32_16x16x32_bf16 v[52:55], a[132:135], v[228:231], v[52:55]\n"
    "s_waitcnt lgkmcnt(5)\n"
    "v_mfma_f32_16x16x32_bf16 v[160:163], a[96:99], a[48:51], v[144:147]\n"
    "v_exp_f32 v156, v156\n"
    "v_mfma_f32_16x16x32_bf16 v[160:163], a[100:103], a[52:55], v[160:163]\n"
    "v_exp_f32 v157, v157\n"
    "v_mfma_f32_16x16x32_bf16 v[160:163], a[104:107], a[56:59], v[160:163]\n"
    "v_exp_f32 v158, v158\n"
    "v_mfma_f32_16x16x32_bf16 v[160:163], a[108:111], a[60:63], v[160:163]\n"
    "v_exp_f32 v159, v159\n"
    "v_mfma_f32_16x16x32_bf16 v[56:59], a[136:139], v[200:203], v[56:59]\n"
    "ds_read_b128 a[92:95], v235 offset:8192\n"
    "v_cvt_pk_bf16_f32 v178, v156, v157\n"
    "v_mfma_f32_16x16x32_bf16 v[56:59], a[140:143], v[204:207], v[56:59]\n"
    "ds_read_b64_tr_b16 a[124:125], v241 offset:57344\n"
    "v_cvt_pk_bf16_f32 v179, v158, v159\n"
    "v_mfma_f32_16x16x32_bf16 v[60:63], a[136:139], v[208:211], v[60:63]\n"
    "ds_read_b64_tr_b16 a[126:127], v241 offset:61440\n"
    "v_mfma_f32_16x16x32_bf16 v[60:63], a[140:143], v[212:215], v[60:63]\n"
    "buffer_store_dwordx4 a[220:223], v252, s[16:19], s14 offen sc1\n"
    "s_add_u32 s14, s14, 4096\n"
    "v_add_u32 v250, 256, v250\n"
    "v_mfma_f32_16x16x32_bf16 v[164:167], a[96:99], a[64:67], v[148:151]\n"
    "v_exp_f32 v160, v160\n"
    "v_mfma_f32_16x16x32_bf16 v[164:167], a[100:103], a[68:71], v[164:167]\n"
    "v_exp_f32 v161, v161\n"
    "v_mfma_f32_16x16x32_bf16 v[164:167], a[104:107], a[72:75], v[164:167]\n"
    "v_exp_f32 v162, v162\n"
    "v_mfma_f32_16x16x32_bf16 v[164:167], a[108:111], a[76:79], v[164:167]\n"
    "v_exp_f32 v163, v163\n"
    "v_mfma_f32_16x16x32_bf16 v[64:67], a[136:139], v[216:219], v[64:67]\n"
    "ds_read_b128 a[96:99], v232 offset:12288\n"
    "v_cvt_pk_bf16_f32 v186, v160, v161\n"
    "v_mfma_f32_16x16x32_bf16 v[64:67], a[140:143], v[220:223], v[64:67]\n"
    "ds_read_b64_tr_b16 a[128:129], v242 offset:49152\n"
    "v_cvt_pk_bf16_f32 v187, v162, v163\n"
    "v_mfma_f32_16x16x32_bf16 v[68:71], a[136:139], v[224:227], v[68:71]\n"
    "ds_read_b64_tr_b16 a[130:131], v242 offset:53248\n"
    "v_mfma_f32_16x16x32_bf16 v[68:71], a[140:143], v[228:231], v[68:71]\n"
    "s_waitcnt lgkmcnt(5)\n"
    "v_mfma_f32_16x16x32_bf16 a[4:7], a[144:147], v[208:211], a[4:7]\n"
    "v_mfma_f32_16x16x32_bf16 a[4:7], a[144:147], v[212:215], a[4:7]\n"
    "v_mfma_f32_16x16x32_bf16 v[152:155], a[80:83], a[16:19], v[136:139]\n"
    "v_exp_f32 v164, v164\n"
    "v_mfma_f32_16x16x32_bf16 v[152:155], a[84:87], a[20:23], v[152:155]\n"
    "v_exp_f32 v165, v165\n"
    "v_mfma_f32_16x16x32_bf16 v[152:155], a[88:91], a[24:27], v[152:155]\n"
    "v_exp_f32 v166, v166\n"
    "v_mfma_f32_16x16x32_bf16 v[152:155], a[92:95], a[28:31], v[152:155]\n"
    "v_exp_f32 v167, v167\n"
    "v_mfma_f32_16x16x32_bf16 v[72:75], a[112:115], v[200:203], v[72:75]\n"
    "ds_read_b128 a[100:103], v233 offset:12288\n"
    "v_cvt_pk_bf16_f32 v194, v164, v165\n"
    "v_mfma_f32_16x16x32_bf16 v[72:75], a[116:119], v[204:207], v[72:75]\n"
    "ds_read_b64_tr_b16 a[132:133], v242 offset:57344\n"
    "v_cvt_pk_bf16_f32 v195, v166, v167\n"
    "v_mfma_f32_16x16x32_bf16 v[76:79], a[112:115], v[208:211], v[76:79]\n"
    "ds_read_b64_tr_b16 a[134:135], v242 offset:61440\n"
    "v_mfma_f32_16x16x32_bf16 v[76:79], a[116:119], v[212:215], v[76:79]\n"
    "buffer_store_dwordx4 a[216:219], v252, s[76:79], s80 offen sc1\n"
    "s_add_u32 s80, s80, 1024\n"
    "v_mfma_f32_16x16x32_bf16 v[156:159], a[80:83], a[32:35], v[140:143]\n"
    "v_exp_f32 v152, v152\n"
    "v_mfma_f32_16x16x32_bf16 v[156:159], a[84:87], a[36:39], v[156:159]\n"
    "v_exp_f32 v153, v153\n"
    "v_mfma_f32_16x16x32_bf16 v[156:159], a[88:91], a[40:43], v[156:159]\n"
    "v_exp_f32 v154, v154\n"
    "v_mfma_f32_16x16x32_bf16 v[156:159], a[92:95], a[44:47], v[156:159]\n"
    "v_exp_f32 v155, v155\n"
    "v_mfma_f32_16x16x32_bf16 v[80:83], a[112:115], v[216:219], v[80:83]\n"
    "ds_read_b128 a[104:107], v234 offset:12288\n"
    "v_cvt_pk_bf16_f32 v172, v152, v153\n"
    "v_mfma_f32_16x16x32_bf16 v[80:83], a[116:119], v[220:223], v[80:83]\n"
    "ds_read_b64_tr_b16 a[136:137], v243 offset:49152\n"
    "v_cvt_pk_bf16_f32 v173, v154, v155\n"
    "v_mfma_f32_16x16x32_bf16 v[84:87], a[112:115], v[224:227], v[84:87]\n"
    "ds_read_b64_tr_b16 a[138:139], v243 offset:53248\n"
    "v_mfma_f32_16x16x32_bf16 v[84:87], a[116:119], v[228:231], v[84:87]\n"
    "s_waitcnt lgkmcnt(5)\n"
    "v_mfma_f32_16x16x32_bf16 v[160:163], a[80:83], a[48:51], v[144:147]\n"
    "v_exp_f32 v156, v156\n"
    "v_mfma_f32_16x16x32_bf16 v[160:163], a[84:87], a[52:55], v[160:163]\n"
    "v_exp_f32 v157, v157\n"
    "v_mfma_f32_16x16x32_bf16 v[160:163], a[88:91], a[56:59], v[160:163]\n"
    "v_exp_f32 v158, v158\n"
    "v_mfma_f32_16x16x32_bf16 v[160:163], a[92:95], a[60:63], v[160:163]\n"
    "v_exp_f32 v159, v159\n"
    "v_mfma_f32_16x16x32_bf16 v[88:91], a[120:123], v[200:203], v[88:91]\n"
    "ds_read_b128 a[108:111], v235 offset:12288\n"
    "v_cvt_pk_bf16_f32 v180, v156, v157\n"
    "v_mfma_f32_16x16x32_bf16 v[88:91], a[124:127], v[204:207], v[88:91]\n"
    "ds_read_b64_tr_b16 a[140:141], v243 offset:57344\n"
    "v_cvt_pk_bf16_f32 v181, v158, v159\n"
    "v_mfma_f32_16x16x32_bf16 v[92:95], a[120:123], v[208:211], v[92:95]\n"
    "ds_read_b64_tr_b16 a[142:143], v243 offset:61440\n"
    "v_mfma_f32_16x16x32_bf16 v[92:95], a[124:127], v[212:215], v[92:95]\n"
    "v_mfma_f32_16x16x32_bf16 v[164:167], a[80:83], a[64:67], v[148:151]\n"
    "v_exp_f32 v160, v160\n"
    "v_mfma_f32_16x16x32_bf16 v[164:167], a[84:87], a[68:71], v[164:167]\n"
    "v_exp_f32 v161, v161\n"
    "v_mfma_f32_16x16x32_bf16 v[164:167], a[88:91], a[72:75], v[164:167]\n"
    "v_exp_f32 v162, v162\n"
    "v_mfma_f32_16x16x32_bf16 v[164:167], a[92:95], a[76:79], v[164:167]\n"
    "s_waitcnt vmcnt(5) lgkmcnt(0)\n"
    "s_barrier\n"
    "v_exp_f32 v163, v163\n"
    "v_mfma_f32_16x16x32_bf16 v[96:99], a[120:123], v[216:219], v[96:99]\n"
    "ds_read_b128 a[80:83], v232 offset:16384\n"
    "v_cvt_pk_bf16_f32 v188, v160, v161\n"
    "v_mfma_f32_16x16x32_bf16 v[96:99], a[124:127], v[220:223], v[96:99]\n"
    "ds_read_b64_tr_b16 a[112:113], v236 offset:32768\n"
    "v_cvt_pk_bf16_f32 v189, v162, v163\n"
    "v_mfma_f32_16x16x32_bf16 v[100:103], a[120:123], v[224:227], v[100:103]\n"
    "ds_read_b64_tr_b16 a[114:115], v236 offset:36864\n"
    "s_add_u32 m0, s84, 0\n"
    "v_mfma_f32_16x16x32_bf16 v[100:103], a[124:127], v[228:231], v[100:103]\n"
    "s_waitcnt lgkmcnt(5)\n"
    "buffer_load_dwordx4 v244, s[56:59], s82 offen lds\n"
    "v_mfma_f32_16x16x32_bf16 a[8:11], a[144:147], v[216:219], a[8:11]\n"
    "buffer_load_dwordx4 v245, s[56:59], s82 offen offset:1024 lds\n"
    "v_mfma_f32_16x16x32_bf16 a[8:11], a[144:147], v[220:223], a[8:11]\n"
    "buffer_load_dwordx4 v246, s[56:59], s82 offen offset:2048 lds\n"
    "v_mfma_f32_16x16x32_bf16 v[152:155], a[96:99], a[16:19], v[136:139]\n"
    "v_exp_f32 v164, v164\n"
    "v_mfma_f32_16x16x32_bf16 v[152:155], a[100:103], a[20:23], v[152:155]\n"
    "v_exp_f32 v165, v165\n"
    "v_mfma_f32_16x16x32_bf16 v[152:155], a[104:107], a[24:27], v[152:155]\n"
    "v_exp_f32 v166, v166\n"
    "v_mfma_f32_16x16x32_bf16 v[152:155], a[108:111], a[28:31], v[152:155]\n"
    "v_exp_f32 v167, v167\n"
    "v_mfma_f32_16x16x32_bf16 v[104:107], a[128:131], v[200:203], v[104:107]\n"
    "ds_read_b128 a[84:87], v233 offset:16384\n"
    "v_cvt_pk_bf16_f32 v196, v164, v165\n"
    "v_mfma_f32_16x16x32_bf16 v[104:107], a[132:135], v[204:207], v[104:107]\n"
    "ds_read_b64_tr_b16 a[116:117], v236 offset:40960\n"
    "v_cvt_pk_bf16_f32 v197, v166, v167\n"
    "v_mfma_f32_16x16x32_bf16 v[108:111], a[128:131], v[208:211], v[108:111]\n"
    "ds_read_b64_tr_b16 a[118:119], v236 offset:45056\n"
    "v_mfma_f32_16x16x32_bf16 v[108:111], a[132:135], v[212:215], v[108:111]\n"
    "buffer_load_dwordx4 v247, s[56:59], s82 offen offset:3072 lds\n"
    "v_mfma_f32_16x16x32_bf16 v[156:159], a[96:99], a[32:35], v[140:143]\n"
    "v_exp_f32 v152, v152\n"
    "v_mfma_f32_16x16x32_bf16 v[156:159], a[100:103], a[36:39], v[156:159]\n"
    "v_exp_f32 v153, v153\n"
    "v_mfma_f32_16x16x32_bf16 v[156:159], a[104:107], a[40:43], v[156:159]\n"
    "v_exp_f32 v154, v154\n"
    "v_mfma_f32_16x16x32_bf16 v[156:159], a[108:111], a[44:47], v[156:159]\n"
    "v_exp_f32 v155, v155\n"
    "v_mfma_f32_16x16x32_bf16 v[112:115], a[128:131], v[216:219], v[112:115]\n"
    "ds_read_b128 a[88:91], v234 offset:16384\n"
    "v_cvt_pk_bf16_f32 v174, v152, v153\n"
    "v_mfma_f32_16x16x32_bf16 v[112:115], a[132:135], v[220:223], v[112:115]\n"
    "ds_read_b64_tr_b16 a[120:121], v237 offset:32768\n"
    "v_cvt_pk_bf16_f32 v175, v154, v155\n"
    "v_mfma_f32_16x16x32_bf16 v[116:119], a[128:131], v[224:227], v[116:119]\n"
    "ds_read_b64_tr_b16 a[122:123], v237 offset:36864\n"
    "s_add_u32 m0, s84, 49152\n"
    "v_mfma_f32_16x16x32_bf16 v[116:119], a[132:135], v[228:231], v[116:119]\n"
    "s_waitcnt lgkmcnt(5)\n"
    "buffer_load_dwordx4 v248, s[60:63], s83 offen lds\n"
    "v_mfma_f32_16x16x32_bf16 v[160:163], a[96:99], a[48:51], v[144:147]\n"
    "v_exp_f32 v156, v156\n"
    "v_mfma_f32_16x16x32_bf16 v[160:163], a[100:103], a[52:55], v[160:163]\n"
    "v_exp_f32 v157, v157\n"
    "v_mfma_f32_16x16x32_bf16 v[160:163], a[104:107], a[56:59], v[160:163]\n"
    "v_exp_f32 v158, v158\n"
    "v_mfma_f32_16x16x32_bf16 v[160:163], a[108:111], a[60:63], v[160:163]\n"
    "v_exp_f32 v159, v159\n"
    "v_mfma_f32_16x16x32_bf16 v[120:123], a[136:139], v[200:203], v[120:123]\n"
    "ds_read_b128 a[92:95], v235 offset:16384\n"
    "v_cvt_pk_bf16_f32 v182, v156, v157\n"
    "v_mfma_f32_16x16x32_bf16 v[120:123], a[140:143], v[204:207], v[120:123]\n"
    "ds_read_b64_tr_b16 a[124:125], v237 offset:40960\n"
    "v_cvt_pk_bf16_f32 v183, v158, v159\n"
    "v_mfma_f32_16x16x32_bf16 v[124:127], a[136:139], v[208:211], v[124:127]\n"
    "ds_read_b64_tr_b16 a[126:127], v237 offset:45056\n"
    "v_mfma_f32_16x16x32_bf16 v[124:127], a[140:143], v[212:215], v[124:127]\n"
    "buffer_load_dwordx4 v249, s[60:63], s83 offen offset:1024 lds\n"
    "v_mfma_f32_16x16x32_bf16 v[164:167], a[96:99], a[64:67], v[148:151]\n"
    "v_exp_f32 v160, v160\n"
    "v_mfma_f32_16x16x32_bf16 v[164:167], a[100:103], a[68:71], v[164:167]\n"
    "v_exp_f32 v161, v161\n"
    "v_mfma_f32_16x16x32_bf16 v[164:167], a[104:107], a[72:75], v[164:167]\n"
    "v_exp_f32 v162, v162\n"
    "v_mfma_f32_16x16x32_bf16 v[164:167], a[108:111], a[76:79], v[164:167]\n"
    "v_exp_f32 v163, v163\n"
    "v_mfma_f32_16x16x32_bf16 v[128:131], a[136:139], v[216:219], v[128:131]\n"
    "ds_read_b128 a[96:99], v232 offset:20480\n"
    "v_cvt_pk_bf16_f32 v190, v160, v161\n"
    "v_mfma_f32_16x16x32_bf16 v[128:131], a[140:143], v[220:223], v[128:131]\n"
    "ds_read_b64_tr_b16 a[128:129], v238 offset:32768\n"
    "v_cvt_pk_bf16_f32 v191, v162, v163\n"
    "v_mfma_f32_16x16x32_bf16 v[132:135], a[136:139], v[224:227], v[132:135]\n"
    "ds_read_b64_tr_b16 a[130:131], v238 offset:36864\n"
    "v_mfma_f32_16x16x32_bf16 v[132:135], a[140:143], v[228:231], v[132:135]\n"
    "s_waitcnt lgkmcnt(5)\n"
    "buffer_load_dwordx4 v248, s[60:63], s83 offen offset:2048 lds\n"
    "v_mfma_f32_16x16x32_bf16 a[12:15], a[144:147], v[224:227], a[12:15]\n"
    "buffer_load_dwordx4 v249, s[60:63], s83 offen offset:3072 lds\n"
    "v_mfma_f32_16x16x32_bf16 a[12:15], a[144:147], v[228:231], a[12:15]\n"
    "s_add_u32 s82, s82, 16384\n"
    "s_add_u32 s83, s83, 16384\n"
    "L_attn_mid_%=:\n"
    "v_mfma_f32_16x16x32_bf16 v[152:155], a[80:83], a[16:19], v[136:139]\n"
    "v_exp_f32 v164, v164\n"
    "v_mfma_f32_16x16x32_bf16 v[152:155], a[84:87], a[20:23], v[152:155]\n"
    "v_exp_f32 v165, v165\n"
    "v_mfma_f32_16x16x32_bf16 v[152:155], a[88:91], a[24:27], v[152:155]\n"
    "v_exp_f32 v166, v166\n"
    "v_mfma_f32_16x16x32_bf16 v[152:155], a[92:95], a[28:31], v[152:155]\n"
    "v_exp_f32 v167, v167\n"
    "v_mfma_f32_16x16x32_bf16 v[8:11], a[112:115], v[168:171], v[8:11]\n"
    "ds_read_b128 a[100:103], v233 offset:20480\n"
    "v_cvt_pk_bf16_f32 v198, v164, v165\n"
    "v_mfma_f32_16x16x32_bf16 v[8:11], a[116:119], v[172:175], v[8:11]\n"
    "ds_read_b64_tr_b16 a[132:133], v238 offset:40960\n"
    "v_cvt_pk_bf16_f32 v199, v166, v167\n"
    "v_mfma_f32_16x16x32_bf16 v[12:15], a[112:115], v[176:179], v[12:15]\n"
    "ds_read_b64_tr_b16 a[134:135], v238 offset:45056\n"
    "v_mfma_f32_16x16x32_bf16 v[12:15], a[116:119], v[180:183], v[12:15]\n"
    "v_mfma_f32_16x16x32_bf16 v[156:159], a[80:83], a[32:35], v[140:143]\n"
    "v_exp_f32 v152, v152\n"
    "v_mfma_f32_16x16x32_bf16 v[156:159], a[84:87], a[36:39], v[156:159]\n"
    "v_exp_f32 v153, v153\n"
    "v_mfma_f32_16x16x32_bf16 v[156:159], a[88:91], a[40:43], v[156:159]\n"
    "v_exp_f32 v154, v154\n"
    "v_mfma_f32_16x16x32_bf16 v[156:159], a[92:95], a[44:47], v[156:159]\n"
    "v_exp_f32 v155, v155\n"
    "v_mfma_f32_16x16x32_bf16 v[16:19], a[112:115], v[184:187], v[16:19]\n"
    "ds_read_b128 a[104:107], v234 offset:20480\n"
    "v_cvt_pk_bf16_f32 v200, v152, v153\n"
    "v_mfma_f32_16x16x32_bf16 v[16:19], a[116:119], v[188:191], v[16:19]\n"
    "ds_read_b64_tr_b16 a[136:137], v239 offset:32768\n"
    "v_cvt_pk_bf16_f32 v201, v154, v155\n"
    "v_mfma_f32_16x16x32_bf16 v[20:23], a[112:115], v[192:195], v[20:23]\n"
    "ds_read_b64_tr_b16 a[138:139], v239 offset:36864\n"
    "v_mfma_f32_16x16x32_bf16 v[20:23], a[116:119], v[196:199], v[20:23]\n"
    "s_waitcnt lgkmcnt(5)\n"
    "v_mfma_f32_16x16x32_bf16 v[160:163], a[80:83], a[48:51], v[144:147]\n"
    "v_exp_f32 v156, v156\n"
    "v_mfma_f32_16x16x32_bf16 v[160:163], a[84:87], a[52:55], v[160:163]\n"
    "v_exp_f32 v157, v157\n"
    "v_mfma_f32_16x16x32_bf16 v[160:163], a[88:91], a[56:59], v[160:163]\n"
    "v_exp_f32 v158, v158\n"
    "v_mfma_f32_16x16x32_bf16 v[160:163], a[92:95], a[60:63], v[160:163]\n"
    "v_exp_f32 v159, v159\n"
    "v_mfma_f32_16x16x32_bf16 v[24:27], a[120:123], v[168:171], v[24:27]\n"
    "ds_read_b128 a[108:111], v235 offset:20480\n"
    "v_cvt_pk_bf16_f32 v208, v156, v157\n"
    "v_mfma_f32_16x16x32_bf16 v[24:27], a[124:127], v[172:175], v[24:27]\n"
    "ds_read_b64_tr_b16 a[140:141], v239 offset:40960\n"
    "v_cvt_pk_bf16_f32 v209, v158, v159\n"
    "v_mfma_f32_16x16x32_bf16 v[28:31], a[120:123], v[176:179], v[28:31]\n"
    "ds_read_b64_tr_b16 a[142:143], v239 offset:45056\n"
    "v_mfma_f32_16x16x32_bf16 v[28:31], a[124:127], v[180:183], v[28:31]\n"
    "v_mfma_f32_16x16x32_bf16 v[164:167], a[80:83], a[64:67], v[148:151]\n"
    "v_exp_f32 v160, v160\n"
    "v_mfma_f32_16x16x32_bf16 v[164:167], a[84:87], a[68:71], v[164:167]\n"
    "v_exp_f32 v161, v161\n"
    "v_mfma_f32_16x16x32_bf16 v[164:167], a[88:91], a[72:75], v[164:167]\n"
    "v_exp_f32 v162, v162\n"
    "v_mfma_f32_16x16x32_bf16 v[164:167], a[92:95], a[76:79], v[164:167]\n"
    "v_exp_f32 v163, v163\n"
    "v_mfma_f32_16x16x32_bf16 v[32:35], a[120:123], v[184:187], v[32:35]\n"
    "ds_read_b128 a[80:83], v232 offset:24576\n"
    "v_cvt_pk_bf16_f32 v216, v160, v161\n"
    "v_mfma_f32_16x16x32_bf16 v[32:35], a[124:127], v[188:191], v[32:35]\n"
    "ds_read_b64_tr_b16 a[112:113], v240 offset:32768\n"
    "v_cvt_pk_bf16_f32 v217, v162, v163\n"
    "v_mfma_f32_16x16x32_bf16 v[36:39], a[120:123], v[192:195], v[36:39]\n"
    "ds_read_b64_tr_b16 a[114:115], v240 offset:36864\n"
    "v_mfma_f32_16x16x32_bf16 v[36:39], a[124:127], v[196:199], v[36:39]\n"
    "s_waitcnt lgkmcnt(5)\n"
    "v_mfma_f32_16x16x32_bf16 a[0:3], a[144:147], v[168:171], a[0:3]\n"
    "v_mfma_f32_16x16x32_bf16 a[0:3], a[144:147], v[172:175], a[0:3]\n"
    "v_mfma_f32_16x16x32_bf16 v[152:155], a[96:99], a[16:19], v[136:139]\n"
    "v_exp_f32 v164, v164\n"
    "v_mfma_f32_16x16x32_bf16 v[152:155], a[100:103], a[20:23], v[152:155]\n"
    "v_exp_f32 v165, v165\n"
    "v_mfma_f32_16x16x32_bf16 v[152:155], a[104:107], a[24:27], v[152:155]\n"
    "v_exp_f32 v166, v166\n"
    "v_mfma_f32_16x16x32_bf16 v[152:155], a[108:111], a[28:31], v[152:155]\n"
    "v_exp_f32 v167, v167\n"
    "v_mfma_f32_16x16x32_bf16 v[40:43], a[128:131], v[168:171], v[40:43]\n"
    "ds_read_b128 a[84:87], v233 offset:24576\n"
    "v_cvt_pk_bf16_f32 v224, v164, v165\n"
    "v_mfma_f32_16x16x32_bf16 v[40:43], a[132:135], v[172:175], v[40:43]\n"
    "ds_read_b64_tr_b16 a[116:117], v240 offset:40960\n"
    "v_cvt_pk_bf16_f32 v225, v166, v167\n"
    "v_mfma_f32_16x16x32_bf16 v[44:47], a[128:131], v[176:179], v[44:47]\n"
    "ds_read_b64_tr_b16 a[118:119], v240 offset:45056\n"
    "v_mfma_f32_16x16x32_bf16 v[44:47], a[132:135], v[180:183], v[44:47]\n"
    "v_mfma_f32_16x16x32_bf16 v[156:159], a[96:99], a[32:35], v[140:143]\n"
    "v_exp_f32 v152, v152\n"
    "v_mfma_f32_16x16x32_bf16 v[156:159], a[100:103], a[36:39], v[156:159]\n"
    "v_exp_f32 v153, v153\n"
    "v_mfma_f32_16x16x32_bf16 v[156:159], a[104:107], a[40:43], v[156:159]\n"
    "v_exp_f32 v154, v154\n"
    "v_mfma_f32_16x16x32_bf16 v[156:159], a[108:111], a[44:47], v[156:159]\n"
    "v_exp_f32 v155, v155\n"
    "v_mfma_f32_16x16x32_bf16 v[48:51], a[128:131], v[184:187], v[48:51]\n"
    "ds_read_b128 a[88:91], v234 offset:24576\n"
    "v_cvt_pk_bf16_f32 v202, v152, v153\n"
    "v_mfma_f32_16x16x32_bf16 v[48:51], a[132:135], v[188:191], v[48:51]\n"
    "ds_read_b64_tr_b16 a[120:121], v241 offset:32768\n"
    "v_cvt_pk_bf16_f32 v203, v154, v155\n"
    "v_mfma_f32_16x16x32_bf16 v[52:55], a[128:131], v[192:195], v[52:55]\n"
    "ds_read_b64_tr_b16 a[122:123], v241 offset:36864\n"
    "v_mfma_f32_16x16x32_bf16 v[52:55], a[132:135], v[196:199], v[52:55]\n"
    "s_waitcnt lgkmcnt(5)\n"
    "v_mfma_f32_16x16x32_bf16 v[160:163], a[96:99], a[48:51], v[144:147]\n"
    "v_exp_f32 v156, v156\n"
    "v_mfma_f32_16x16x32_bf16 v[160:163], a[100:103], a[52:55], v[160:163]\n"
    "v_exp_f32 v157, v157\n"
    "v_mfma_f32_16x16x32_bf16 v[160:163], a[104:107], a[56:59], v[160:163]\n"
    "v_exp_f32 v158, v158\n"
    "v_mfma_f32_16x16x32_bf16 v[160:163], a[108:111], a[60:63], v[160:163]\n"
    "v_exp_f32 v159, v159\n"
    "v_mfma_f32_16x16x32_bf16 v[56:59], a[136:139], v[168:171], v[56:59]\n"
    "ds_read_b128 a[92:95], v235 offset:24576\n"
    "v_cvt_pk_bf16_f32 v210, v156, v157\n"
    "v_mfma_f32_16x16x32_bf16 v[56:59], a[140:143], v[172:175], v[56:59]\n"
    "ds_read_b64_tr_b16 a[124:125], v241 offset:40960\n"
    "v_cvt_pk_bf16_f32 v211, v158, v159\n"
    "v_mfma_f32_16x16x32_bf16 v[60:63], a[136:139], v[176:179], v[60:63]\n"
    "ds_read_b64_tr_b16 a[126:127], v241 offset:45056\n"
    "v_mfma_f32_16x16x32_bf16 v[60:63], a[140:143], v[180:183], v[60:63]\n"
    "v_mfma_f32_16x16x32_bf16 v[164:167], a[96:99], a[64:67], v[148:151]\n"
    "v_exp_f32 v160, v160\n"
    "v_mfma_f32_16x16x32_bf16 v[164:167], a[100:103], a[68:71], v[164:167]\n"
    "v_exp_f32 v161, v161\n"
    "v_mfma_f32_16x16x32_bf16 v[164:167], a[104:107], a[72:75], v[164:167]\n"
    "v_exp_f32 v162, v162\n"
    "v_mfma_f32_16x16x32_bf16 v[164:167], a[108:111], a[76:79], v[164:167]\n"
    "v_exp_f32 v163, v163\n"
    "v_mfma_f32_16x16x32_bf16 v[64:67], a[136:139], v[184:187], v[64:67]\n"
    "ds_read_b128 a[96:99], v232 offset:28672\n"
    "v_cvt_pk_bf16_f32 v218, v160, v161\n"
    "v_mfma_f32_16x16x32_bf16 v[64:67], a[140:143], v[188:191], v[64:67]\n"
    "ds_read_b64_tr_b16 a[128:129], v242 offset:32768\n"
    "v_cvt_pk_bf16_f32 v219, v162, v163\n"
    "v_mfma_f32_16x16x32_bf16 v[68:71], a[136:139], v[192:195], v[68:71]\n"
    "ds_read_b64_tr_b16 a[130:131], v242 offset:36864\n"
    "v_mfma_f32_16x16x32_bf16 v[68:71], a[140:143], v[196:199], v[68:71]\n"
    "s_waitcnt lgkmcnt(5)\n"
    "v_mfma_f32_16x16x32_bf16 a[4:7], a[144:147], v[176:179], a[4:7]\n"
    "v_mfma_f32_16x16x32_bf16 a[4:7], a[144:147], v[180:183], a[4:7]\n"
    "v_mfma_f32_16x16x32_bf16 v[152:155], a[80:83], a[16:19], v[136:139]\n"
    "v_exp_f32 v164, v164\n"
    "v_mfma_f32_16x16x32_bf16 v[152:155], a[84:87], a[20:23], v[152:155]\n"
    "v_exp_f32 v165, v165\n"
    "v_mfma_f32_16x16x32_bf16 v[152:155], a[88:91], a[24:27], v[152:155]\n"
    "v_exp_f32 v166, v166\n"
    "v_mfma_f32_16x16x32_bf16 v[152:155], a[92:95], a[28:31], v[152:155]\n"
    "v_exp_f32 v167, v167\n"
    "v_mfma_f32_16x16x32_bf16 v[72:75], a[112:115], v[168:171], v[72:75]\n"
    "ds_read_b128 a[100:103], v233 offset:28672\n"
    "v_cvt_pk_bf16_f32 v226, v164, v165\n"
    "v_mfma_f32_16x16x32_bf16 v[72:75], a[116:119], v[172:175], v[72:75]\n"
    "ds_read_b64_tr_b16 a[132:133], v242 offset:40960\n"
    "v_cvt_pk_bf16_f32 v227, v166, v167\n"
    "v_mfma_f32_16x16x32_bf16 v[76:79], a[112:115], v[176:179], v[76:79]\n"
    "ds_read_b64_tr_b16 a[134:135], v242 offset:45056\n"
    "v_mfma_f32_16x16x32_bf16 v[76:79], a[116:119], v[180:183], v[76:79]\n"
    "v_mfma_f32_16x16x32_bf16 v[156:159], a[80:83], a[32:35], v[140:143]\n"
    "v_exp_f32 v152, v152\n"
    "v_mfma_f32_16x16x32_bf16 v[156:159], a[84:87], a[36:39], v[156:159]\n"
    "v_exp_f32 v153, v153\n"
    "v_mfma_f32_16x16x32_bf16 v[156:159], a[88:91], a[40:43], v[156:159]\n"
    "v_exp_f32 v154, v154\n"
    "v_mfma_f32_16x16x32_bf16 v[156:159], a[92:95], a[44:47], v[156:159]\n"
    "v_exp_f32 v155, v155\n"
    "v_mfma_f32_16x16x32_bf16 v[80:83], a[112:115], v[184:187], v[80:83]\n"
    "ds_read_b128 a[104:107], v234 offset:28672\n"
    "v_cvt_pk_bf16_f32 v204, v152, v153\n"
    "v_mfma_f32_16x16x32_bf16 v[80:83], a[116:119], v[188:191], v[80:83]\n"
    "ds_read_b64_tr_b16 a[136:137], v243 offset:32768\n"
    "v_cvt_pk_bf16_f32 v205, v154, v155\n"
    "v_mfma_f32_16x16x32_bf16 v[84:87], a[112:115], v[192:195], v[84:87]\n"
    "ds_read_b64_tr_b16 a[138:139], v243 offset:36864\n"
    "v_mfma_f32_16x16x32_bf16 v[84:87], a[116:119], v[196:199], v[84:87]\n"
    "s_waitcnt lgkmcnt(5)\n"
    "v_mfma_f32_16x16x32_bf16 v[160:163], a[80:83], a[48:51], v[144:147]\n"
    "v_exp_f32 v156, v156\n"
    "v_mfma_f32_16x16x32_bf16 v[160:163], a[84:87], a[52:55], v[160:163]\n"
    "v_exp_f32 v157, v157\n"
    "v_mfma_f32_16x16x32_bf16 v[160:163], a[88:91], a[56:59], v[160:163]\n"
    "v_exp_f32 v158, v158\n"
    "v_mfma_f32_16x16x32_bf16 v[160:163], a[92:95], a[60:63], v[160:163]\n"
    "v_exp_f32 v159, v159\n"
    "v_mfma_f32_16x16x32_bf16 v[88:91], a[120:123], v[168:171], v[88:91]\n"
    "ds_read_b128 a[108:111], v235 offset:28672\n"
    "v_cvt_pk_bf16_f32 v212, v156, v157\n"
    "v_mfma_f32_16x16x32_bf16 v[88:91], a[124:127], v[172:175], v[88:91]\n"
    "ds_read_b64_tr_b16 a[140:141], v243 offset:40960\n"
    "v_cvt_pk_bf16_f32 v213, v158, v159\n"
    "v_mfma_f32_16x16x32_bf16 v[92:95], a[120:123], v[176:179], v[92:95]\n"
    "ds_read_b64_tr_b16 a[142:143], v243 offset:45056\n"
    "v_mfma_f32_16x16x32_bf16 v[92:95], a[124:127], v[180:183], v[92:95]\n"
    "v_mfma_f32_16x16x32_bf16 v[164:167], a[80:83], a[64:67], v[148:151]\n"
    "v_exp_f32 v160, v160\n"
    "v_mfma_f32_16x16x32_bf16 v[164:167], a[84:87], a[68:71], v[164:167]\n"
    "v_exp_f32 v161, v161\n"
    "v_mfma_f32_16x16x32_bf16 v[164:167], a[88:91], a[72:75], v[164:167]\n"
    "v_exp_f32 v162, v162\n"
    "v_mfma_f32_16x16x32_bf16 v[164:167], a[92:95], a[76:79], v[164:167]\n"
    "s_waitcnt vmcnt(0) lgkmcnt(0)\n"
    "s_barrier\n"
    "v_exp_f32 v163, v163\n"
    "v_mfma_f32_16x16x32_bf16 v[96:99], a[120:123], v[184:187], v[96:99]\n"
    "s_cmp_eq_u32 s71, s72\n"
    "s_cbranch_scc1 L_evt_%=\n"
    "L_evt_ret_%=:\n"
    "ds_read_b128 a[80:83], v232 offset:0\n"
    "v_cvt_pk_bf16_f32 v220, v160, v161\n"
    "v_mfma_f32_16x16x32_bf16 v[96:99], a[124:127], v[188:191], v[96:99]\n"
    "ds_read_b64_tr_b16 a[112:113], v236 offset:49152\n"
    "v_cvt_pk_bf16_f32 v221, v162, v163\n"
    "v_mfma_f32_16x16x32_bf16 v[100:103], a[120:123], v[192:195], v[100:103]\n"
    "ds_read_b64_tr_b16 a[114:115], v236 offset:53248\n"
    "s_add_u32 m0, s84, 16384\n"
    "v_mfma_f32_16x16x32_bf16 v[100:103], a[124:127], v[196:199], v[100:103]\n"
    "s_waitcnt lgkmcnt(5)\n"
    "buffer_load_dwordx4 v244, s[56:59], s82 offen lds\n"
    "v_mfma_f32_16x16x32_bf16 a[8:11], a[144:147], v[184:187], a[8:11]\n"
    "buffer_load_dwordx4 v245, s[56:59], s82 offen offset:1024 lds\n"
    "v_mfma_f32_16x16x32_bf16 a[8:11], a[144:147], v[188:191], a[8:11]\n"
    "buffer_load_dwordx4 v246, s[56:59], s82 offen offset:2048 lds\n"
    "v_mfma_f32_16x16x32_bf16 v[152:155], a[96:99], a[16:19], v[136:139]\n"
    "v_exp_f32 v164, v164\n"
    "v_mfma_f32_16x16x32_bf16 v[152:155], a[100:103], a[20:23], v[152:155]\n"
    "v_exp_f32 v165, v165\n"
    "v_mfma_f32_16x16x32_bf16 v[152:155], a[104:107], a[24:27], v[152:155]\n"
    "v_exp_f32 v166, v166\n"
    "v_mfma_f32_16x16x32_bf16 v[152:155], a[108:111], a[28:31], v[152:155]\n"
    "v_exp_f32 v167, v167\n"
    "v_mfma_f32_16x16x32_bf16 v[104:107], a[128:131], v[168:171], v[104:107]\n"
    "ds_read_b128 a[84:87], v233 offset:0\n"
    "v_cvt_pk_bf16_f32 v228, v164, v165\n"
    "v_mfma_f32_16x16x32_bf16 v[104:107], a[132:135], v[172:175], v[104:107]\n"
    "ds_read_b64_tr_b16 a[116:117], v236 offset:57344\n"
    "v_cvt_pk_bf16_f32 v229, v166, v167\n"
    "v_mfma_f32_16x16x32_bf16 v[108:111], a[128:131], v[176:179], v[108:111]\n"
    "ds_read_b64_tr_b16 a[118:119], v236 offset:61440\n"
    "v_mfma_f32_16x16x32_bf16 v[108:111], a[132:135], v[180:183], v[108:111]\n"
    "buffer_load_dwordx4 v247, s[56:59], s82 offen offset:3072 lds\n"
    "v_mfma_f32_16x16x32_bf16 v[156:159], a[96:99], a[32:35], v[140:143]\n"
    "v_exp_f32 v152, v152\n"
    "v_mfma_f32_16x16x32_bf16 v[156:159], a[100:103], a[36:39], v[156:159]\n"
    "v_exp_f32 v153, v153\n"
    "v_mfma_f32_16x16x32_bf16 v[156:159], a[104:107], a[40:43], v[156:159]\n"
    "v_exp_f32 v154, v154\n"
    "v_mfma_f32_16x16x32_bf16 v[156:159], a[108:111], a[44:47], v[156:159]\n"
    "v_exp_f32 v155, v155\n"
    "v_mfma_f32_16x16x32_bf16 v[112:115], a[128:131], v[184:187], v[112:115]\n"
    "ds_read_b128 a[88:91], v234 offset:0\n"
    "v_cvt_pk_bf16_f32 v206, v152, v153\n"
    "v_mfma_f32_16x16x32_bf16 v[112:115], a[132:135], v[188:191], v[112:115]\n"
    "ds_read_b64_tr_b16 a[120:121], v237 offset:49152\n"
    "v_cvt_pk_bf16_f32 v207, v154, v155\n"
    "v_mfma_f32_16x16x32_bf16 v[116:119], a[128:131], v[192:195], v[116:119]\n"
    "ds_read_b64_tr_b16 a[122:123], v237 offset:53248\n"
    "s_add_u32 m0, s84, 32768\n"
    "v_mfma_f32_16x16x32_bf16 v[116:119], a[132:135], v[196:199], v[116:119]\n"
    "s_waitcnt lgkmcnt(5)\n"
    "buffer_load_dwordx4 v248, s[60:63], s83 offen lds\n"
    "v_mfma_f32_16x16x32_bf16 v[160:163], a[96:99], a[48:51], v[144:147]\n"
    "s_sub_u32 s71, s71, 1\n"
    "v_exp_f32 v156, v156\n"
    "v_mfma_f32_16x16x32_bf16 v[160:163], a[100:103], a[52:55], v[160:163]\n"
    "v_exp_f32 v157, v157\n"
    "v_mfma_f32_16x16x32_bf16 v[160:163], a[104:107], a[56:59], v[160:163]\n"
    "v_exp_f32 v158, v158\n"
    "v_mfma_f32_16x16x32_bf16 v[160:163], a[108:111], a[60:63], v[160:163]\n"
    "v_exp_f32 v159, v159\n"
    "v_mfma_f32_16x16x32_bf16 v[120:123], a[136:139], v[168:171], v[120:123]\n"
    "ds_read_b128 a[92:95], v235 offset:0\n"
    "v_cvt_pk_bf16_f32 v214, v156, v157\n"
    "v_mfma_f32_16x16x32_bf16 v[120:123], a[140:143], v[172:175], v[120:123]\n"
    "ds_read_b64_tr_b16 a[124:125], v237 offset:57344\n"
    "v_cvt_pk_bf16_f32 v215, v158, v159\n"
    "v_mfma_f32_16x16x32_bf16 v[124:127], a[136:139], v[176:179], v[124:127]\n"
    "ds_read_b64_tr_b16 a[126:127], v237 offset:61440\n"
    "v_mfma_f32_16x16x32_bf16 v[124:127], a[140:143], v[180:183], v[124:127]\n"
    "buffer_load_dwordx4 v249, s[60:63], s83 offen offset:1024 lds\n"
    "v_mfma_f32_16x16x32_bf16 v[164:167], a[96:99], a[64:67], v[148:151]\n"
    "v_exp_f32 v160, v160\n"
    "v_mfma_f32_16x16x32_bf16 v[164:167], a[100:103], a[68:71], v[164:167]\n"
    "v_exp_f32 v161, v161\n"
    "v_mfma_f32_16x16x32_bf16 v[164:167], a[104:107], a[72:75], v[164:167]\n"
    "v_exp_f32 v162, v162\n"
    "v_mfma_f32_16x16x32_bf16 v[164:167], a[108:111], a[76:79], v[164:167]\n"
    "v_exp_f32 v163, v163\n"
    "v_mfma_f32_16x16x32_bf16 v[128:131], a[136:139], v[184:187], v[128:131]\n"
    "ds_read_b128 a[96:99], v232 offset:4096\n"
    "v_cvt_pk_bf16_f32 v222, v160, v161\n"
    "v_mfma_f32_16x16x32_bf16 v[128:131], a[140:143], v[188:191], v[128:131]\n"
    "ds_read_b64_tr_b16 a[128:129], v238 offset:49152\n"
    "v_cvt_pk_bf16_f32 v223, v162, v163\n"
    "v_mfma_f32_16x16x32_bf16 v[132:135], a[136:139], v[192:195], v[132:135]\n"
    "ds_read_b64_tr_b16 a[130:131], v238 offset:53248\n"
    "v_mfma_f32_16x16x32_bf16 v[132:135], a[140:143], v[196:199], v[132:135]\n"
    "s_waitcnt lgkmcnt(5)\n"
    "buffer_load_dwordx4 v248, s[60:63], s83 offen offset:2048 lds\n"
    "v_mfma_f32_16x16x32_bf16 a[12:15], a[144:147], v[192:195], a[12:15]\n"
    "buffer_load_dwordx4 v249, s[60:63], s83 offen offset:3072 lds\n"
    "v_mfma_f32_16x16x32_bf16 a[12:15], a[144:147], v[196:199], a[12:15]\n"
    "s_add_u32 m0, s85, 163328\n"
    "s_nop 0\n"
    "buffer_load_dword v251, s[56:59], s5 offen lds\n"
    "buffer_load_dword v251, s[60:63], s5 offen lds\n"
    "s_add_u32 s5, s5, 32768\n"
    "s_add_u32 s82, s82, 16384\n"
    "s_add_u32 s83, s83, 16384\n"
    "s_cmp_lg_u32 s71, 0\n"
    "s_cbranch_scc1 L_attn_loop_%=\n"
    "v_mfma_f32_16x16x32_bf16 v[8:11], a[112:115], v[200:203], v[8:11]\n"
    "s_add_u32 m0, s51, 0\n"
    "s_nop 0\n"
    "buffer_load_dwordx4 v252, s[40:43], s48 offen offset:0 lds\n"
    "v_exp_f32 v164, v164\n"
    "v_mfma_f32_16x16x32_bf16 v[8:11], a[116:119], v[204:207], v[8:11]\n"
    "v_exp_f32 v165, v165\n"
    "v_mfma_f32_16x16x32_bf16 v[12:15], a[112:115], v[208:211], v[12:15]\n"
    "buffer_load_dwordx4 v252, s[40:43], s48 offen offset:1024 lds\n"
    "v_exp_f32 v166, v166\n"
    "v_mfma_f32_16x16x32_bf16 v[12:15], a[116:119], v[212:215], v[12:15]\n"
    "v_exp_f32 v167, v167\n"
    "v_mfma_f32_16x16x32_bf16 v[16:19], a[112:115], v[216:219], v[16:19]\n"
    "buffer_load_dwordx4 v252, s[40:43], s48 offen offset:2048 lds\n"
    "v_cvt_pk_bf16_f32 v230, v164, v165\n"
    "v_cvt_pk_bf16_f32 v231, v166, v167\n"
    "v_mfma_f32_16x16x32_bf16 v[16:19], a[116:119], v[220:223], v[16:19]\n"
    "ds_read_b64_tr_b16 a[132:133], v238 offset:57344\n"
    "v_mfma_f32_16x16x32_bf16 v[20:23], a[112:115], v[224:227], v[20:23]\n"
    "buffer_load_dwordx4 v252, s[40:43], s48 offen offset:3072 lds\n"
    "s_add_u32 s48, s48, 4096\n"
    "ds_read_b64_tr_b16 a[134:135], v238 offset:61440\n"
    "v_mfma_f32_16x16x32_bf16 v[20:23], a[116:119], v[228:231], v[20:23]\n"
    "ds_read_b64_tr_b16 a[136:137], v239 offset:49152\n"
    "v_mfma_f32_16x16x32_bf16 v[24:27], a[120:123], v[200:203], v[24:27]\n"
    "s_add_u32 m0, s51, 4096\n"
    "s_nop 0\n"
    "buffer_load_dwordx4 v252, s[40:43], s48 offen offset:0 lds\n"
    "ds_read_b64_tr_b16 a[138:139], v239 offset:53248\n"
    "s_waitcnt lgkmcnt(7)\n"
    "v_mfma_f32_16x16x32_bf16 v[24:27], a[124:127], v[204:207], v[24:27]\n"
    "ds_read_b64_tr_b16 a[140:141], v239 offset:57344\n"
    "v_mfma_f32_16x16x32_bf16 v[28:31], a[120:123], v[208:211], v[28:31]\n"
    "buffer_load_dwordx4 v252, s[40:43], s48 offen offset:1024 lds\n"
    "ds_read_b64_tr_b16 a[142:143], v239 offset:61440\n"
    "v_mfma_f32_16x16x32_bf16 v[28:31], a[124:127], v[212:215], v[28:31]\n"
    "ds_read_b64_tr_b16 a[80:81], v240 offset:49152\n"
    "v_mfma_f32_16x16x32_bf16 v[32:35], a[120:123], v[216:219], v[32:35]\n"
    "buffer_load_dwordx4 v252, s[40:43], s48 offen offset:2048 lds\n"
    "ds_read_b64_tr_b16 a[82:83], v240 offset:53248\n"
    "v_mfma_f32_16x16x32_bf16 v[32:35], a[124:127], v[220:223], v[32:35]\n"
    "ds_read_b64_tr_b16 a[84:85], v240 offset:57344\n"
    "v_mfma_f32_16x16x32_bf16 v[36:39], a[120:123], v[224:227], v[36:39]\n"
    "buffer_load_dwordx4 v252, s[40:43], s48 offen offset:3072 lds\n"
    "s_add_u32 s48, s48, 4096\n"
    "ds_read_b64_tr_b16 a[86:87], v240 offset:61440\n"
    "v_mfma_f32_16x16x32_bf16 v[36:39], a[124:127], v[228:231], v[36:39]\n"
    "ds_read_b64_tr_b16 a[88:89], v241 offset:49152\n"
    "v_mfma_f32_16x16x32_bf16 a[0:3], a[144:147], v[200:203], a[0:3]\n"
    "s_add_u32 m0, s51, 8192\n"
    "s_nop 0\n"
    "buffer_load_dwordx4 v252, s[40:43], s48 offen offset:0 lds\n"
    "s_add_u32 s48, s48, 139264\n"
    "ds_read_b64_tr_b16 a[90:91], v241 offset:53248\n"
    "v_mfma_f32_16x16x32_bf16 a[0:3], a[144:147], v[204:207], a[0:3]\n"
    "ds_read_b64_tr_b16 a[92:93], v241 offset:57344\n"
    "v_mfma_f32_16x16x32_bf16 a[4:7], a[144:147], v[208:211], a[4:7]\n"
    "buffer_load_dwordx4 a[148:151], v252, s[44:47], s49 offen offset:0 sc1\n"
    "ds_read_b64_tr_b16 a[94:95], v241 offset:61440\n"
    "v_mfma_f32_16x16x32_bf16 a[4:7], a[144:147], v[212:215], a[4:7]\n"
    "ds_read_b64_tr_b16 a[96:97], v242 offset:49152\n"
    "v_mfma_f32_16x16x32_bf16 a[8:11], a[144:147], v[216:219], a[8:11]\n"
    "buffer_load_dwordx4 a[152:155], v252, s[44:47], s49 offen offset:1024 sc1\n"
    "ds_read_b64_tr_b16 a[98:99], v242 offset:53248\n"
    "v_mfma_f32_16x16x32_bf16 a[8:11], a[144:147], v[220:223], a[8:11]\n"
    "ds_read_b64_tr_b16 a[100:101], v242 offset:57344\n"
    "v_mfma_f32_16x16x32_bf16 a[12:15], a[144:147], v[224:227], a[12:15]\n"
    "buffer_load_dwordx4 a[156:159], v252, s[44:47], s49 offen offset:2048 sc1\n"
    "ds_read_b64_tr_b16 a[102:103], v242 offset:61440\n"
    "v_mfma_f32_16x16x32_bf16 a[12:15], a[144:147], v[228:231], a[12:15]\n"
    "ds_read_b64_tr_b16 a[104:105], v243 offset:49152\n"
    "s_waitcnt lgkmcnt(15)\n"
    "v_mfma_f32_16x16x32_bf16 v[40:43], a[128:131], v[200:203], v[40:43]\n"
    "buffer_load_dwordx4 a[160:163], v252, s[44:47], s49 offen offset:3072 sc1\n"
    "s_add_u32 s49, s49, 4096\n"
    "ds_read_b64_tr_b16 a[106:107], v243 offset:53248\n"
    "s_waitcnt lgkmcnt(15)\n"
    "v_mfma_f32_16x16x32_bf16 v[40:43], a[132:135], v[204:207], v[40:43]\n"
    "ds_read_b64_tr_b16 a[108:109], v243 offset:57344\n"
    "s_waitcnt lgkmcnt(15)\n"
    "v_mfma_f32_16x16x32_bf16 v[56:59], a[136:139], v[200:203], v[56:59]\n"
    "buffer_load_dwordx4 a[164:167], v252, s[44:47], s50 offen offset:0 sc1\n"
    "ds_read_b64_tr_b16 a[110:111], v243 offset:61440\n"
    "s_waitcnt lgkmcnt(15)\n"
    "v_mfma_f32_16x16x32_bf16 v[56:59], a[140:143], v[204:207], v[56:59]\n"
    "v_accvgpr_read_b32 v250, a0\n"
    "v_accvgpr_read_b32 v251, a4\n"
    "s_waitcnt lgkmcnt(14)\n"
    "v_mfma_f32_16x16x32_bf16 v[72:75], a[80:83], v[200:203], v[72:75]\n"
    "buffer_load_dwordx4 a[168:171], v252, s[44:47], s50 offen offset:1024 sc1\n"
    "v_accvgpr_read_b32 v253, a8\n"
    "v_accvgpr_read_b32 v255, a12\n"
    "s_waitcnt lgkmcnt(12)\n"
    "v_mfma_f32_16x16x32_bf16 v[72:75], a[84:87], v[204:207], v[72:75]\n"
    "s_mov_b32 s96, 0x4f800000\n"
    "v_cmp_nge_f32 vcc, s96, v250\n"
    "v_cmp_nge_f32 s[98:99], s96, v251\n"
    "s_nop 1\n"
    "s_or_b64 vcc, vcc, s[98:99]\n"
    "v_cmp_nge_f32 s[98:99], s96, v253\n"
    "s_nop 1\n"
    "s_or_b64 vcc, vcc, s[98:99]\n"
    "v_cmp_nge_f32 s[98:99], s96, v255\n"
    "s_nop 1\n"
    "s_or_b64 vcc, vcc, s[98:99]\n"
    "s_cmp_lg_u64 vcc, 0\n"
    "s_cselect_b32 s96, 1, 0\n"
    "v_mov_b32 v4, s96\n"
    "v_mov_b32 v5, s85\n"
    "v_add_u32 v5, 162816, v5\n"
    "ds_or_b32 v5, v4\n"
    "s_waitcnt lgkmcnt(11)\n"
    "v_mfma_f32_16x16x32_bf16 v[88:91], a[88:91], v[200:203], v[88:91]\n"
    "buffer_load_dwordx4 a[172:175], v252, s[44:47], s50 offen offset:2048 sc1\n"
    "v_rcp_f32 v250, v250\n"
    "v_rcp_f32 v251, v251\n"
    "s_waitcnt lgkmcnt(9)\n"
    "v_mfma_f32_16x16x32_bf16 v[88:91], a[92:95], v[204:207], v[88:91]\n"
    "v_rcp_f32 v253, v253\n"
    "v_rcp_f32 v255, v255\n"
    "s_waitcnt lgkmcnt(7)\n"
    "v_mfma_f32_16x16x32_bf16 v[104:107], a[96:99], v[200:203], v[104:107]\n"
    "buffer_load_dwordx4 a[176:179], v252, s[44:47], s50 offen offset:3072 sc1\n"
    "s_add_u32 s50, s50, 4096\n"
    "s_waitcnt lgkmcnt(5)\n"
    "v_mfma_f32_16x16x32_bf16 v[104:107], a[100:103], v[204:207], v[104:107]\n"
    "s_waitcnt lgkmcnt(3)\n"
    "v_mfma_f32_16x16x32_bf16 v[120:123], a[104:107], v[200:203], v[120:123]\n"
    "s_add_u32 m0, s51, 36864\n"
    "s_nop 0\n"
    "buffer_load_dwordx4 v252, s[40:43], s48 offen offset:0 lds\n"
    "s_waitcnt lgkmcnt(1)\n"
    "v_mfma_f32_16x16x32_bf16 v[120:123], a[108:111], v[204:207], v[120:123]\n"
    "v_mfma_f32_16x16x32_bf16 v[44:47], a[128:131], v[208:211], v[44:47]\n"
    "buffer_load_dwordx4 v252, s[40:43], s48 offen offset:1024 lds\n"
    "v_mfma_f32_16x16x32_bf16 v[44:47], a[132:135], v[212:215], v[44:47]\n"
    "v_mul_f32 v8, v8, v250\n"
    "v_mul_f32 v9, v9, v250\n"
    "v_mul_f32 v10, v10, v250\n"
    "v_mul_f32 v11, v11, v250\n"
    "v_mfma_f32_16x16x32_bf16 v[60:63], a[136:139], v[208:211], v[60:63]\n"
    "buffer_load_dwordx4 v252, s[40:43], s48 offen offset:2048 lds\n"
    "v_cvt_pk_bf16_f32 v136, v8, v9\n"
    "v_cvt_pk_bf16_f32 v137, v10, v11\n"
    "v_mul_f32 v24, v24, v250\n"
    "v_mul_f32 v25, v25, v250\n"
    "v_mfma_f32_16x16x32_bf16 v[60:63], a[140:143], v[212:215], v[60:63]\n"
    "v_mul_f32 v26, v26, v250\n"
    "v_mul_f32 v27, v27, v250\n"
    "v_cvt_pk_bf16_f32 v138, v24, v25\n"
    "v_cvt_pk_bf16_f32 v139, v26, v27\n"
    "v_mul_f32 v40, v40, v250\n"
    "v_mfma_f32_16x16x32_bf16 v[76:79], a[80:83], v[208:211], v[76:79]\n"
    "buffer_load_dwordx4 v252, s[40:43], s48 offen offset:3072 lds\n"
    "s_add_u32 s48, s48, 4096\n"
    "v_mul_f32 v41, v41, v250\n"
    "v_mul_f32 v42, v42, v250\n"
    "v_mul_f32 v43, v43, v250\n"
    "v_cvt_pk_bf16_f32 v140, v40, v41\n"
    "v_mfma_f32_16x16x32_bf16 v[76:79], a[84:87], v[212:215], v[76:79]\n"
    "v_cvt_pk_bf16_f32 v141, v42, v43\n"
    "v_mul_f32 v56, v56, v250\n"
    "v_mul_f32 v57, v57, v250\n"
    "v_mul_f32 v58, v58, v250\n"
    "v_mfma_f32_16x16x32_bf16 v[92:95], a[88:91], v[208:211], v[92:95]\n"
    "s_add_u32 m0, s51, 40960\n"
    "s_nop 0\n"
    "buffer_load_dwordx4 v252, s[40:43], s48 offen offset:0 lds\n"
    "v_mul_f32 v59, v59, v250\n"
    "v_cvt_pk_bf16_f32 v142, v56, v57\n"
    "v_cvt_pk_bf16_f32 v143, v58, v59\n"
    "v_mul_f32 v72, v72, v250\n"
    "v_mul_f32 v73, v73, v250\n"
    "v_mfma_f32_16x16x32_bf16 v[92:95], a[92:95], v[212:215], v[92:95]\n"
    "v_mul_f32 v74, v74, v250\n"
    "v_mul_f32 v75, v75, v250\n"
    "v_cvt_pk_bf16_f32 v144, v72, v73\n"
    "v_cvt_pk_bf16_f32 v145, v74, v75\n"
    "v_mfma_f32_16x16x32_bf16 v[108:111], a[96:99], v[208:211], v[108:111]\n"
    "buffer_load_dwordx4 v252, s[40:43], s48 offen offset:1024 lds\n"
    "v_mul_f32 v88, v88, v250\n"
    "v_mul_f32 v89, v89, v250\n"
    "v_mul_f32 v90, v90, v250\n"
    "v_mul_f32 v91, v91, v250\n"
    "v_mfma_f32_16x16x32_bf16 v[108:111], a[100:103], v[212:215], v[108:111]\n"
    "v_cvt_pk_bf16_f32 v146, v88, v89\n"
    "v_cvt_pk_bf16_f32 v147, v90, v91\n"
    "v_mul_f32 v104, v104, v250\n"
    "v_mul_f32 v105, v105, v250\n"
    "v_mul_f32 v106, v106, v250\n"
    "v_mfma_f32_16x16x32_bf16 v[124:127], a[104:107], v[208:211], v[124:127]\n"
    "buffer_load_dwordx4 v252, s[40:43], s48 offen offset:2048 lds\n"
    "v_mul_f32 v107, v107, v250\n"
    "v_cvt_pk_bf16_f32 v148, v104, v105\n"
    "v_cvt_pk_bf16_f32 v149, v106, v107\n"
    "v_mul_f32 v120, v120, v250\n"
    "v_mfma_f32_16x16x32_bf16 v[124:127], a[108:111], v[212:215], v[124:127]\n"
    "v_mul_f32 v121, v121, v250\n"
    "v_mul_f32 v122, v122, v250\n"
    "v_mul_f32 v123, v123, v250\n"
    "v_cvt_pk_bf16_f32 v150, v120, v121\n"
    "v_cvt_pk_bf16_f32 v151, v122, v123\n"
    "v_mfma_f32_16x16x32_bf16 v[48:51], a[128:131], v[216:219], v[48:51]\n"
    "buffer_load_dwordx4 v252, s[40:43], s48 offen offset:3072 lds\n"
    "s_add_u32 s48, s48, 4096\n"
    "v_mfma_f32_16x16x32_bf16 v[48:51], a[132:135], v[220:223], v[48:51]\n"
    "v_mul_f32 v12, v12, v251\n"
    "v_mul_f32 v13, v13, v251\n"
    "v_mul_f32 v14, v14, v251\n"
    "v_mul_f32 v15, v15, v251\n"
    "v_mfma_f32_16x16x32_bf16 v[64:67], a[136:139], v[216:219], v[64:67]\n"
    "s_add_u32 m0, s51, 45056\n"
    "s_nop 0\n"
    "buffer_load_dwordx4 v252, s[40:43], s48 offen offset:0 lds\n"
    "s_add_u32 s48, s48, 139264\n"
    "v_cvt_pk_bf16_f32 v152, v12, v13\n"
    "v_cvt_pk_bf16_f32 v153, v14, v15\n"
    "v_mul_f32 v28, v28, v251\n"
    "v_mul_f32 v29, v29, v251\n"
    "v_mfma_f32_16x16x32_bf16 v[64:67], a[140:143], v[220:223], v[64:67]\n"
    "v_mul_f32 v30, v30, v251\n"
    "v_mul_f32 v31, v31, v251\n"
    "v_cvt_pk_bf16_f32 v154, v28, v29\n"
    "v_cvt_pk_bf16_f32 v155, v30, v31\n"
    "v_mul_f32 v44, v44, v251\n"
    "v_mfma_f32_16x16x32_bf16 v[80:83], a[80:83], v[216:219], v[80:83]\n"
    "buffer_load_dwordx4 a[180:183], v252, s[44:47], s49 offen offset:0 sc1\n"
    "v_mul_f32 v45, v45, v251\n"
    "v_mul_f32 v46, v46, v251\n"
    "v_mul_f32 v47, v47, v251\n"
    "v_cvt_pk_bf16_f32 v156, v44, v45\n"
    "v_mfma_f32_16x16x32_bf16 v[80:83], a[84:87], v[220:223], v[80:83]\n"
    "v_cvt_pk_bf16_f32 v157, v46, v47\n"
    "v_mul_f32 v60, v60, v251\n"
    "v_mul_f32 v61, v61, v251\n"
    "v_mul_f32 v62, v62, v251\n"
    "v_mfma_f32_16x16x32_bf16 v[96:99], a[88:91], v[216:219], v[96:99]\n"
    "buffer_load_dwordx4 a[184:187], v252, s[44:47], s49 offen offset:1024 sc1\n"
    "v_mul_f32 v63, v63, v251\n"
    "v_cvt_pk_bf16_f32 v158, v60, v61\n"
    "v_cvt_pk_bf16_f32 v159, v62, v63\n"
    "v_mul_f32 v76, v76, v251\n"
    "v_mul_f32 v77, v77, v251\n"
    "v_mfma_f32_16x16x32_bf16 v[96:99], a[92:95], v[220:223], v[96:99]\n"
    "v_mul_f32 v78, v78, v251\n"
    "v_mul_f32 v79, v79, v251\n"
    "v_cvt_pk_bf16_f32 v160, v76, v77\n"
    "v_cvt_pk_bf16_f32 v161, v78, v79\n"
    "v_mfma_f32_16x16x32_bf16 v[112:115], a[96:99], v[216:219], v[112:115]\n"
    "buffer_load_dwordx4 a[188:191], v252, s[44:47], s49 offen offset:2048 sc1\n"
    "v_mul_f32 v92, v92, v251\n"
    "v_mul_f32 v93, v93, v251\n"
    "v_mul_f32 v94, v94, v251\n"
    "v_mul_f32 v95, v95, v251\n"
    "v_mfma_f32_16x16x32_bf16 v[112:115], a[100:103], v[220:223], v[112:115]\n"
    "v_cvt_pk_bf16_f32 v162, v92, v93\n"
    "v_cvt_pk_bf16_f32 v163, v94, v95\n"
    "v_mul_f32 v108, v108, v251\n"
    "v_mul_f32 v109, v109, v251\n"
    "v_mul_f32 v110, v110, v251\n"
    "v_mfma_f32_16x16x32_bf16 v[128:131], a[104:107], v[216:219], v[128:131]\n"
    "buffer_load_dwordx4 a[192:195], v252, s[44:47], s49 offen offset:3072 sc1\n"
    "s_add_u32 s49, s49, 4096\n"
    "v_mul_f32 v111, v111, v251\n"
    "v_cvt_pk_bf16_f32 v164, v108, v109\n"
    "v_cvt_pk_bf16_f32 v165, v110, v111\n"
    "v_mul_f32 v124, v124, v251\n"
    "v_mfma_f32_16x16x32_bf16 v[128:131], a[108:111], v[220:223], v[128:131]\n"
    "v_mul_f32 v125, v125, v251\n"
    "v_mul_f32 v126, v126, v251\n"
    "v_mul_f32 v127, v127, v251\n"
    "v_cvt_pk_bf16_f32 v166, v124, v125\n"
    "v_cvt_pk_bf16_f32 v167, v126, v127\n"
    "v_mfma_f32_16x16x32_bf16 v[52:55], a[128:131], v[224:227], v[52:55]\n"
    "buffer_load_dwordx4 a[196:199], v252, s[44:47], s50 offen offset:0 sc1\n"
    "v_mfma_f32_16x16x32_bf16 v[52:55], a[132:135], v[228:231], v[52:55]\n"
    "v_mul_f32 v16, v16, v253\n"
    "v_mul_f32 v17, v17, v253\n"
    "v_mul_f32 v18, v18, v253\n"
    "v_mul_f32 v19, v19, v253\n"
    "v_mfma_f32_16x16x32_bf16 v[68:71], a[136:139], v[224:227], v[68:71]\n"
    "buffer_load_dwordx4 a[200:203], v252, s[44:47], s50 offen offset:1024 sc1\n"
    "v_cvt_pk_bf16_f32 v168, v16, v17\n"
    "v_cvt_pk_bf16_f32 v169, v18, v19\n"
    "v_mul_f32 v32, v32, v253\n"
    "v_mul_f32 v33, v33, v253\n"
    "v_mfma_f32_16x16x32_bf16 v[68:71], a[140:143], v[228:231], v[68:71]\n"
    "v_mul_f32 v34, v34, v253\n"
    "v_mul_f32 v35, v35, v253\n"
    "v_cvt_pk_bf16_f32 v170, v32, v33\n"
    "v_cvt_pk_bf16_f32 v171, v34, v35\n"
    "v_mul_f32 v48, v48, v253\n"
    "v_mfma_f32_16x16x32_bf16 v[84:87], a[80:83], v[224:227], v[84:87]\n"
    "buffer_load_dwordx4 a[204:207], v252, s[44:47], s50 offen offset:2048 sc1\n"
    "v_mul_f32 v49, v49, v253\n"
    "v_mul_f32 v50, v50, v253\n"
    "v_mul_f32 v51, v51, v253\n"
    "v_cvt_pk_bf16_f32 v172, v48, v49\n"
    "v_mfma_f32_16x16x32_bf16 v[84:87], a[84:87], v[228:231], v[84:87]\n"
    "v_cvt_pk_bf16_f32 v173, v50, v51\n"
    "v_mul_f32 v64, v64, v253\n"
    "v_mul_f32 v65, v65, v253\n"
    "v_mul_f32 v66, v66, v253\n"
    "v_mfma_f32_16x16x32_bf16 v[100:103], a[88:91], v[224:227], v[100:103]\n"
    "buffer_load_dwordx4 a[208:211], v252, s[44:47], s50 offen offset:3072 sc1\n"
    "s_add_u32 s50, s50, 4096\n"
    "v_mul_f32 v67, v67, v253\n"
    "v_cvt_pk_bf16_f32 v174, v64, v65\n"
    "v_cvt_pk_bf16_f32 v175, v66, v67\n"
    "v_mul_f32 v80, v80, v253\n"
    "v_mul_f32 v81, v81, v253\n"
    "v_mfma_f32_16x16x32_bf16 v[100:103], a[92:95], v[228:231], v[100:103]\n"
    "v_mul_f32 v82, v82, v253\n"
    "v_mul_f32 v83, v83, v253\n"
    "v_cvt_pk_bf16_f32 v176, v80, v81\n"
    "v_cvt_pk_bf16_f32 v177, v82, v83\n"
    "v_mfma_f32_16x16x32_bf16 v[116:119], a[96:99], v[224:227], v[116:119]\n"
    "v_mul_f32 v96, v96, v253\n"
    "v_mul_f32 v97, v97, v253\n"
    "v_mul_f32 v98, v98, v253\n"
    "v_mul_f32 v99, v99, v253\n"
    "v_mfma_f32_16x16x32_bf16 v[116:119], a[100:103], v[228:231], v[116:119]\n"
    "v_cvt_pk_bf16_f32 v178, v96, v97\n"
    "v_cvt_pk_bf16_f32 v179, v98, v99\n"
    "v_mul_f32 v112, v112, v253\n"
    "v_mul_f32 v113, v113, v253\n"
    "v_mul_f32 v114, v114, v253\n"
    "v_mfma_f32_16x16x32_bf16 v[132:135], a[104:107], v[224:227], v[132:135]\n"
    "v_mul_f32 v115, v115, v253\n"
    "v_cvt_pk_bf16_f32 v180, v112, v113\n"
    "v_cvt_pk_bf16_f32 v181, v114, v115\n"
    "v_mul_f32 v128, v128, v253\n"
    "v_mfma_f32_16x16x32_bf16 v[132:135], a[108:111], v[228:231], v[132:135]\n"
    "v_mul_f32 v129, v129, v253\n"
    "v_mul_f32 v130, v130, v253\n"
    "v_mul_f32 v131, v131, v253\n"
    "v_cvt_pk_bf16_f32 v182, v128, v129\n"
    "v_cvt_pk_bf16_f32 v183, v130, v131\n"
    "s_nop 7\n"
    "v_mul_f32 v20, v20, v255\n"
    "v_mul_f32 v21, v21, v255\n"
    "v_mul_f32 v22, v22, v255\n"
    "v_mul_f32 v23, v23, v255\n"
    "v_cvt_pk_bf16_f32 v184, v20, v21\n"
    "v_cvt_pk_bf16_f32 v185, v22, v23\n"
    "v_mul_f32 v36, v36, v255\n"
    "v_mul_f32 v37, v37, v255\n"
    "v_mul_f32 v38, v38, v255\n"
    "v_mul_f32 v39, v39, v255\n"
    "v_cvt_pk_bf16_f32 v186, v36, v37\n"
    "v_cvt_pk_bf16_f32 v187, v38, v39\n"
    "v_mul_f32 v52, v52, v255\n"
    "v_mul_f32 v53, v53, v255\n"
    "v_mul_f32 v54, v54, v255\n"
    "v_mul_f32 v55, v55, v255\n"
    "v_cvt_pk_bf16_f32 v188, v52, v53\n"
    "v_cvt_pk_bf16_f32 v189, v54, v55\n"
    "v_mul_f32 v68, v68, v255\n"
    "v_mul_f32 v69, v69, v255\n"
    "v_mul_f32 v70, v70, v255\n"
    "v_mul_f32 v71, v71, v255\n"
    "v_cvt_pk_bf16_f32 v190, v68, v69\n"
    "v_cvt_pk_bf16_f32 v191, v70, v71\n"
    "v_mul_f32 v84, v84, v255\n"
    "v_mul_f32 v85, v85, v255\n"
    "v_mul_f32 v86, v86, v255\n"
    "v_mul_f32 v87, v87, v255\n"
    "v_cvt_pk_bf16_f32 v192, v84, v85\n"
    "v_cvt_pk_bf16_f32 v193, v86, v87\n"
    "v_mul_f32 v100, v100, v255\n"
    "v_mul_f32 v101, v101, v255\n"
    "v_mul_f32 v102, v102, v255\n"
    "v_mul_f32 v103, v103, v255\n"
    "v_cvt_pk_bf16_f32 v194, v100, v101\n"
    "v_cvt_pk_bf16_f32 v195, v102, v103\n"
    "v_mul_f32 v116, v116, v255\n"
    "v_mul_f32 v117, v117, v255\n"
    "v_mul_f32 v118, v118, v255\n"
    "v_mul_f32 v119, v119, v255\n"
    "v_cvt_pk_bf16_f32 v196, v116, v117\n"
    "v_cvt_pk_bf16_f32 v197, v118, v119\n"
    "v_mul_f32 v132, v132, v255\n"
    "v_mul_f32 v133, v133, v255\n"
    "v_mul_f32 v134, v134, v255\n"
    "v_mul_f32 v135, v135, v255\n"
    "v_cvt_pk_bf16_f32 v198, v132, v133\n"
    "v_cvt_pk_bf16_f32 v199, v134, v135\n"
    "L_oepi_done_%=:\n"
    "s_lshl_b32 s96, s88, 4\n"
    "s_lshl_b32 s97, s39, 2\n"
    "s_add_u32 s96, s96, s97\n"
    "s_lshl_b32 s96, s96, 7\n"
    "s_lshl_b32 s97, s87, 2\n"
    "s_add_u32 s96, s96, s97\n"
    "s_lshl_b32 s96, s96, 10\n"
    "s_add_u32 s56, s36, s96\n"
    "s_addc_u32 s57, s37, 0\n"
    "s_and_b32 s57, s57, 0xffff\n"
    "s_mov_b32 s58, 524288\n"
    "s_mov_b32 s59, 0x20000\n"
    "s_mov_b32 s73, 0\n"
    "s_mov_b32 s74, 131072\n"
    "s_mov_b32 s75, 262144\n"
    "s_mov_b32 s76, 393216\n"
    "s_lshr_b32 s80, s87, 2\n"
    "s_and_b32 s80, s80, 1\n"
    "s_mul_i32 s80, s80, 3\n"
    "v_add_u32 v244, s85, v252\n"
    "v_mov_b32 v2, 0\n"
    "v_mov_b32 v3, 0\n"
    "v_mov_b32 v4, 0\n"
    "v_mov_b32 v5, 0\n"
    "v_mov_b32 v128, 0\n"
    "v_mov_b32 v129, 0\n"
    "v_mov_b32 v130, 0\n"
    "v_mov_b32 v131, 0\n"
    "v_mov_b32 v132, 0\n"
    "v_mov_b32 v133, 0\n"
    "v_mov_b32 v134, 0\n"
    "v_mov_b32 v135, 0\n"
    "v_mfma_f32_32x32x16_bf16 v[64:79], v[2:5], v[2:5], 0\n"
    "v_mfma_f32_32x32x16_bf16 v[80:95], v[2:5], v[2:5], 0\n"
    "v_mfma_f32_32x32x16_bf16 v[96:111], v[2:5], v[2:5], 0\n"
    "v_mfma_f32_32x32x16_bf16 v[112:127], v[2:5], v[2:5], 0\n"
    "s_waitcnt vmcnt(17) lgkmcnt(0)\n"
    "s_barrier\n"
    "v_mov_b32 v5, s85\n"
    "v_add_u32 v5, 162816, v5\n"
    "ds_read_b32 v4, v5\n"
    "s_waitcnt lgkmcnt(0)\n"
    "v_readfirstlane_b32 s96, v4\n"
    "s_nop 3\n"
    "s_andn2_b32 s96, s96, s15\n"
    "s_cmp_lg_u32 s96, 0\n"
    "s_cbranch_scc1 L_attn_fb_%=\n"
    "ds_read_b128 a[0:3], v244 offset:0\n"
    "ds_read_b128 a[4:7], v244 offset:9216\n"
    "ds_read_b128 a[8:11], v244 offset:18432\n"
    "ds_read_b128 a[12:15], v244 offset:27648\n"
    "ds_read_b128 a[16:19], v244 offset:1024\n"
    "ds_read_b128 a[20:23], v244 offset:10240\n"
    "ds_read_b128 a[24:27], v244 offset:19456\n"
    "ds_read_b128 a[28:31], v244 offset:28672\n"
    "s_mov_b32 s64, 17\n"
    "s_sub_u32 s77, 14, s80\n"
    "s_mov_b32 s78, 4\n"
    "s_mov_b32 s79, 11\n"
    "s_cmp_eq_u32 s80, 0\n"
    "s_cbranch_scc0 L_bN_%=\n"
    "L_bS_%=:\n"
    "s_waitcnt lgkmcnt(4)\n"
    "v_mfma_f32_16x16x32_bf16 v[64:67], a[0:3], a[148:151], v[64:67]\n"
    "ds_read_b128 a[32:35], v244 offset:2048\n"
    "v_mfma_f32_16x16x32_bf16 v[64:67], a[4:7], a[152:155], v[64:67]\n"
    "buffer_store_dwordx4 v[136:139], v252, s[56:59], s73 offen offset:0 sc1\n"
    "v_mfma_f32_16x16x32_bf16 v[64:67], a[8:11], a[156:159], v[64:67]\n"
    "ds_read_b128 a[36:39], v244 offset:11264\n"
    "v_mfma_f32_16x16x32_bf16 v[64:67], a[12:15], a[160:163], v[64:67]\n"
    "buffer_store_dwordx4 v[140:143], v252, s[56:59], s73 offen offset:1024 sc1\n"
    "v_mfma_f32_16x16x32_bf16 v[100:103], a[0:3], a[164:167], v[100:103]\n"
    "ds_read_b128 a[40:43], v244 offset:20480\n"
    "v_mfma_f32_16x16x32_bf16 v[100:103], a[4:7], a[168:171], v[100:103]\n"
    "v_mfma_f32_16x16x32_bf16 v[100:103], a[8:11], a[172:175], v[100:103]\n"
    "ds_read_b128 a[44:47], v244 offset:29696\n"
    "v_mfma_f32_16x16x32_bf16 v[100:103], a[12:15], a[176:179], v[100:103]\n"
    "buffer_store_dwordx4 v[144:147], v252, s[56:59], s73 offen offset:2048 sc1\n"
    "s_waitcnt lgkmcnt(4)\n"
    "v_mfma_f32_16x16x32_bf16 v[68:71], a[16:19], a[148:151], v[68:71]\n"
    "ds_read_b128 a[0:3], v244 offset:3072\n"
    "v_mfma_f32_16x16x32_bf16 v[68:71], a[20:23], a[152:155], v[68:71]\n"
    "buffer_store_dwordx4 v[148:151], v252, s[56:59], s73 offen offset:3072 sc1\n"
    "v_mfma_f32_16x16x32_bf16 v[68:71], a[24:27], a[156:159], v[68:71]\n"
    "ds_read_b128 a[4:7], v244 offset:12288\n"
    "v_mfma_f32_16x16x32_bf16 v[68:71], a[28:31], a[160:163], v[68:71]\n"
    "v_mfma_f32_16x16x32_bf16 v[104:107], a[16:19], a[164:167], v[104:107]\n"
    "ds_read_b128 a[8:11], v244 offset:21504\n"
    "v_mfma_f32_16x16x32_bf16 v[104:107], a[20:23], a[168:171], v[104:107]\n"
    "buffer_store_dwordx4 v[152:155], v252, s[56:59], s74 offen offset:0 sc1\n"
    "v_mfma_f32_16x16x32_bf16 v[104:107], a[24:27], a[172:175], v[104:107]\n"
    "ds_read_b128 a[12:15], v244 offset:30720\n"
    "v_mfma_f32_16x16x32_bf16 v[104:107], a[28:31], a[176:179], v[104:107]\n"
    "buffer_store_dwordx4 v[156:159], v252, s[56:59], s74 offen offset:1024 sc1\n"
    "s_waitcnt lgkmcnt(4)\n"
    "v_mfma_f32_16x16x32_bf16 v[72:75], a[32:35], a[148:151], v[72:75]\n"
    "ds_read_b128 a[16:19], v244 offset:4096\n"
    "v_mfma_f32_16x16x32_bf16 v[72:75], a[36:39], a[152:155], v[72:75]\n"
    "v_mfma_f32_16x16x32_bf16 v[72:75], a[40:43], a[156:159], v[72:75]\n"
    "ds_read_b128 a[20:23], v244 offset:13312\n"
    "v_mfma_f32_16x16x32_bf16 v[72:75], a[44:47], a[160:163], v[72:75]\n"
    "s_add_u32 m0, s51, 73728\n"
    "s_nop 0\n"
    "buffer_load_dwordx4 v252, s[40:43], s48 offen offset:0 lds\n"
    "v_mfma_f32_16x16x32_bf16 v[108:111], a[32:35], a[164:167], v[108:111]\n"
    "ds_read_b128 a[24:27], v244 offset:22528\n"
    "v_mfma_f32_16x16x32_bf16 v[108:111], a[36:39], a[168:171], v[108:111]\n"
    "buffer_load_dwordx4 v252, s[40:43], s48 offen offset:1024 lds\n"
    "v_mfma_f32_16x16x32_bf16 v[108:111], a[40:43], a[172:175], v[108:111]\n"
    "ds_read_b128 a[28:31], v244 offset:31744\n"
    "v_mfma_f32_16x16x32_bf16 v[108:111], a[44:47], a[176:179], v[108:111]\n"
    "s_waitcnt lgkmcnt(4)\n"
    "v_mfma_f32_16x16x32_bf16 v[76:79], a[0:3], a[148:151], v[76:79]\n"
    "ds_read_b128 a[32:35], v244 offset:5120\n"
    "v_mfma_f32_16x16x32_bf16 v[76:79], a[4:7], a[152:155], v[76:79]\n"
    "buffer_load_dwordx4 v252, s[40:43], s48 offen offset:2048 lds\n"
    "v_mfma_f32_16x16x32_bf16 v[76:79], a[8:11], a[156:159], v[76:79]\n"
    "ds_read_b128 a[36:39], v244 offset:14336\n"
    "v_mfma_f32_16x16x32_bf16 v[76:79], a[12:15], a[160:163], v[76:79]\n"
    "v_mfma_f32_16x16x32_bf16 v[112:115], a[0:3], a[164:167], v[112:115]\n"
    "ds_read_b128 a[40:43], v244 offset:23552\n"
    "v_mfma_f32_16x16x32_bf16 v[112:115], a[4:7], a[168:171], v[112:115]\n"
    "buffer_load_dwordx4 v252, s[40:43], s48 offen offset:3072 lds\n"
    "s_add_u32 s48, s48, 4096\n"
    "v_mfma_f32_16x16x32_bf16 v[112:115], a[8:11], a[172:175], v[112:115]\n"
    "ds_read_b128 a[44:47], v244 offset:32768\n"
    "v_mfma_f32_16x16x32_bf16 v[112:115], a[12:15], a[176:179], v[112:115]\n"
    "s_add_u32 m0, s51, 77824\n"
    "s_nop 0\n"
    "buffer_load_dwordx4 v252, s[40:43], s48 offen offset:0 lds\n"
    "s_waitcnt lgkmcnt(4)\n"
    "v_mfma_f32_16x16x32_bf16 v[80:83], a[16:19], a[148:151], v[80:83]\n"
    "ds_read_b128 a[0:3], v244 offset:6144\n"
    "v_mfma_f32_16x16x32_bf16 v[80:83], a[20:23], a[152:155], v[80:83]\n"
    "v_mfma_f32_16x16x32_bf16 v[80:83], a[24:27], a[156:159], v[80:83]\n"
    "ds_read_b128 a[4:7], v244 offset:15360\n"
    "v_mfma_f32_16x16x32_bf16 v[80:83], a[28:31], a[160:163], v[80:83]\n"
    "buffer_load_dwordx4 v252, s[40:43], s48 offen offset:1024 lds\n"
    "v_mfma_f32_16x16x32_bf16 v[116:119], a[16:19], a[164:167], v[116:119]\n"
    "ds_read_b128 a[8:11], v244 offset:24576\n"
    "v_mfma_f32_16x16x32_bf16 v[116:119], a[20:23], a[168:171], v[116:119]\n"
    "buffer_load_dwordx4 v252, s[40:43], s48 offen offset:2048 lds\n"
    "v_mfma_f32_16x16x32_bf16 v[116:119], a[24:27], a[172:175], v[116:119]\n"
    "ds_read_b128 a[12:15], v244 offset:33792\n"
    "v_mfma_f32_16x16x32_bf16 v[116:119], a[28:31], a[176:179], v[116:119]\n"
    "s_waitcnt lgkmcnt(4)\n"
    "v_mfma_f32_16x16x32_bf16 v[84:87], a[32:35], a[148:151], v[84:87]\n"
    "ds_read_b128 a[16:19], v244 offset:7168\n"
    "v_mfma_f32_16x16x32_bf16 v[84:87], a[36:39], a[152:155], v[84:87]\n"
    "buffer_load_dwordx4 v252, s[40:43], s48 offen offset:3072 lds\n"
    "s_add_u32 s48, s48, 4096\n"
    "v_mfma_f32_16x16x32_bf16 v[84:87], a[40:43], a[156:159], v[84:87]\n"
    "ds_read_b128 a[20:23], v244 offset:16384\n"
    "v_mfma_f32_16x16x32_bf16 v[84:87], a[44:47], a[160:163], v[84:87]\n"
    "s_add_u32 m0, s51, 81920\n"
    "s_nop 0\n"
    "buffer_load_dwordx4 v252, s[40:43], s48 offen offset:0 lds\n"
    "s_add_u32 s48, s48, 139264\n"
    "v_mfma_f32_16x16x32_bf16 v[120:123], a[32:35], a[164:167], v[120:123]\n"
    "ds_read_b128 a[24:27], v244 offset:25600\n"
    "v_mfma_f32_16x16x32_bf16 v[120:123], a[36:39], a[168:171], v[120:123]\n"
    "v_mfma_f32_16x16x32_bf16 v[120:123], a[40:43], a[172:175], v[120:123]\n"
    "ds_read_b128 a[28:31], v244 offset:34816\n"
    "v_mfma_f32_16x16x32_bf16 v[120:123], a[44:47], a[176:179], v[120:123]\n"
    "buffer_load_dwordx4 a[212:215], v252, s[44:47], s49 offen offset:0 sc1\n"
    "s_waitcnt lgkmcnt(4)\n"
    "v_mfma_f32_16x16x32_bf16 v[88:91], a[0:3], a[148:151], v[88:91]\n"
    "ds_read_b128 a[32:35], v244 offset:8192\n"
    "v_mfma_f32_16x16x32_bf16 v[88:91], a[4:7], a[152:155], v[88:91]\n"
    "v_mfma_f32_16x16x32_bf16 v[88:91], a[8:11], a[156:159], v[88:91]\n"
    "ds_read_b128 a[36:39], v244 offset:17408\n"
    "v_mfma_f32_16x16x32_bf16 v[88:91], a[12:15], a[160:163], v[88:91]\n"
    "buffer_load_dwordx4 a[216:219], v252, s[44:47], s49 offen offset:1024 sc1\n"
    "v_mfma_f32_16x16x32_bf16 v[124:127], a[0:3], a[164:167], v[124:127]\n"
    "ds_read_b128 a[40:43], v244 offset:26624\n"
    "v_mfma_f32_16x16x32_bf16 v[124:127], a[4:7], a[168:171], v[124:127]\n"
    "buffer_load_dwordx4 a[220:223], v252, s[44:47], s49 offen offset:2048 sc1\n"
    "v_mfma_f32_16x16x32_bf16 v[124:127], a[8:11], a[172:175], v[124:127]\n"
    "ds_read_b128 a[44:47], v244 offset:35840\n"
    "v_mfma_f32_16x16x32_bf16 v[124:127], a[12:15], a[176:179], v[124:127]\n"
    "s_waitcnt vmcnt(18) lgkmcnt(0)\n"
    "s_barrier\n"
    "v_add_u32 v244, 36864, v244\n"
    "v_mfma_f32_16x16x32_bf16 v[92:95], a[16:19], a[148:151], v[92:95]\n"
    "ds_read_b128 a[0:3], v244 offset:0\n"
    "v_mfma_f32_16x16x32_bf16 v[92:95], a[20:23], a[152:155], v[92:95]\n"
    "buffer_load_dwordx4 a[224:227], v252, s[44:47], s49 offen offset:3072 sc1\n"
    "s_add_u32 s49, s49, 4096\n"
    "v_mfma_f32_16x16x32_bf16 v[92:95], a[24:27], a[156:159], v[92:95]\n"
    "ds_read_b128 a[4:7], v244 offset:9216\n"
    "v_mfma_f32_16x16x32_bf16 v[92:95], a[28:31], a[160:163], v[92:95]\n"
    "buffer_load_dwordx4 a[228:231], v252, s[44:47], s50 offen offset:0 sc1\n"
    "v_mfma_f32_16x16x32_bf16 v[128:131], a[16:19], a[164:167], v[128:131]\n"
    "ds_read_b128 a[8:11], v244 offset:18432\n"
    "v_mfma_f32_16x16x32_bf16 v[128:131], a[20:23], a[168:171], v[128:131]\n"
    "v_mfma_f32_16x16x32_bf16 v[128:131], a[24:27], a[172:175], v[128:131]\n"
    "ds_read_b128 a[12:15], v244 offset:27648\n"
    "v_mfma_f32_16x16x32_bf16 v[128:131], a[28:31], a[176:179], v[128:131]\n"
    "buffer_load_dwordx4 a[232:235], v252, s[44:47], s50 offen offset:1024 sc1\n"
    "s_waitcnt lgkmcnt(4)\n"
    "v_mfma_f32_16x16x32_bf16 v[96:99], a[32:35], a[148:151], v[96:99]\n"
    "ds_read_b128 a[16:19], v244 offset:1024\n"
    "v_mfma_f32_16x16x32_bf16 v[96:99], a[36:39], a[152:155], v[96:99]\n"
    "buffer_load_dwordx4 a[236:239], v252, s[44:47], s50 offen offset:2048 sc1\n"
    "v_mfma_f32_16x16x32_bf16 v[96:99], a[40:43], a[156:159], v[96:99]\n"
    "ds_read_b128 a[20:23], v244 offset:10240\n"
    "v_mfma_f32_16x16x32_bf16 v[96:99], a[44:47], a[160:163], v[96:99]\n"
    "v_mfma_f32_16x16x32_bf16 v[132:135], a[32:35], a[164:167], v[132:135]\n"
    "ds_read_b128 a[24:27], v244 offset:19456\n"
    "v_mfma_f32_16x16x32_bf16 v[132:135], a[36:39], a[168:171], v[132:135]\n"
    "buffer_load_dwordx4 a[240:243], v252, s[44:47], s50 offen offset:3072 sc1\n"
    "s_add_u32 s50, s50, 4096\n"
    "v_mfma_f32_16x16x32_bf16 v[132:135], a[40:43], a[172:175], v[132:135]\n"
    "ds_read_b128 a[28:31], v244 offset:28672\n"
    "v_mfma_f32_16x16x32_bf16 v[132:135], a[44:47], a[176:179], v[132:135]\n"
    "s_cmp_eq_u32 s64, s79\n"
    "s_cbranch_scc1 L_bswS_%=\n"
    "L_bswS_ret_%=:\n"
    "s_sub_u32 s64, s64, 1\n"
    "s_waitcnt lgkmcnt(4)\n"
    "v_mfma_f32_16x16x32_bf16 v[64:67], a[0:3], a[180:183], v[64:67]\n"
    "ds_read_b128 a[32:35], v244 offset:2048\n"
    "v_mfma_f32_16x16x32_bf16 v[64:67], a[4:7], a[184:187], v[64:67]\n"
    "buffer_store_dwordx4 v[160:163], v252, s[56:59], s74 offen offset:2048 sc1\n"
    "v_mfma_f32_16x16x32_bf16 v[64:67], a[8:11], a[188:191], v[64:67]\n"
    "ds_read_b128 a[36:39], v244 offset:11264\n"
    "v_mfma_f32_16x16x32_bf16 v[64:67], a[12:15], a[192:195], v[64:67]\n"
    "buffer_store_dwordx4 v[164:167], v252, s[56:59], s74 offen offset:3072 sc1\n"
    "v_mfma_f32_16x16x32_bf16 v[100:103], a[0:3], a[196:199], v[100:103]\n"
    "ds_read_b128 a[40:43], v244 offset:20480\n"
    "v_mfma_f32_16x16x32_bf16 v[100:103], a[4:7], a[200:203], v[100:103]\n"
    "v_mfma_f32_16x16x32_bf16 v[100:103], a[8:11], a[204:207], v[100:103]\n"
    "ds_read_b128 a[44:47], v244 offset:29696\n"
    "v_mfma_f32_16x16x32_bf16 v[100:103], a[12:15], a[208:211], v[100:103]\n"
    "buffer_store_dwordx4 v[168:171], v252, s[56:59], s75 offen offset:0 sc1\n"
    "s_waitcnt lgkmcnt(4)\n"
    "v_mfma_f32_16x16x32_bf16 v[68:71], a[16:19], a[180:183], v[68:71]\n"
    "ds_read_b128 a[0:3], v244 offset:3072\n"
    "v_mfma_f32_16x16x32_bf16 v[68:71], a[20:23], a[184:187], v[68:71]\n"
    "buffer_store_dwordx4 v[172:175], v252, s[56:59], s75 offen offset:1024 sc1\n"
    "v_mfma_f32_16x16x32_bf16 v[68:71], a[24:27], a[188:191], v[68:71]\n"
    "ds_read_b128 a[4:7], v244 offset:12288\n"
    "v_mfma_f32_16x16x32_bf16 v[68:71], a[28:31], a[192:195], v[68:71]\n"
    "v_mfma_f32_16x16x32_bf16 v[104:107], a[16:19], a[196:199], v[104:107]\n"
    "ds_read_b128 a[8:11], v244 offset:21504\n"
    "v_mfma_f32_16x16x32_bf16 v[104:107], a[20:23], a[200:203], v[104:107]\n"
    "buffer_store_dwordx4 v[176:179], v252, s[56:59], s75 offen offset:2048 sc1\n"
    "v_mfma_f32_16x16x32_bf16 v[104:107], a[24:27], a[204:207], v[104:107]\n"
    "ds_read_b128 a[12:15], v244 offset:30720\n"
    "v_mfma_f32_16x16x32_bf16 v[104:107], a[28:31], a[208:211], v[104:107]\n"
    "s_waitcnt lgkmcnt(4)\n"
    "v_mfma_f32_16x16x32_bf16 v[72:75], a[32:35], a[180:183], v[72:75]\n"
    "ds_read_b128 a[16:19], v244 offset:4096\n"
    "v_mfma_f32_16x16x32_bf16 v[72:75], a[36:39], a[184:187], v[72:75]\n"
    "s_add_u32 m0, s51, 0\n"
    "s_nop 0\n"
    "buffer_load_dwordx4 v252, s[40:43], s48 offen offset:0 lds\n"
    "v_mfma_f32_16x16x32_bf16 v[72:75], a[40:43], a[188:191], v[72:75]\n"
    "ds_read_b128 a[20:23], v244 offset:13312\n"
    "v_mfma_f32_16x16x32_bf16 v[72:75], a[44:47], a[192:195], v[72:75]\n"
    "buffer_load_dwordx4 v252, s[40:43], s48 offen offset:1024 lds\n"
    "v_mfma_f32_16x16x32_bf16 v[108:111], a[32:35], a[196:199], v[108:111]\n"
    "ds_read_b128 a[24:27], v244 offset:22528\n"
    "v_mfma_f32_16x16x32_bf16 v[108:111], a[36:39], a[200:203], v[108:111]\n"
    "v_mfma_f32_16x16x32_bf16 v[108:111], a[40:43], a[204:207], v[108:111]\n"
    "ds_read_b128 a[28:31], v244 offset:31744\n"
    "v_mfma_f32_16x16x32_bf16 v[108:111], a[44:47], a[208:211], v[108:111]\n"
    "buffer_load_dwordx4 v252, s[40:43], s48 offen offset:2048 lds\n"
    "s_waitcnt lgkmcnt(4)\n"
    "v_mfma_f32_16x16x32_bf16 v[76:79], a[0:3], a[180:183], v[76:79]\n"
    "ds_read_b128 a[32:35], v244 offset:5120\n"
    "v_mfma_f32_16x16x32_bf16 v[76:79], a[4:7], a[184:187], v[76:79]\n"
    "v_mfma_f32_16x16x32_bf16 v[76:79], a[8:11], a[188:191], v[76:79]\n"
    "ds_read_b128 a[36:39], v244 offset:14336\n"
    "v_mfma_f32_16x16x32_bf16 v[76:79], a[12:15], a[192:195], v[76:79]\n"
    "buffer_load_dwordx4 v252, s[40:43], s48 offen offset:3072 lds\n"
    "s_add_u32 s48, s48, 4096\n"
    "v_mfma_f32_16x16x32_bf16 v[112:115], a[0:3], a[196:199], v[112:115]\n"
    "ds_read_b128 a[40:43], v244 offset:23552\n"
    "v_mfma_f32_16x16x32_bf16 v[112:115], a[4:7], a[200:203], v[112:115]\n"
    "s_add_u32 m0, s51, 4096\n"
    "s_nop 0\n"
    "buffer_load_dwordx4 v252, s[40:43], s48 offen offset:0 lds\n"
    "v_mfma_f32_16x16x32_bf16 v[112:115], a[8:11], a[204:207], v[112:115]\n"
    "ds_read_b128 a[44:47], v244 offset:32768\n"
    "v_mfma_f32_16x16x32_bf16 v[112:115], a[12:15], a[208:211], v[112:115]\n"
    "s_waitcnt lgkmcnt(4)\n"
    "v_mfma_f32_16x16x32_bf16 v[80:83], a[16:19], a[180:183], v[80:83]\n"
    "ds_read_b128 a[0:3], v244 offset:6144\n"
    "v_mfma_f32_16x16x32_bf16 v[80:83], a[20:23], a[184:187], v[80:83]\n"
    "buffer_load_dwordx4 v252, s[40:43], s48 offen offset:1024 lds\n"
    "v_mfma_f32_16x16x32_bf16 v[80:83], a[24:27], a[188:191], v[80:83]\n"
    "ds_read_b128 a[4:7], v244 offset:15360\n"
    "v_mfma_f32_16x16x32_bf16 v[80:83], a[28:31], a[192:195], v[80:83]\n"
    "v_mfma_f32_16x16x32_bf16 v[116:119], a[16:19], a[196:199], v[116:119]\n"
    "ds_read_b128 a[8:11], v244 offset:24576\n"
    "v_mfma_f32_16x16x32_bf16 v[116:119], a[20:23], a[200:203], v[116:119]\n"
    "buffer_load_dwordx4 v252, s[40:43], s48 offen offset:2048 lds\n"
    "v_mfma_f32_16x16x32_bf16 v[116:119], a[24:27], a[204:207], v[116:119]\n"
    "ds_read_b128 a[12:15], v244 offset:33792\n"
    "v_mfma_f32_16x16x32_bf16 v[116:119], a[28:31], a[208:211], v[116:119]\n"
    "buffer_load_dwordx4 v252, s[40:43], s48 offen offset:3072 lds\n"
    "s_add_u32 s48, s48, 4096\n"
    "s_waitcnt lgkmcnt(4)\n"
    "v_mfma_f32_16x16x32_bf16 v[84:87], a[32:35], a[180:183], v[84:87]\n"
    "ds_read_b128 a[16:19], v244 offset:7168\n"
    "v_mfma_f32_16x16x32_bf16 v[84:87], a[36:39], a[184:187], v[84:87]\n"
    "v_mfma_f32_16x16x32_bf16 v[84:87], a[40:43], a[188:191], v[84:87]\n"
    "ds_read_b128 a[20:23], v244 offset:16384\n"
    "v_mfma_f32_16x16x32_bf16 v[84:87], a[44:47], a[192:195], v[84:87]\n"
    "s_add_u32 m0, s51, 8192\n"
    "s_nop 0\n"
    "buffer_load_dwordx4 v252, s[40:43], s48 offen offset:0 lds\n"
    "s_add_u32 s48, s48, 139264\n"
    "v_mfma_f32_16x16x32_bf16 v[120:123], a[32:35], a[196:199], v[120:123]\n"
    "ds_read_b128 a[24:27], v244 offset:25600\n"
    "v_mfma_f32_16x16x32_bf16 v[120:123], a[36:39], a[200:203], v[120:123]\n"
    "buffer_load_dwordx4 a[148:151], v252, s[44:47], s49 offen offset:0 sc1\n"
    "v_mfma_f32_16x16x32_bf16 v[120:123], a[40:43], a[204:207], v[120:123]\n"
    "ds_read_b128 a[28:31], v244 offset:34816\n"
    "v_mfma_f32_16x16x32_bf16 v[120:123], a[44:47], a[208:211], v[120:123]\n"
    "s_waitcnt lgkmcnt(4)\n"
    "v_mfma_f32_16x16x32_bf16 v[88:91], a[0:3], a[180:183], v[88:91]\n"
    "ds_read_b128 a[32:35], v244 offset:8192\n"
    "v_mfma_f32_16x16x32_bf16 v[88:91], a[4:7], a[184:187], v[88:91]\n"
    "buffer_load_dwordx4 a[152:155], v252, s[44:47], s49 offen offset:1024 sc1\n"
    "v_mfma_f32_16x16x32_bf16 v[88:91], a[8:11], a[188:191], v[88:91]\n"
    "ds_read_b128 a[36:39], v244 offset:17408\n"
    "v_mfma_f32_16x16x32_bf16 v[88:91], a[12:15], a[192:195], v[88:91]\n"
    "v_mfma_f32_16x16x32_bf16 v[124:127], a[0:3], a[196:199], v[124:127]\n"
    "ds_read_b128 a[40:43], v244 offset:26624\n"
    "v_mfma_f32_16x16x32_bf16 v[124:127], a[4:7], a[200:203], v[124:127]\n"
    "buffer_load_dwordx4 a[156:159], v252, s[44:47], s49 offen offset:2048 sc1\n"
    "v_mfma_f32_16x16x32_bf16 v[124:127], a[8:11], a[204:207], v[124:127]\n"
    "ds_read_b128 a[44:47], v244 offset:35840\n"
    "v_mfma_f32_16x16x32_bf16 v[124:127], a[12:15], a[208:211], v[124:127]\n"
    "buffer_load_dwordx4 a[160:163], v252, s[44:47], s49 offen offset:3072 sc1\n"
    "s_add_u32 s49, s49, 4096\n"
    "s_waitcnt vmcnt(18) lgkmcnt(0)\n"
    "s_barrier\n"
    "v_add_u32 v244, 36864, v244\n"
    "v_mfma_f32_16x16x32_bf16 v[92:95], a[16:19], a[180:183], v[92:95]\n"
    "ds_read_b128 a[0:3], v244 offset:0\n"
    "v_mfma_f32_16x16x32_bf16 v[92:95], a[20:23], a[184:187], v[92:95]\n"
    "v_mfma_f32_16x16x32_bf16 v[92:95], a[24:27], a[188:191], v[92:95]\n"
    "ds_read_b128 a[4:7], v244 offset:9216\n"
    "v_mfma_f32_16x16x32_bf16 v[92:95], a[28:31], a[192:195], v[92:95]\n"
    "buffer_load_dwordx4 a[164:167], v252, s[44:47], s50 offen offset:0 sc1\n"
    "v_mfma_f32_16x16x32_bf16 v[128:131], a[16:19], a[196:199], v[128:131]\n"
    "ds_read_b128 a[8:11], v244 offset:18432\n"
    "v_mfma_f32_16x16x32_bf16 v[128:131], a[20:23], a[200:203], v[128:131]\n"
    "v_mfma_f32_16x16x32_bf16 v[128:131], a[24:27], a[204:207], v[128:131]\n"
    "ds_read_b128 a[12:15], v244 offset:27648\n"
    "v_mfma_f32_16x16x32_bf16 v[128:131], a[28:31], a[208:211], v[128:131]\n"
    "buffer_load_dwordx4 a[168:171], v252, s[44:47], s50 offen offset:1024 sc1\n"
    "s_waitcnt lgkmcnt(4)\n"
    "v_mfma_f32_16x16x32_bf16 v[96:99], a[32:35], a[180:183], v[96:99]\n"
    "ds_read_b128 a[16:19], v244 offset:1024\n"
    "v_mfma_f32_16x16x32_bf16 v[96:99], a[36:39], a[184:187], v[96:99]\n"
    "buffer_load_dwordx4 a[172:175], v252, s[44:47], s50 offen offset:2048 sc1\n"
    "v_mfma_f32_16x16x32_bf16 v[96:99], a[40:43], a[188:191], v[96:99]\n"
    "ds_read_b128 a[20:23], v244 offset:10240\n"
    "v_mfma_f32_16x16x32_bf16 v[96:99], a[44:47], a[192:195], v[96:99]\n"
    "v_mfma_f32_16x16x32_bf16 v[132:135], a[32:35], a[196:199], v[132:135]\n"
    "ds_read_b128 a[24:27], v244 offset:19456\n"
    "v_mfma_f32_16x16x32_bf16 v[132:135], a[36:39], a[200:203], v[132:135]\n"
    "buffer_load_dwordx4 a[176:179], v252, s[44:47], s50 offen offset:3072 sc1\n"
    "s_add_u32 s50, s50, 4096\n"
    "v_mfma_f32_16x16x32_bf16 v[132:135], a[40:43], a[204:207], v[132:135]\n"
    "ds_read_b128 a[28:31], v244 offset:28672\n"
    "v_mfma_f32_16x16x32_bf16 v[132:135], a[44:47], a[208:211], v[132:135]\n"
    "s_sub_u32 s64, s64, 1\n"
    "s_waitcnt lgkmcnt(4)\n"
    "v_mfma_f32_16x16x32_bf16 v[64:67], a[0:3], a[212:215], v[64:67]\n"
    "ds_read_b128 a[32:35], v244 offset:2048\n"
    "v_mfma_f32_16x16x32_bf16 v[64:67], a[4:7], a[216:219], v[64:67]\n"
    "buffer_store_dwordx4 v[180:183], v252, s[56:59], s75 offen offset:3072 sc1\n"
    "v_mfma_f32_16x16x32_bf16 v[64:67], a[8:11], a[220:223], v[64:67]\n"
    "ds_read_b128 a[36:39], v244 offset:11264\n"
    "v_mfma_f32_16x16x32_bf16 v[64:67], a[12:15], a[224:227], v[64:67]\n"
    "buffer_store_dwordx4 v[184:187], v252, s[56:59], s76 offen offset:0 sc1\n"
    "v_mfma_f32_16x16x32_bf16 v[100:103], a[0:3], a[228:231], v[100:103]\n"
    "ds_read_b128 a[40:43], v244 offset:20480\n"
    "v_mfma_f32_16x16x32_bf16 v[100:103], a[4:7], a[232:235], v[100:103]\n"
    "v_mfma_f32_16x16x32_bf16 v[100:103], a[8:11], a[236:239], v[100:103]\n"
    "ds_read_b128 a[44:47], v244 offset:29696\n"
    "v_mfma_f32_16x16x32_bf16 v[100:103], a[12:15], a[240:243], v[100:103]\n"
    "buffer_store_dwordx4 v[188:191], v252, s[56:59], s76 offen offset:1024 sc1\n"
    "s_waitcnt lgkmcnt(4)\n"
    "v_mfma_f32_16x16x32_bf16 v[68:71], a[16:19], a[212:215], v[68:71]\n"
    "ds_read_b128 a[0:3], v244 offset:3072\n"
    "v_mfma_f32_16x16x32_bf16 v[68:71], a[20:23], a[216:219], v[68:71]\n"
    "buffer_store_dwordx4 v[192:195], v252, s[56:59], s76 offen offset:2048 sc1\n"
    "v_mfma_f32_16x16x32_bf16 v[68:71], a[24:27], a[220:223], v[68:71]\n"
    "ds_read_b128 a[4:7], v244 offset:12288\n"
    "v_mfma_f32_16x16x32_bf16 v[68:71], a[28:31], a[224:227], v[68:71]\n"
    "v_mfma_f32_16x16x32_bf16 v[104:107], a[16:19], a[228:231], v[104:107]\n"
    "ds_read_b128 a[8:11], v244 offset:21504\n"
    "v_mfma_f32_16x16x32_bf16 v[104:107], a[20:23], a[232:235], v[104:107]\n"
    "buffer_store_dwordx4 v[196:199], v252, s[56:59], s76 offen offset:3072 sc1\n"
    "v_mfma_f32_16x16x32_bf16 v[104:107], a[24:27], a[236:239], v[104:107]\n"
    "ds_read_b128 a[12:15], v244 offset:30720\n"
    "v_mfma_f32_16x16x32_bf16 v[104:107], a[28:31], a[240:243], v[104:107]\n"
    "s_waitcnt lgkmcnt(4)\n"
    "v_mfma_f32_16x16x32_bf16 v[72:75], a[32:35], a[212:215], v[72:75]\n"
    "ds_read_b128 a[16:19], v244 offset:4096\n"
    "v_mfma_f32_16x16x32_bf16 v[72:75], a[36:39], a[216:219], v[72:75]\n"
    "s_add_u32 m0, s51, 36864\n"
    "s_nop 0\n"
    "buffer_load_dwordx4 v252, s[40:43], s48 offen offset:0 lds\n"
    "v_mfma_f32_16x16x32_bf16 v[72:75], a[40:43], a[220:223], v[72:75]\n"
    "ds_read_b128 a[20:23], v244 offset:13312\n"
    "v_mfma_f32_16x16x32_bf16 v[72:75], a[44:47], a[224:227], v[72:75]\n"
    "buffer_load_dwordx4 v252, s[40:43], s48 offen offset:1024 lds\n"
    "v_mfma_f32_16x16x32_bf16 v[108:111], a[32:35], a[228:231], v[108:111]\n"
    "ds_read_b128 a[24:27], v244 offset:22528\n"
    "v_mfma_f32_16x16x32_bf16 v[108:111], a[36:39], a[232:235], v[108:111]\n"
    "v_mfma_f32_16x16x32_bf16 v[108:111], a[40:43], a[236:239], v[108:111]\n"
    "ds_read_b128 a[28:31], v244 offset:31744\n"
    "v_mfma_f32_16x16x32_bf16 v[108:111], a[44:47], a[240:243], v[108:111]\n"
    "buffer_load_dwordx4 v252, s[40:43], s48 offen offset:2048 lds\n"
    "s_waitcnt lgkmcnt(4)\n"
    "v_mfma_f32_16x16x32_bf16 v[76:79], a[0:3], a[212:215], v[76:79]\n"
    "ds_read_b128 a[32:35], v244 offset:5120\n"
    "v_mfma_f32_16x16x32_bf16 v[76:79], a[4:7], a[216:219], v[76:79]\n"
    "v_mfma_f32_16x16x32_bf16 v[76:79], a[8:11], a[220:223], v[76:79]\n"
    "ds_read_b128 a[36:39], v244 offset:14336\n"
    "v_mfma_f32_16x16x32_bf16 v[76:79], a[12:15], a[224:227], v[76:79]\n"
    "buffer_load_dwordx4 v252, s[40:43], s48 offen offset:3072 lds\n"
    "s_add_u32 s48, s48, 4096\n"
    "v_mfma_f32_16x16x32_bf16 v[112:115], a[0:3], a[228:231], v[112:115]\n"
    "ds_read_b128 a[40:43], v244 offset:23552\n"
    "v_mfma_f32_16x16x32_bf16 v[112:115], a[4:7], a[232:235], v[112:115]\n"
    "s_add_u32 m0, s51, 40960\n"
    "s_nop 0\n"
    "buffer_load_dwordx4 v252, s[40:43], s48 offen offset:0 lds\n"
    "v_mfma_f32_16x16x32_bf16 v[112:115], a[8:11], a[236:239], v[112:115]\n"
    "ds_read_b128 a[44:47], v244 offset:32768\n"
    "v_mfma_f32_16x16x32_bf16 v[112:115], a[12:15], a[240:243], v[112:115]\n"
    "s_waitcnt lgkmcnt(4)\n"
    "v_mfma_f32_16x16x32_bf16 v[80:83], a[16:19], a[212:215], v[80:83]\n"
    "ds_read_b128 a[0:3], v244 offset:6144\n"
    "v_mfma_f32_16x16x32_bf16 v[80:83], a[20:23], a[216:219], v[80:83]\n"
    "buffer_load_dwordx4 v252, s[40:43], s48 offen offset:1024 lds\n"
    "v_mfma_f32_16x16x32_bf16 v[80:83], a[24:27], a[220:223], v[80:83]\n"
    "ds_read_b128 a[4:7], v244 offset:15360\n"
    "v_mfma_f32_16x16x32_bf16 v[80:83], a[28:31], a[224:227], v[80:83]\n"
    "v_mfma_f32_16x16x32_bf16 v[116:119], a[16:19], a[228:231], v[116:119]\n"
    "ds_read_b128 a[8:11], v244 offset:24576\n"
    "v_mfma_f32_16x16x32_bf16 v[116:119], a[20:23], a[232:235], v[116:119]\n"
    "buffer_load_dwordx4 v252, s[40:43], s48 offen offset:2048 lds\n"
    "v_mfma_f32_16x16x32_bf16 v[116:119], a[24:27], a[236:239], v[116:119]\n"
    "ds_read_b128 a[12:15], v244 offset:33792\n"
    "v_mfma_f32_16x16x32_bf16 v[116:119], a[28:31], a[240:243], v[116:119]\n"
    "buffer_load_dwordx4 v252, s[40:43], s48 offen offset:3072 lds\n"
    "s_add_u32 s48, s48, 4096\n"
    "s_waitcnt lgkmcnt(4)\n"
    "v_mfma_f32_16x16x32_bf16 v[84:87], a[32:35], a[212:215], v[84:87]\n"
    "ds_read_b128 a[16:19], v244 offset:7168\n"
    "v_mfma_f32_16x16x32_bf16 v[84:87], a[36:39], a[216:219], v[84:87]\n"
    "v_mfma_f32_16x16x32_bf16 v[84:87], a[40:43], a[220:223], v[84:87]\n"
    "ds_read_b128 a[20:23], v244 offset:16384\n"
    "v_mfma_f32_16x16x32_bf16 v[84:87], a[44:47], a[224:227], v[84:87]\n"
    "s_add_u32 m0, s51, 45056\n"
    "s_nop 0\n"
    "buffer_load_dwordx4 v252, s[40:43], s48 offen offset:0 lds\n"
    "s_add_u32 s48, s48, 139264\n"
    "v_mfma_f32_16x16x32_bf16 v[120:123], a[32:35], a[228:231], v[120:123]\n"
    "ds_read_b128 a[24:27], v244 offset:25600\n"
    "v_mfma_f32_16x16x32_bf16 v[120:123], a[36:39], a[232:235], v[120:123]\n"
    "buffer_load_dwordx4 a[180:183], v252, s[44:47], s49 offen offset:0 sc1\n"
    "v_mfma_f32_16x16x32_bf16 v[120:123], a[40:43], a[236:239], v[120:123]\n"
    "ds_read_b128 a[28:31], v244 offset:34816\n"
    "v_mfma_f32_16x16x32_bf16 v[120:123], a[44:47], a[240:243], v[120:123]\n"
    "s_waitcnt lgkmcnt(4)\n"
    "v_mfma_f32_16x16x32_bf16 v[88:91], a[0:3], a[212:215], v[88:91]\n"
    "ds_read_b128 a[32:35], v244 offset:8192\n"
    "v_mfma_f32_16x16x32_bf16 v[88:91], a[4:7], a[216:219], v[88:91]\n"
    "buffer_load_dwordx4 a[184:187], v252, s[44:47], s49 offen offset:1024 sc1\n"
    "v_mfma_f32_16x16x32_bf16 v[88:91], a[8:11], a[220:223], v[88:91]\n"
    "ds_read_b128 a[36:39], v244 offset:17408\n"
    "v_mfma_f32_16x16x32_bf16 v[88:91], a[12:15], a[224:227], v[88:91]\n"
    "v_mfma_f32_16x16x32_bf16 v[124:127], a[0:3], a[228:231], v[124:127]\n"
    "ds_read_b128 a[40:43], v244 offset:26624\n"
    "v_mfma_f32_16x16x32_bf16 v[124:127], a[4:7], a[232:235], v[124:127]\n"
    "buffer_load_dwordx4 a[188:191], v252, s[44:47], s49 offen offset:2048 sc1\n"
    "v_mfma_f32_16x16x32_bf16 v[124:127], a[8:11], a[236:239], v[124:127]\n"
    "ds_read_b128 a[44:47], v244 offset:35840\n"
    "v_mfma_f32_16x16x32_bf16 v[124:127], a[12:15], a[240:243], v[124:127]\n"
    "buffer_load_dwordx4 a[192:195], v252, s[44:47], s49 offen offset:3072 sc1\n"
    "s_add_u32 s49, s49, 4096\n"
    "s_waitcnt vmcnt(18) lgkmcnt(0)\n"
    "s_barrier\n"
    "v_add_u32 v244, 4294893568, v244\n"
    "v_mfma_f32_16x16x32_bf16 v[92:95], a[16:19], a[212:215], v[92:95]\n"
    "ds_read_b128 a[0:3], v244 offset:0\n"
    "v_mfma_f32_16x16x32_bf16 v[92:95], a[20:23], a[216:219], v[92:95]\n"
    "v_mfma_f32_16x16x32_bf16 v[92:95], a[24:27], a[220:223], v[92:95]\n"
    "ds_read_b128 a[4:7], v244 offset:9216\n"
    "v_mfma_f32_16x16x32_bf16 v[92:95], a[28:31], a[224:227], v[92:95]\n"
    "buffer_load_dwordx4 a[196:199], v252, s[44:47], s50 offen offset:0 sc1\n"
    "v_mfma_f32_16x16x32_bf16 v[128:131], a[16:19], a[228:231], v[128:131]\n"
    "ds_read_b128 a[8:11], v244 offset:18432\n"
    "v_mfma_f32_16x16x32_bf16 v[128:131], a[20:23], a[232:235], v[128:131]\n"
    "v_mfma_f32_16x16x32_bf16 v[128:131], a[24:27], a[236:239], v[128:131]\n"
    "ds_read_b128 a[12:15], v244 offset:27648\n"
    "v_mfma_f32_16x16x32_bf16 v[128:131], a[28:31], a[240:243], v[128:131]\n"
    "buffer_load_dwordx4 a[200:203], v252, s[44:47], s50 offen offset:1024 sc1\n"
    "s_waitcnt lgkmcnt(4)\n"
    "v_mfma_f32_16x16x32_bf16 v[96:99], a[32:35], a[212:215], v[96:99]\n"
    "ds_read_b128 a[16:19], v244 offset:1024\n"
    "v_mfma_f32_16x16x32_bf16 v[96:99], a[36:39], a[216:219], v[96:99]\n"
    "buffer_load_dwordx4 a[204:207], v252, s[44:47], s50 offen offset:2048 sc1\n"
    "v_mfma_f32_16x16x32_bf16 v[96:99], a[40:43], a[220:223], v[96:99]\n"
    "ds_read_b128 a[20:23], v244 offset:10240\n"
    "v_mfma_f32_16x16x32_bf16 v[96:99], a[44:47], a[224:227], v[96:99]\n"
    "v_mfma_f32_16x16x32_bf16 v[132:135], a[32:35], a[228:231], v[132:135]\n"
    "ds_read_b128 a[24:27], v244 offset:19456\n"
    "v_mfma_f32_16x16x32_bf16 v[132:135], a[36:39], a[232:235], v[132:135]\n"
    "buffer_load_dwordx4 a[208:211], v252, s[44:47], s50 offen offset:3072 sc1\n"
    "s_add_u32 s50, s50, 4096\n"
    "v_mfma_f32_16x16x32_bf16 v[132:135], a[40:43], a[236:239], v[132:135]\n"
    "ds_read_b128 a[28:31], v244 offset:28672\n"
    "v_mfma_f32_16x16x32_bf16 v[132:135], a[44:47], a[240:243], v[132:135]\n"
    "s_sub_u32 s64, s64, 1\n"
    "s_branch L_btop_%=\n"
    "L_bN_%=:\n"
    "s_waitcnt lgkmcnt(4)\n"
    "v_mfma_f32_16x16x32_bf16 v[64:67], a[0:3], a[148:151], v[64:67]\n"
    "ds_read_b128 a[32:35], v244 offset:2048\n"
    "v_mfma_f32_16x16x32_bf16 v[64:67], a[4:7], a[152:155], v[64:67]\n"
    "s_add_u32 m0, s51, 73728\n"
    "s_nop 0\n"
    "buffer_load_dwordx4 v252, s[40:43], s48 offen offset:0 lds\n"
    "v_mfma_f32_16x16x32_bf16 v[64:67], a[8:11], a[156:159], v[64:67]\n"
    "ds_read_b128 a[36:39], v244 offset:11264\n"
    "v_mfma_f32_16x16x32_bf16 v[64:67], a[12:15], a[160:163], v[64:67]\n"
    "v_mfma_f32_16x16x32_bf16 v[100:103], a[0:3], a[164:167], v[100:103]\n"
    "ds_read_b128 a[40:43], v244 offset:20480\n"
    "v_mfma_f32_16x16x32_bf16 v[100:103], a[4:7], a[168:171], v[100:103]\n"
    "buffer_load_dwordx4 v252, s[40:43], s48 offen offset:1024 lds\n"
    "v_mfma_f32_16x16x32_bf16 v[100:103], a[8:11], a[172:175], v[100:103]\n"
    "ds_read_b128 a[44:47], v244 offset:29696\n"
    "v_mfma_f32_16x16x32_bf16 v[100:103], a[12:15], a[176:179], v[100:103]\n"
    "s_waitcnt lgkmcnt(4)\n"
    "v_mfma_f32_16x16x32_bf16 v[68:71], a[16:19], a[148:151], v[68:71]\n"
    "ds_read_b128 a[0:3], v244 offset:3072\n"
    "v_mfma_f32_16x16x32_bf16 v[68:71], a[20:23], a[152:155], v[68:71]\n"
    "buffer_load_dwordx4 v252, s[40:43], s48 offen offset:2048 lds\n"
    "v_mfma_f32_16x16x32_bf16 v[68:71], a[24:27], a[156:159], v[68:71]\n"
    "ds_read_b128 a[4:7], v244 offset:12288\n"
    "v_mfma_f32_16x16x32_bf16 v[68:71], a[28:31], a[160:163], v[68:71]\n"
    "v_mfma_f32_16x16x32_bf16 v[104:107], a[16:19], a[164:167], v[104:107]\n"
    "ds_read_b128 a[8:11], v244 offset:21504\n"
    "v_mfma_f32_16x16x32_bf16 v[104:107], a[20:23], a[168:171], v[104:107]\n"
    "buffer_load_dwordx4 v252, s[40:43], s48 offen offset:3072 lds\n"
    "s_add_u32 s48, s48, 4096\n"
    "v_mfma_f32_16x16x32_bf16 v[104:107], a[24:27], a[172:175], v[104:107]\n"
    "ds_read_b128 a[12:15], v244 offset:30720\n"
    "v_mfma_f32_16x16x32_bf16 v[104:107], a[28:31], a[176:179], v[104:107]\n"
    "s_waitcnt lgkmcnt(4)\n"
    "v_mfma_f32_16x16x32_bf16 v[72:75], a[32:35], a[148:151], v[72:75]\n"
    "ds_read_b128 a[16:19], v244 offset:4096\n"
    "v_mfma_f32_16x16x32_bf16 v[72:75], a[36:39], a[152:155], v[72:75]\n"
    "s_add_u32 m0, s51, 77824\n"
    "s_nop 0\n"
    "buffer_load_dwordx4 v252, s[40:43], s48 offen offset:0 lds\n"
    "v_mfma_f32_16x16x32_bf16 v[72:75], a[40:43], a[156:159], v[72:75]\n"
    "ds_read_b128 a[20:23], v244 offset:13312\n"
    "v_mfma_f32_16x16x32_bf16 v[72:75], a[44:47], a[160:163], v[72:75]\n"
    "v_mfma_f32_16x16x32_bf16 v[108:111], a[32:35], a[164:167], v[108:111]\n"
    "ds_read_b128 a[24:27], v244 offset:22528\n"
    "v_mfma_f32_16x16x32_bf16 v[108:111], a[36:39], a[168:171], v[108:111]\n"
    "buffer_load_dwordx4 v252, s[40:43], s48 offen offset:1024 lds\n"
    "v_mfma_f32_16x16x32_bf16 v[108:111], a[40:43], a[172:175], v[108:111]\n"
    "ds_read_b128 a[28:31], v244 offset:31744\n"
    "v_mfma_f32_16x16x32_bf16 v[108:111], a[44:47], a[176:179], v[108:111]\n"
    "s_waitcnt lgkmcnt(4)\n"
    "v_mfma_f32_16x16x32_bf16 v[76:79], a[0:3], a[148:151], v[76:79]\n"
    "ds_read_b128 a[32:35], v244 offset:5120\n"
    "v_mfma_f32_16x16x32_bf16 v[76:79], a[4:7], a[152:155], v[76:79]\n"
    "buffer_load_dwordx4 v252, s[40:43], s48 offen offset:2048 lds\n"
    "v_mfma_f32_16x16x32_bf16 v[76:79], a[8:11], a[156:159], v[76:79]\n"
    "ds_read_b128 a[36:39], v244 offset:14336\n"
    "v_mfma_f32_16x16x32_bf16 v[76:79], a[12:15], a[160:163], v[76:79]\n"
    "v_mfma_f32_16x16x32_bf16 v[112:115], a[0:3], a[164:167], v[112:115]\n"
    "ds_read_b128 a[40:43], v244 offset:23552\n"
    "v_mfma_f32_16x16x32_bf16 v[112:115], a[4:7], a[168:171], v[112:115]\n"
    "buffer_load_dwordx4 v252, s[40:43], s48 offen offset:3072 lds\n"
    "s_add_u32 s48, s48, 4096\n"
    "v_mfma_f32_16x16x32_bf16 v[112:115], a[8:11], a[172:175], v[112:115]\n"
    "ds_read_b128 a[44:47], v244 offset:32768\n"
    "v_mfma_f32_16x16x32_bf16 v[112:115], a[12:15], a[176:179], v[112:115]\n"
    "s_waitcnt lgkmcnt(4)\n"
    "v_mfma_f32_16x16x32_bf16 v[80:83], a[16:19], a[148:151], v[80:83]\n"
    "ds_read_b128 a[0:3], v244 offset:6144\n"
    "v_mfma_f32_16x16x32_bf16 v[80:83], a[20:23], a[152:155], v[80:83]\n"
    "s_add_u32 m0, s51, 81920\n"
    "s_nop 0\n"
    "buffer_load_dwordx4 v252, s[40:43], s48 offen offset:0 lds\n"
    "s_add_u32 s48, s48, 139264\n"
    "v_mfma_f32_16x16x32_bf16 v[80:83], a[24:27], a[156:159], v[80:83]\n"
    "ds_read_b128 a[4:7], v244 offset:15360\n"
    "v_mfma_f32_16x16x32_bf16 v[80:83], a[28:31], a[160:163], v[80:83]\n"
    "v_mfma_f32_16x16x32_bf16 v[116:119], a[16:19], a[164:167], v[116:119]\n"
    "ds_read_b128 a[8:11], v244 offset:24576\n"
    "v_mfma_f32_16x16x32_bf16 v[116:119], a[20:23], a[168:171], v[116:119]\n"
    "v_mfma_f32_16x16x32_bf16 v[116:119], a[24:27], a[172:175], v[116:119]\n"
    "ds_read_b128 a[12:15], v244 offset:33792\n"
    "v_mfma_f32_16x16x32_bf16 v[116:119], a[28:31], a[176:179], v[116:119]\n"
    "buffer_load_dwordx4 a[212:215], v252, s[44:47], s49 offen offset:0 sc1\n"
    "s_waitcnt lgkmcnt(4)\n"
    "v_mfma_f32_16x16x32_bf16 v[84:87], a[32:35], a[148:151], v[84:87]\n"
    "ds_read_b128 a[16:19], v244 offset:7168\n"
    "v_mfma_f32_16x16x32_bf16 v[84:87], a[36:39], a[152:155], v[84:87]\n"
    "v_mfma_f32_16x16x32_bf16 v[84:87], a[40:43], a[156:159], v[84:87]\n"
    "ds_read_b128 a[20:23], v244 offset:16384\n"
    "v_mfma_f32_16x16x32_bf16 v[84:87], a[44:47], a[160:163], v[84:87]\n"
    "buffer_load_dwordx4 a[216:219], v252, s[44:47], s49 offen offset:1024 sc1\n"
    "v_mfma_f32_16x16x32_bf16 v[120:123], a[32:35], a[164:167], v[120:123]\n"
    "ds_read_b128 a[24:27], v244 offset:25600\n"
    "v_mfma_f32_16x16x32_bf16 v[120:123], a[36:39], a[168:171], v[120:123]\n"
    "v_mfma_f32_16x16x32_bf16 v[120:123], a[40:43], a[172:175], v[120:123]\n"
    "ds_read_b128 a[28:31], v244 offset:34816\n"
    "v_mfma_f32_16x16x32_bf16 v[120:123], a[44:47], a[176:179], v[120:123]\n"
    "buffer_load_dwordx4 a[220:223], v252, s[44:47], s49 offen offset:2048 sc1\n"
    "s_waitcnt lgkmcnt(4)\n"
    "v_mfma_f32_16x16x32_bf16 v[88:91], a[0:3], a[148:151], v[88:91]\n"
    "ds_read_b128 a[32:35], v244 offset:8192\n"
    "v_mfma_f32_16x16x32_bf16 v[88:91], a[4:7], a[152:155], v[88:91]\n"
    "v_mfma_f32_16x16x32_bf16 v[88:91], a[8:11], a[156:159], v[88:91]\n"
    "ds_read_b128 a[36:39], v244 offset:17408\n"
    "v_mfma_f32_16x16x32_bf16 v[88:91], a[12:15], a[160:163], v[88:91]\n"
    "buffer_load_dwordx4 a[224:227], v252, s[44:47], s49 offen offset:3072 sc1\n"
    "s_add_u32 s49, s49, 4096\n"
    "v_mfma_f32_16x16x32_bf16 v[124:127], a[0:3], a[164:167], v[124:127]\n"
    "ds_read_b128 a[40:43], v244 offset:26624\n"
    "v_mfma_f32_16x16x32_bf16 v[124:127], a[4:7], a[168:171], v[124:127]\n"
    "v_mfma_f32_16x16x32_bf16 v[124:127], a[8:11], a[172:175], v[124:127]\n"
    "ds_read_b128 a[44:47], v244 offset:35840\n"
    "v_mfma_f32_16x16x32_bf16 v[124:127], a[12:15], a[176:179], v[124:127]\n"
    "buffer_load_dwordx4 a[228:231], v252, s[44:47], s50 offen offset:0 sc1\n"
    "s_waitcnt vmcnt(14) lgkmcnt(0)\n"
    "s_barrier\n"
    "v_add_u32 v244, 36864, v244\n"
    "s_cmp_eq_u32 s64, s77\n"
    "s_cbranch_scc1 L_bb0_%=\n"
    "L_bb0_ret_%=:\n"
    "v_mfma_f32_16x16x32_bf16 v[92:95], a[16:19], a[148:151], v[92:95]\n"
    "ds_read_b128 a[0:3], v244 offset:0\n"
    "v_mfma_f32_16x16x32_bf16 v[92:95], a[20:23], a[152:155], v[92:95]\n"
    "v_mfma_f32_16x16x32_bf16 v[92:95], a[24:27], a[156:159], v[92:95]\n"
    "ds_read_b128 a[4:7], v244 offset:9216\n"
    "v_mfma_f32_16x16x32_bf16 v[92:95], a[28:31], a[160:163], v[92:95]\n"
    "buffer_load_dwordx4 a[232:235], v252, s[44:47], s50 offen offset:1024 sc1\n"
    "v_mfma_f32_16x16x32_bf16 v[128:131], a[16:19], a[164:167], v[128:131]\n"
    "ds_read_b128 a[8:11], v244 offset:18432\n"
    "v_mfma_f32_16x16x32_bf16 v[128:131], a[20:23], a[168:171], v[128:131]\n"
    "v_mfma_f32_16x16x32_bf16 v[128:131], a[24:27], a[172:175], v[128:131]\n"
    "ds_read_b128 a[12:15], v244 offset:27648\n"
    "v_mfma_f32_16x16x32_bf16 v[128:131], a[28:31], a[176:179], v[128:131]\n"
    "buffer_load_dwordx4 a[236:239], v252, s[44:47], s50 offen offset:2048 sc1\n"
    "s_waitcnt lgkmcnt(4)\n"
    "v_mfma_f32_16x16x32_bf16 v[96:99], a[32:35], a[148:151], v[96:99]\n"
    "ds_read_b128 a[16:19], v244 offset:1024\n"
    "v_mfma_f32_16x16x32_bf16 v[96:99], a[36:39], a[152:155], v[96:99]\n"
    "v_mfma_f32_16x16x32_bf16 v[96:99], a[40:43], a[156:159], v[96:99]\n"
    "ds_read_b128 a[20:23], v244 offset:10240\n"
    "v_mfma_f32_16x16x32_bf16 v[96:99], a[44:47], a[160:163], v[96:99]\n"
    "buffer_load_dwordx4 a[240:243], v252, s[44:47], s50 offen offset:3072 sc1\n"
    "s_add_u32 s50, s50, 4096\n"
    "v_mfma_f32_16x16x32_bf16 v[132:135], a[32:35], a[164:167], v[132:135]\n"
    "ds_read_b128 a[24:27], v244 offset:19456\n"
    "v_mfma_f32_16x16x32_bf16 v[132:135], a[36:39], a[168:171], v[132:135]\n"
    "v_mfma_f32_16x16x32_bf16 v[132:135], a[40:43], a[172:175], v[132:135]\n"
    "ds_read_b128 a[28:31], v244 offset:28672\n"
    "v_mfma_f32_16x16x32_bf16 v[132:135], a[44:47], a[176:179], v[132:135]\n"
    "s_cmp_eq_u32 s64, s79\n"
    "s_cbranch_scc1 L_bswN_%=\n"
    "L_bswN_ret_%=:\n"
    "s_sub_u32 s64, s64, 1\n"
    "s_waitcnt lgkmcnt(4)\n"
    "v_mfma_f32_16x16x32_bf16 v[64:67], a[0:3], a[180:183], v[64:67]\n"
    "ds_read_b128 a[32:35], v244 offset:2048\n"
    "v_mfma_f32_16x16x32_bf16 v[64:67], a[4:7], a[184:187], v[64:67]\n"
    "s_add_u32 m0, s51, 0\n"
    "s_nop 0\n"
    "buffer_load_dwordx4 v252, s[40:43], s48 offen offset:0 lds\n"
    "v_mfma_f32_16x16x32_bf16 v[64:67], a[8:11], a[188:191], v[64:67]\n"
    "ds_read_b128 a[36:39], v244 offset:11264\n"
    "v_mfma_f32_16x16x32_bf16 v[64:67], a[12:15], a[192:195], v[64:67]\n"
    "v_mfma_f32_16x16x32_bf16 v[100:103], a[0:3], a[196:199], v[100:103]\n"
    "ds_read_b128 a[40:43], v244 offset:20480\n"
    "v_mfma_f32_16x16x32_bf16 v[100:103], a[4:7], a[200:203], v[100:103]\n"
    "buffer_load_dwordx4 v252, s[40:43], s48 offen offset:1024 lds\n"
    "v_mfma_f32_16x16x32_bf16 v[100:103], a[8:11], a[204:207], v[100:103]\n"
    "ds_read_b128 a[44:47], v244 offset:29696\n"
    "v_mfma_f32_16x16x32_bf16 v[100:103], a[12:15], a[208:211], v[100:103]\n"
    "s_waitcnt lgkmcnt(4)\n"
    "v_mfma_f32_16x16x32_bf16 v[68:71], a[16:19], a[180:183], v[68:71]\n"
    "ds_read_b128 a[0:3], v244 offset:3072\n"
    "v_mfma_f32_16x16x32_bf16 v[68:71], a[20:23], a[184:187], v[68:71]\n"
    "buffer_load_dwordx4 v252, s[40:43], s48 offen offset:2048 lds\n"
    "v_mfma_f32_16x16x32_bf16 v[68:71], a[24:27], a[188:191], v[68:71]\n"
    "ds_read_b128 a[4:7], v244 offset:12288\n"
    "v_mfma_f32_16x16x32_bf16 v[68:71], a[28:31], a[192:195], v[68:71]\n"
    "v_mfma_f32_16x16x32_bf16 v[104:107], a[16:19], a[196:199], v[104:107]\n"
    "ds_read_b128 a[8:11], v244 offset:21504\n"
    "v_mfma_f32_16x16x32_bf16 v[104:107], a[20:23], a[200:203], v[104:107]\n"
    "buffer_load_dwordx4 v252, s[40:43], s48 offen offset:3072 lds\n"
    "s_add_u32 s48, s48, 4096\n"
    "v_mfma_f32_16x16x32_bf16 v[104:107], a[24:27], a[204:207], v[104:107]\n"
    "ds_read_b128 a[12:15], v244 offset:30720\n"
    "v_mfma_f32_16x16x32_bf16 v[104:107], a[28:31], a[208:211], v[104:107]\n"
    "s_waitcnt lgkmcnt(4)\n"
    "v_mfma_f32_16x16x32_bf16 v[72:75], a[32:35], a[180:183], v[72:75]\n"
    "ds_read_b128 a[16:19], v244 offset:4096\n"
    "v_mfma_f32_16x16x32_bf16 v[72:75], a[36:39], a[184:187], v[72:75]\n"
    "s_add_u32 m0, s51, 4096\n"
    "s_nop 0\n"
    "buffer_load_dwordx4 v252, s[40:43], s48 offen offset:0 lds\n"
    "v_mfma_f32_16x16x32_bf16 v[72:75], a[40:43], a[188:191], v[72:75]\n"
    "ds_read_b128 a[20:23], v244 offset:13312\n"
    "v_mfma_f32_16x16x32_bf16 v[72:75], a[44:47], a[192:195], v[72:75]\n"
    "v_mfma_f32_16x16x32_bf16 v[108:111], a[32:35], a[196:199], v[108:111]\n"
    "ds_read_b128 a[24:27], v244 offset:22528\n"
    "v_mfma_f32_16x16x32_bf16 v[108:111], a[36:39], a[200:203], v[108:111]\n"
    "buffer_load_dwordx4 v252, s[40:43], s48 offen offset:1024 lds\n"
    "v_mfma_f32_16x16x32_bf16 v[108:111], a[40:43], a[204:207], v[108:111]\n"
    "ds_read_b128 a[28:31], v244 offset:31744\n"
    "v_mfma_f32_16x16x32_bf16 v[108:111], a[44:47], a[208:211], v[108:111]\n"
    "s_waitcnt lgkmcnt(4)\n"
    "v_mfma_f32_16x16x32_bf16 v[76:79], a[0:3], a[180:183], v[76:79]\n"
    "ds_read_b128 a[32:35], v244 offset:5120\n"
    "v_mfma_f32_16x16x32_bf16 v[76:79], a[4:7], a[184:187], v[76:79]\n"
    "buffer_load_dwordx4 v252, s[40:43], s48 offen offset:2048 lds\n"
    "v_mfma_f32_16x16x32_bf16 v[76:79], a[8:11], a[188:191], v[76:79]\n"
    "ds_read_b128 a[36:39], v244 offset:14336\n"
    "v_mfma_f32_16x16x32_bf16 v[76:79], a[12:15], a[192:195], v[76:79]\n"
    "v_mfma_f32_16x16x32_bf16 v[112:115], a[0:3], a[196:199], v[112:115]\n"
    "ds_read_b128 a[40:43], v244 offset:23552\n"
    "v_mfma_f32_16x16x32_bf16 v[112:115], a[4:7], a[200:203], v[112:115]\n"
    "buffer_load_dwordx4 v252, s[40:43], s48 offen offset:3072 lds\n"
    "s_add_u32 s48, s48, 4096\n"
    "v_mfma_f32_16x16x32_bf16 v[112:115], a[8:11], a[204:207], v[112:115]\n"
    "ds_read_b128 a[44:47], v244 offset:32768\n"
    "v_mfma_f32_16x16x32_bf16 v[112:115], a[12:15], a[208:211], v[112:115]\n"
    "s_waitcnt lgkmcnt(4)\n"
    "v_mfma_f32_16x16x32_bf16 v[80:83], a[16:19], a[180:183], v[80:83]\n"
    "ds_read_b128 a[0:3], v244 offset:6144\n"
    "v_mfma_f32_16x16x32_bf16 v[80:83], a[20:23], a[184:187], v[80:83]\n"
    "s_add_u32 m0, s51, 8192\n"
    "s_nop 0\n"
    "buffer_load_dwordx4 v252, s[40:43], s48 offen offset:0 lds\n"
    "s_add_u32 s48, s48, 139264\n"
    "v_mfma_f32_16x16x32_bf16 v[80:83], a[24:27], a[188:191], v[80:83]\n"
    "ds_read_b128 a[4:7], v244 offset:15360\n"
    "v_mfma_f32_16x16x32_bf16 v[80:83], a[28:31], a[192:195], v[80:83]\n"
    "v_mfma_f32_16x16x32_bf16 v[116:119], a[16:19], a[196:199], v[116:119]\n"
    "ds_read_b128 a[8:11], v244 offset:24576\n"
    "v_mfma_f32_16x16x32_bf16 v[116:119], a[20:23], a[200:203], v[116:119]\n"
    "v_mfma_f32_16x16x32_bf16 v[116:119], a[24:27], a[204:207], v[116:119]\n"
    "ds_read_b128 a[12:15], v244 offset:33792\n"
    "v_mfma_f32_16x16x32_bf16 v[116:119], a[28:31], a[208:211], v[116:119]\n"
    "buffer_load_dwordx4 a[148:151], v252, s[44:47], s49 offen offset:0 sc1\n"
    "s_waitcnt lgkmcnt(4)\n"
    "v_mfma_f32_16x16x32_bf16 v[84:87], a[32:35], a[180:183], v[84:87]\n"
    "ds_read_b128 a[16:19], v244 offset:7168\n"
    "v_mfma_f32_16x16x32_bf16 v[84:87], a[36:39], a[184:187], v[84:87]\n"
    "v_mfma_f32_16x16x32_bf16 v[84:87], a[40:43], a[188:191], v[84:87]\n"
    "ds_read_b128 a[20:23], v244 offset:16384\n"
    "v_mfma_f32_16x16x32_bf16 v[84:87], a[44:47], a[192:195], v[84:87]\n"
    "buffer_load_dwordx4 a[152:155], v252, s[44:47], s49 offen offset:1024 sc1\n"
    "v_mfma_f32_16x16x32_bf16 v[120:123], a[32:35], a[196:199], v[120:123]\n"
    "ds_read_b128 a[24:27], v244 offset:25600\n"
    "v_mfma_f32_16x16x32_bf16 v[120:123], a[36:39], a[200:203], v[120:123]\n"
    "v_mfma_f32_16x16x32_bf16 v[120:123], a[40:43], a[204:207], v[120:123]\n"
    "ds_read_b128 a[28:31], v244 offset:34816\n"
    "v_mfma_f32_16x16x32_bf16 v[120:123], a[44:47], a[208:211], v[120:123]\n"
    "buffer_load_dwordx4 a[156:159], v252, s[44:47], s49 offen offset:2048 sc1\n"
    "s_waitcnt lgkmcnt(4)\n"
    "v_mfma_f32_16x16x32_bf16 v[88:91], a[0:3], a[180:183], v[88:91]\n"
    "ds_read_b128 a[32:35], v244 offset:8192\n"
    "v_mfma_f32_16x16x32_bf16 v[88:91], a[4:7], a[184:187], v[88:91]\n"
    "v_mfma_f32_16x16x32_bf16 v[88:91], a[8:11], a[188:191], v[88:91]\n"
    "ds_read_b128 a[36:39], v244 offset:17408\n"
    "v_mfma_f32_16x16x32_bf16 v[88:91], a[12:15], a[192:195], v[88:91]\n"
    "buffer_load_dwordx4 a[160:163], v252, s[44:47], s49 offen offset:3072 sc1\n"
    "s_add_u32 s49, s49, 4096\n"
    "v_mfma_f32_16x16x32_bf16 v[124:127], a[0:3], a[196:199], v[124:127]\n"
    "ds_read_b128 a[40:43], v244 offset:26624\n"
    "v_mfma_f32_16x16x32_bf16 v[124:127], a[4:7], a[200:203], v[124:127]\n"
    "v_mfma_f32_16x16x32_bf16 v[124:127], a[8:11], a[204:207], v[124:127]\n"
    "ds_read_b128 a[44:47], v244 offset:35840\n"
    "v_mfma_f32_16x16x32_bf16 v[124:127], a[12:15], a[208:211], v[124:127]\n"
    "buffer_load_dwordx4 a[164:167], v252, s[44:47], s50 offen offset:0 sc1\n"
    "s_waitcnt vmcnt(14) lgkmcnt(0)\n"
    "s_barrier\n"
    "v_add_u32 v244, 36864, v244\n"
    "s_cmp_eq_u32 s64, s78\n"
    "s_cbranch_scc1 L_bb1_%=\n"
    "L_bb1_ret_%=:\n"
    "v_mfma_f32_16x16x32_bf16 v[92:95], a[16:19], a[180:183], v[92:95]\n"
    "ds_read_b128 a[0:3], v244 offset:0\n"
    "v_mfma_f32_16x16x32_bf16 v[92:95], a[20:23], a[184:187], v[92:95]\n"
    "v_mfma_f32_16x16x32_bf16 v[92:95], a[24:27], a[188:191], v[92:95]\n"
    "ds_read_b128 a[4:7], v244 offset:9216\n"
    "v_mfma_f32_16x16x32_bf16 v[92:95], a[28:31], a[192:195], v[92:95]\n"
    "buffer_load_dwordx4 a[168:171], v252, s[44:47], s50 offen offset:1024 sc1\n"
    "v_mfma_f32_16x16x32_bf16 v[128:131], a[16:19], a[196:199], v[128:131]\n"
    "ds_read_b128 a[8:11], v244 offset:18432\n"
    "v_mfma_f32_16x16x32_bf16 v[128:131], a[20:23], a[200:203], v[128:131]\n"
    "v_mfma_f32_16x16x32_bf16 v[128:131], a[24:27], a[204:207], v[128:131]\n"
    "ds_read_b128 a[12:15], v244 offset:27648\n"
    "v_mfma_f32_16x16x32_bf16 v[128:131], a[28:31], a[208:211], v[128:131]\n"
    "buffer_load_dwordx4 a[172:175], v252, s[44:47], s50 offen offset:2048 sc1\n"
    "s_waitcnt lgkmcnt(4)\n"
    "v_mfma_f32_16x16x32_bf16 v[96:99], a[32:35], a[180:183], v[96:99]\n"
    "ds_read_b128 a[16:19], v244 offset:1024\n"
    "v_mfma_f32_16x16x32_bf16 v[96:99], a[36:39], a[184:187], v[96:99]\n"
    "v_mfma_f32_16x16x32_bf16 v[96:99], a[40:43], a[188:191], v[96:99]\n"
    "ds_read_b128 a[20:23], v244 offset:10240\n"
    "v_mfma_f32_16x16x32_bf16 v[96:99], a[44:47], a[192:195], v[96:99]\n"
    "buffer_load_dwordx4 a[176:179], v252, s[44:47], s50 offen offset:3072 sc1\n"
    "s_add_u32 s50, s50, 4096\n"
    "v_mfma_f32_16x16x32_bf16 v[132:135], a[32:35], a[196:199], v[132:135]\n"
    "ds_read_b128 a[24:27], v244 offset:19456\n"
    "v_mfma_f32_16x16x32_bf16 v[132:135], a[36:39], a[200:203], v[132:135]\n"
    "v_mfma_f32_16x16x32_bf16 v[132:135], a[40:43], a[204:207], v[132:135]\n"
    "ds_read_b128 a[28:31], v244 offset:28672\n"
    "v_mfma_f32_16x16x32_bf16 v[132:135], a[44:47], a[208:211], v[132:135]\n"
    "s_sub_u32 s64, s64, 1\n"
    "s_waitcnt lgkmcnt(4)\n"
    "v_mfma_f32_16x16x32_bf16 v[64:67], a[0:3], a[212:215], v[64:67]\n"
    "ds_read_b128 a[32:35], v244 offset:2048\n"
    "v_mfma_f32_16x16x32_bf16 v[64:67], a[4:7], a[216:219], v[64:67]\n"
    "s_add_u32 m0, s51, 36864\n"
    "s_nop 0\n"
    "buffer_load_dwordx4 v252, s[40:43], s48 offen offset:0 lds\n"
    "v_mfma_f32_16x16x32_bf16 v[64:67], a[8:11], a[220:223], v[64:67]\n"
    "ds_read_b128 a[36:39], v244 offset:11264\n"
    "v_mfma_f32_16x16x32_bf16 v[64:67], a[12:15], a[224:227], v[64:67]\n"
    "v_mfma_f32_16x16x32_bf16 v[100:103], a[0:3], a[228:231], v[100:103]\n"
    "ds_read_b128 a[40:43], v244 offset:20480\n"
    "v_mfma_f32_16x16x32_bf16 v[100:103], a[4:7], a[232:235], v[100:103]\n"
    "buffer_load_dwordx4 v252, s[40:43], s48 offen offset:1024 lds\n"
    "v_mfma_f32_16x16x32_bf16 v[100:103], a[8:11], a[236:239], v[100:103]\n"
    "ds_read_b128 a[44:47], v244 offset:29696\n"
    "v_mfma_f32_16x16x32_bf16 v[100:103], a[12:15], a[240:243], v[100:103]\n"
    "s_waitcnt lgkmcnt(4)\n"
    "v_mfma_f32_16x16x32_bf16 v[68:71], a[16:19], a[212:215], v[68:71]\n"
    "ds_read_b128 a[0:3], v244 offset:3072\n"
    "v_mfma_f32_16x16x32_bf16 v[68:71], a[20:23], a[216:219], v[68:71]\n"
    "buffer_load_dwordx4 v252, s[40:43], s48 offen offset:2048 lds\n"
    "v_mfma_f32_16x16x32_bf16 v[68:71], a[24:27], a[220:223], v[68:71]\n"
    "ds_read_b128 a[4:7], v244 offset:12288\n"
    "v_mfma_f32_16x16x32_bf16 v[68:71], a[28:31], a[224:227], v[68:71]\n"
    "v_mfma_f32_16x16x32_bf16 v[104:107], a[16:19], a[228:231], v[104:107]\n"
    "ds_read_b128 a[8:11], v244 offset:21504\n"
    "v_mfma_f32_16x16x32_bf16 v[104:107], a[20:23], a[232:235], v[104:107]\n"
    "buffer_load_dwordx4 v252, s[40:43], s48 offen offset:3072 lds\n"
    "s_add_u32 s48, s48, 4096\n"
    "v_mfma_f32_16x16x32_bf16 v[104:107], a[24:27], a[236:239], v[104:107]\n"
    "ds_read_b128 a[12:15], v244 offset:30720\n"
    "v_mfma_f32_16x16x32_bf16 v[104:107], a[28:31], a[240:243], v[104:107]\n"
    "s_waitcnt lgkmcnt(4)\n"
    "v_mfma_f32_16x16x32_bf16 v[72:75], a[32:35], a[212:215], v[72:75]\n"
    "ds_read_b128 a[16:19], v244 offset:4096\n"
    "v_mfma_f32_16x16x32_bf16 v[72:75], a[36:39], a[216:219], v[72:75]\n"
    "s_add_u32 m0, s51, 40960\n"
    "s_nop 0\n"
    "buffer_load_dwordx4 v252, s[40:43], s48 offen offset:0 lds\n"
    "v_mfma_f32_16x16x32_bf16 v[72:75], a[40:43], a[220:223], v[72:75]\n"
    "ds_read_b128 a[20:23], v244 offset:13312\n"
    "v_mfma_f32_16x16x32_bf16 v[72:75], a[44:47], a[224:227], v[72:75]\n"
    "v_mfma_f32_16x16x32_bf16 v[108:111], a[32:35], a[228:231], v[108:111]\n"
    "ds_read_b128 a[24:27], v244 offset:22528\n"
    "v_mfma_f32_16x16x32_bf16 v[108:111], a[36:39], a[232:235], v[108:111]\n"
    "buffer_load_dwordx4 v252, s[40:43], s48 offen offset:1024 lds\n"
    "v_mfma_f32_16x16x32_bf16 v[108:111], a[40:43], a[236:239], v[108:111]\n"
    "ds_read_b128 a[28:31], v244 offset:31744\n"
    "v_mfma_f32_16x16x32_bf16 v[108:111], a[44:47], a[240:243], v[108:111]\n"
    "s_waitcnt lgkmcnt(4)\n"
    "v_mfma_f32_16x16x32_bf16 v[76:79], a[0:3], a[212:215], v[76:79]\n"
    "ds_read_b128 a[32:35], v244 offset:5120\n"
    "v_mfma_f32_16x16x32_bf16 v[76:79], a[4:7], a[216:219], v[76:79]\n"
    "buffer_load_dwordx4 v252, s[40:43], s48 offen offset:2048 lds\n"
    "v_mfma_f32_16x16x32_bf16 v[76:79], a[8:11], a[220:223], v[76:79]\n"
    "ds_read_b128 a[36:39], v244 offset:14336\n"
    "v_mfma_f32_16x16x32_bf16 v[76:79], a[12:15], a[224:227], v[76:79]\n"
    "v_mfma_f32_16x16x32_bf16 v[112:115], a[0:3], a[228:231], v[112:115]\n"
    "ds_read_b128 a[40:43], v244 offset:23552\n"
    "v_mfma_f32_16x16x32_bf16 v[112:115], a[4:7], a[232:235], v[112:115]\n"
    "buffer_load_dwordx4 v252, s[40:43], s48 offen offset:3072 lds\n"
    "s_add_u32 s48, s48, 4096\n"
    "v_mfma_f32_16x16x32_bf16 v[112:115], a[8:11], a[236:239], v[112:115]\n"
    "ds_read_b128 a[44:47], v244 offset:32768\n"
    "v_mfma_f32_16x16x32_bf16 v[112:115], a[12:15], a[240:243], v[112:115]\n"
    "s_waitcnt lgkmcnt(4)\n"
    "v_mfma_f32_16x16x32_bf16 v[80:83], a[16:19], a[212:215], v[80:83]\n"
    "ds_read_b128 a[0:3], v244 offset:6144\n"
    "v_mfma_f32_16x16x32_bf16 v[80:83], a[20:23], a[216:219], v[80:83]\n"
    "s_add_u32 m0, s51, 45056\n"
    "s_nop 0\n"
    "buffer_load_dwordx4 v252, s[40:43], s48 offen offset:0 lds\n"
    "s_add_u32 s48, s48, 139264\n"
    "v_mfma_f32_16x16x32_bf16 v[80:83], a[24:27], a[220:223], v[80:83]\n"
    "ds_read_b128 a[4:7], v244 offset:15360\n"
    "v_mfma_f32_16x16x32_bf16 v[80:83], a[28:31], a[224:227], v[80:83]\n"
    "v_mfma_f32_16x16x32_bf16 v[116:119], a[16:19], a[228:231], v[116:119]\n"
    "ds_read_b128 a[8:11], v244 offset:24576\n"
    "v_mfma_f32_16x16x32_bf16 v[116:119], a[20:23], a[232:235], v[116:119]\n"
    "v_mfma_f32_16x16x32_bf16 v[116:119], a[24:27], a[236:239], v[116:119]\n"
    "ds_read_b128 a[12:15], v244 offset:33792\n"
    "v_mfma_f32_16x16x32_bf16 v[116:119], a[28:31], a[240:243], v[116:119]\n"
    "buffer_load_dwordx4 a[180:183], v252, s[44:47], s49 offen offset:0 sc1\n"
    "s_waitcnt lgkmcnt(4)\n"
    "v_mfma_f32_16x16x32_bf16 v[84:87], a[32:35], a[212:215], v[84:87]\n"
    "ds_read_b128 a[16:19], v244 offset:7168\n"
    "v_mfma_f32_16x16x32_bf16 v[84:87], a[36:39], a[216:219], v[84:87]\n"
    "v_mfma_f32_16x16x32_bf16 v[84:87], a[40:43], a[220:223], v[84:87]\n"
    "ds_read_b128 a[20:23], v244 offset:16384\n"
    "v_mfma_f32_16x16x32_bf16 v[84:87], a[44:47], a[224:227], v[84:87]\n"
    "buffer_load_dwordx4 a[184:187], v252, s[44:47], s49 offen offset:1024 sc1\n"
    "v_mfma_f32_16x16x32_bf16 v[120:123], a[32:35], a[228:231], v[120:123]\n"
    "ds_read_b128 a[24:27], v244 offset:25600\n"
    "v_mfma_f32_16x16x32_bf16 v[120:123], a[36:39], a[232:235], v[120:123]\n"
    "v_mfma_f32_16x16x32_bf16 v[120:123], a[40:43], a[236:239], v[120:123]\n"
    "ds_read_b128 a[28:31], v244 offset:34816\n"
    "v_mfma_f32_16x16x32_bf16 v[120:123], a[44:47], a[240:243], v[120:123]\n"
    "buffer_load_dwordx4 a[188:191], v252, s[44:47], s49 offen offset:2048 sc1\n"
    "s_waitcnt lgkmcnt(4)\n"
    "v_mfma_f32_16x16x32_bf16 v[88:91], a[0:3], a[212:215], v[88:91]\n"
    "ds_read_b128 a[32:35], v244 offset:8192\n"
    "v_mfma_f32_16x16x32_bf16 v[88:91], a[4:7], a[216:219], v[88:91]\n"
    "v_mfma_f32_16x16x32_bf16 v[88:91], a[8:11], a[220:223], v[88:91]\n"
    "ds_read_b128 a[36:39], v244 offset:17408\n"
    "v_mfma_f32_16x16x32_bf16 v[88:91], a[12:15], a[224:227], v[88:91]\n"
    "buffer_load_dwordx4 a[192:195], v252, s[44:47], s49 offen offset:3072 sc1\n"
    "s_add_u32 s49, s49, 4096\n"
    "v_mfma_f32_16x16x32_bf16 v[124:127], a[0:3], a[228:231], v[124:127]\n"
    "ds_read_b128 a[40:43], v244 offset:26624\n"
    "v_mfma_f32_16x16x32_bf16 v[124:127], a[4:7], a[232:235], v[124:127]\n"
    "v_mfma_f32_16x16x32_bf16 v[124:127], a[8:11], a[236:239], v[124:127]\n"
    "ds_read_b128 a[44:47], v244 offset:35840\n"
    "v_mfma_f32_16x16x32_bf16 v[124:127], a[12:15], a[240:243], v[124:127]\n"
    "buffer_load_dwordx4 a[196:199], v252, s[44:47], s50 offen offset:0 sc1\n"
    "s_waitcnt vmcnt(14) lgkmcnt(0)\n"
    "s_barrier\n"
    "v_add_u32 v244, 4294893568, v244\n"
    "v_mfma_f32_16x16x32_bf16 v[92:95], a[16:19], a[212:215], v[92:95]\n"
    "ds_read_b128 a[0:3], v244 offset:0\n"
    "v_mfma_f32_16x16x32_bf16 v[92:95], a[20:23], a[216:219], v[92:95]\n"
    "v_mfma_f32_16x16x32_bf16 v[92:95], a[24:27], a[220:223], v[92:95]\n"
    "ds_read_b128 a[4:7], v244 offset:9216\n"
    "v_mfma_f32_16x16x32_bf16 v[92:95], a[28:31], a[224:227], v[92:95]\n"
    "buffer_load_dwordx4 a[200:203], v252, s[44:47], s50 offen offset:1024 sc1\n"
    "v_mfma_f32_16x16x32_bf16 v[128:131], a[16:19], a[228:231], v[128:131]\n"
    "ds_read_b128 a[8:11], v244 offset:18432\n"
    "v_mfma_f32_16x16x32_bf16 v[128:131], a[20:23], a[232:235], v[128:131]\n"
    "v_mfma_f32_16x16x32_bf16 v[128:131], a[24:27], a[236:239], v[128:131]\n"
    "ds_read_b128 a[12:15], v244 offset:27648\n"
    "v_mfma_f32_16x16x32_bf16 v[128:131], a[28:31], a[240:243], v[128:131]\n"
    "buffer_load_dwordx4 a[204:207], v252, s[44:47], s50 offen offset:2048 sc1\n"
    "s_waitcnt lgkmcnt(4)\n"
    "v_mfma_f32_16x16x32_bf16 v[96:99], a[32:35], a[212:215], v[96:99]\n"
    "ds_read_b128 a[16:19], v244 offset:1024\n"
    "v_mfma_f32_16x16x32_bf16 v[96:99], a[36:39], a[216:219], v[96:99]\n"
    "v_mfma_f32_16x16x32_bf16 v[96:99], a[40:43], a[220:223], v[96:99]\n"
    "ds_read_b128 a[20:23], v244 offset:10240\n"
    "v_mfma_f32_16x16x32_bf16 v[96:99], a[44:47], a[224:227], v[96:99]\n"
    "buffer_load_dwordx4 a[208:211], v252, s[44:47], s50 offen offset:3072 sc1\n"
    "s_add_u32 s50, s50, 4096\n"
    "v_mfma_f32_16x16x32_bf16 v[132:135], a[32:35], a[228:231], v[132:135]\n"
    "ds_read_b128 a[24:27], v244 offset:19456\n"
    "v_mfma_f32_16x16x32_bf16 v[132:135], a[36:39], a[232:235], v[132:135]\n"
    "v_mfma_f32_16x16x32_bf16 v[132:135], a[40:43], a[236:239], v[132:135]\n"
    "ds_read_b128 a[28:31], v244 offset:28672\n"
    "v_mfma_f32_16x16x32_bf16 v[132:135], a[44:47], a[240:243], v[132:135]\n"
    "s_sub_u32 s64, s64, 1\n"
    "s_branch L_btop_%=\n"
    "L_bF_%=:\n"
    "s_waitcnt lgkmcnt(4)\n"
    "v_mfma_f32_16x16x32_bf16 v[64:67], a[0:3], a[148:151], v[64:67]\n"
    "ds_read_b128 a[32:35], v244 offset:2048\n"
    "v_mfma_f32_16x16x32_bf16 v[64:67], a[4:7], a[152:155], v[64:67]\n"
    "v_mfma_f32_16x16x32_bf16 v[64:67], a[8:11], a[156:159], v[64:67]\n"
    "ds_read_b128 a[36:39], v244 offset:11264\n"
    "v_mfma_f32_16x16x32_bf16 v[64:67], a[12:15], a[160:163], v[64:67]\n"
    "v_mfma_f32_16x16x32_bf16 v[100:103], a[0:3], a[164:167], v[100:103]\n"
    "ds_read_b128 a[40:43], v244 offset:20480\n"
    "v_mfma_f32_16x16x32_bf16 v[100:103], a[4:7], a[168:171], v[100:103]\n"
    "v_mfma_f32_16x16x32_bf16 v[100:103], a[8:11], a[172:175], v[100:103]\n"
    "ds_read_b128 a[44:47], v244 offset:29696\n"
    "v_mfma_f32_16x16x32_bf16 v[100:103], a[12:15], a[176:179], v[100:103]\n"
    "s_waitcnt lgkmcnt(4)\n"
    "v_mfma_f32_16x16x32_bf16 v[68:71], a[16:19], a[148:151], v[68:71]\n"
    "ds_read_b128 a[0:3], v244 offset:3072\n"
    "v_mfma_f32_16x16x32_bf16 v[68:71], a[20:23], a[152:155], v[68:71]\n"
    "v_mfma_f32_16x16x32_bf16 v[68:71], a[24:27], a[156:159], v[68:71]\n"
    "ds_read_b128 a[4:7], v244 offset:12288\n"
    "v_mfma_f32_16x16x32_bf16 v[68:71], a[28:31], a[160:163], v[68:71]\n"
    "v_mfma_f32_16x16x32_bf16 v[104:107], a[16:19], a[164:167], v[104:107]\n"
    "ds_read_b128 a[8:11], v244 offset:21504\n"
    "v_mfma_f32_16x16x32_bf16 v[104:107], a[20:23], a[168:171], v[104:107]\n"
    "v_mfma_f32_16x16x32_bf16 v[104:107], a[24:27], a[172:175], v[104:107]\n"
    "ds_read_b128 a[12:15], v244 offset:30720\n"
    "v_mfma_f32_16x16x32_bf16 v[104:107], a[28:31], a[176:179], v[104:107]\n"
    "s_waitcnt lgkmcnt(4)\n"
    "v_mfma_f32_16x16x32_bf16 v[72:75], a[32:35], a[148:151], v[72:75]\n"
    "ds_read_b128 a[16:19], v244 offset:4096\n"
    "v_mfma_f32_16x16x32_bf16 v[72:75], a[36:39], a[152:155], v[72:75]\n"
    "v_mfma_f32_16x16x32_bf16 v[72:75], a[40:43], a[156:159], v[72:75]\n"
    "ds_read_b128 a[20:23], v244 offset:13312\n"
    "v_mfma_f32_16x16x32_bf16 v[72:75], a[44:47], a[160:163], v[72:75]\n"
    "v_mfma_f32_16x16x32_bf16 v[108:111], a[32:35], a[164:167], v[108:111]\n"
    "ds_read_b128 a[24:27], v244 offset:22528\n"
    "v_mfma_f32_16x16x32_bf16 v[108:111], a[36:39], a[168:171], v[108:111]\n"
    "v_mfma_f32_16x16x32_bf16 v[108:111], a[40:43], a[172:175], v[108:111]\n"
    "ds_read_b128 a[28:31], v244 offset:31744\n"
    "v_mfma_f32_16x16x32_bf16 v[108:111], a[44:47], a[176:179], v[108:111]\n"
    "s_waitcnt lgkmcnt(4)\n"
    "v_mfma_f32_16x16x32_bf16 v[76:79], a[0:3], a[148:151], v[76:79]\n"
    "ds_read_b128 a[32:35], v244 offset:5120\n"
    "v_mfma_f32_16x16x32_bf16 v[76:79], a[4:7], a[152:155], v[76:79]\n"
    "v_mfma_f32_16x16x32_bf16 v[76:79], a[8:11], a[156:159], v[76:79]\n"
    "ds_read_b128 a[36:39], v244 offset:14336\n"
    "v_mfma_f32_16x16x32_bf16 v[76:79], a[12:15], a[160:163], v[76:79]\n"
    "v_mfma_f32_16x16x32_bf16 v[112:115], a[0:3], a[164:167], v[112:115]\n"
    "ds_read_b128 a[40:43], v244 offset:23552\n"
    "v_mfma_f32_16x16x32_bf16 v[112:115], a[4:7], a[168:171], v[112:115]\n"
    "v_mfma_f32_16x16x32_bf16 v[112:115], a[8:11], a[172:175], v[112:115]\n"
    "ds_read_b128 a[44:47], v244 offset:32768\n"
    "v_mfma_f32_16x16x32_bf16 v[112:115], a[12:15], a[176:179], v[112:115]\n"
    "s_waitcnt lgkmcnt(4)\n"
    "v_mfma_f32_16x16x32_bf16 v[80:83], a[16:19], a[148:151], v[80:83]\n"
    "ds_read_b128 a[0:3], v244 offset:6144\n"
    "v_mfma_f32_16x16x32_bf16 v[80:83], a[20:23], a[152:155], v[80:83]\n"
    "v_mfma_f32_16x16x32_bf16 v[80:83], a[24:27], a[156:159], v[80:83]\n"
    "ds_read_b128 a[4:7], v244 offset:15360\n"
    "v_mfma_f32_16x16x32_bf16 v[80:83], a[28:31], a[160:163], v[80:83]\n"
    "v_mfma_f32_16x16x32_bf16 v[116:119], a[16:19], a[164:167], v[116:119]\n"
    "ds_read_b128 a[8:11], v244 offset:24576\n"
    "v_mfma_f32_16x16x32_bf16 v[116:119], a[20:23], a[168:171], v[116:119]\n"
    "v_mfma_f32_16x16x32_bf16 v[116:119], a[24:27], a[172:175], v[116:119]\n"
    "ds_read_b128 a[12:15], v244 offset:33792\n"
    "v_mfma_f32_16x16x32_bf16 v[116:119], a[28:31], a[176:179], v[116:119]\n"
    "s_waitcnt lgkmcnt(4)\n"
    "v_mfma_f32_16x16x32_bf16 v[84:87], a[32:35], a[148:151], v[84:87]\n"
    "ds_read_b128 a[16:19], v244 offset:7168\n"
    "v_mfma_f32_16x16x32_bf16 v[84:87], a[36:39], a[152:155], v[84:87]\n"
    "v_mfma_f32_16x16x32_bf16 v[84:87], a[40:43], a[156:159], v[84:87]\n"
    "ds_read_b128 a[20:23], v244 offset:16384\n"
    "v_mfma_f32_16x16x32_bf16 v[84:87], a[44:47], a[160:163], v[84:87]\n"
    "v_mfma_f32_16x16x32_bf16 v[120:123], a[32:35], a[164:167], v[120:123]\n"
    "ds_read_b128 a[24:27], v244 offset:25600\n"
    "v_mfma_f32_16x16x32_bf16 v[120:123], a[36:39], a[168:171], v[120:123]\n"
    "v_mfma_f32_16x16x32_bf16 v[120:123], a[40:43], a[172:175], v[120:123]\n"
    "ds_read_b128 a[28:31], v244 offset:34816\n"
    "v_mfma_f32_16x16x32_bf16 v[120:123], a[44:47], a[176:179], v[120:123]\n"
    "s_waitcnt lgkmcnt(4)\n"
    "v_mfma_f32_16x16x32_bf16 v[88:91], a[0:3], a[148:151], v[88:91]\n"
    "ds_read_b128 a[32:35], v244 offset:8192\n"
    "v_mfma_f32_16x16x32_bf16 v[88:91], a[4:7], a[152:155], v[88:91]\n"
    "v_mfma_f32_16x16x32_bf16 v[88:91], a[8:11], a[156:159], v[88:91]\n"
    "ds_read_b128 a[36:39], v244 offset:17408\n"
    "v_mfma_f32_16x16x32_bf16 v[88:91], a[12:15], a[160:163], v[88:91]\n"
    "v_mfma_f32_16x16x32_bf16 v[124:127], a[0:3], a[164:167], v[124:127]\n"
    "ds_read_b128 a[40:43], v244 offset:26624\n"
    "v_mfma_f32_16x16x32_bf16 v[124:127], a[4:7], a[168:171], v[124:127]\n"
    "v_mfma_f32_16x16x32_bf16 v[124:127], a[8:11], a[172:175], v[124:127]\n"
    "ds_read_b128 a[44:47], v244 offset:35840\n"
    "v_mfma_f32_16x16x32_bf16 v[124:127], a[12:15], a[176:179], v[124:127]\n"
    "s_waitcnt vmcnt(0) lgkmcnt(0)\n"
    "s_barrier\n"
    "v_add_u32 v244, 36864, v244\n"
    "v_mfma_f32_16x16x32_bf16 v[92:95], a[16:19], a[148:151], v[92:95]\n"
    "ds_read_b128 a[0:3], v244 offset:0\n"
    "v_mfma_f32_16x16x32_bf16 v[92:95], a[20:23], a[152:155], v[92:95]\n"
    "s_add_u32 s96, s52, 1\n"
    "s_and_b32 s96, s96, 3\n"
    "s_lshl_b32 s97, s70, 2\n"
    "s_add_u32 s96, s96, s97\n"
    "s_lshl_b32 s96, s96, 3\n"
    "s_add_u32 s96, s96, s52\n"
    "s_add_u32 s96, s96, 0\n"
    "s_mul_i32 s96, s96, 9216\n"
    "s_lshl_b32 s97, s39, 10\n"
    "s_add_u32 s65, s96, s97\n"
    "s_add_u32 s96, s52, 2\n"
    "s_and_b32 s96, s96, 3\n"
    "s_lshl_b32 s97, s70, 2\n"
    "s_add_u32 s96, s96, s97\n"
    "s_lshl_b32 s96, s96, 3\n"
    "v_mfma_f32_16x16x32_bf16 v[92:95], a[24:27], a[156:159], v[92:95]\n"
    "ds_read_b128 a[4:7], v244 offset:9216\n"
    "v_mfma_f32_16x16x32_bf16 v[92:95], a[28:31], a[160:163], v[92:95]\n"
    "s_add_u32 s96, s96, s52\n"
    "s_add_u32 s96, s96, 0\n"
    "s_mul_i32 s96, s96, 9216\n"
    "s_lshl_b32 s97, s39, 10\n"
    "s_add_u32 s66, s96, s97\n"
    "s_add_u32 s96, s52, 3\n"
    "s_and_b32 s96, s96, 3\n"
    "s_lshl_b32 s97, s70, 2\n"
    "s_add_u32 s96, s96, s97\n"
    "s_lshl_b32 s96, s96, 3\n"
    "s_add_u32 s96, s96, s52\n"
    "s_add_u32 s96, s96, 0\n"
    "s_mul_i32 s96, s96, 9216\n"
    "s_lshl_b32 s97, s39, 10\n"
    "s_add_u32 s67, s96, s97\n"
    "v_mfma_f32_16x16x32_bf16 v[128:131], a[16:19], a[164:167], v[128:131]\n"
    "ds_read_b128 a[8:11], v244 offset:18432\n"
    "v_mfma_f32_16x16x32_bf16 v[128:131], a[20:23], a[168:171], v[128:131]\n"
    "s_add_u32 s96, s65, 0\n"
    "buffer_load_dwordx4 v[12:15], v252, s[8:11], s96 offen sc1\n"
    "v_mfma_f32_16x16x32_bf16 v[128:131], a[24:27], a[172:175], v[128:131]\n"
    "ds_read_b128 a[12:15], v244 offset:27648\n"
    "v_mfma_f32_16x16x32_bf16 v[128:131], a[28:31], a[176:179], v[128:131]\n"
    "s_add_u32 s96, s66, 0\n"
    "buffer_load_dwordx4 v[16:19], v252, s[8:11], s96 offen sc1\n"
    "s_waitcnt lgkmcnt(4)\n"
    "v_mfma_f32_16x16x32_bf16 v[96:99], a[32:35], a[148:151], v[96:99]\n"
    "ds_read_b128 a[16:19], v244 offset:1024\n"
    "v_mfma_f32_16x16x32_bf16 v[96:99], a[36:39], a[152:155], v[96:99]\n"
    "s_add_u32 s96, s67, 0\n"
    "buffer_load_dwordx4 v[20:23], v252, s[8:11], s96 offen sc1\n"
    "v_mfma_f32_16x16x32_bf16 v[96:99], a[40:43], a[156:159], v[96:99]\n"
    "ds_read_b128 a[20:23], v244 offset:10240\n"
    "v_mfma_f32_16x16x32_bf16 v[96:99], a[44:47], a[160:163], v[96:99]\n"
    "s_add_u32 s96, s65, 4096\n"
    "buffer_load_dwordx4 v[28:31], v252, s[8:11], s96 offen sc1\n"
    "v_mfma_f32_16x16x32_bf16 v[132:135], a[32:35], a[164:167], v[132:135]\n"
    "ds_read_b128 a[24:27], v244 offset:19456\n"
    "v_mfma_f32_16x16x32_bf16 v[132:135], a[36:39], a[168:171], v[132:135]\n"
    "s_add_u32 s96, s66, 4096\n"
    "buffer_load_dwordx4 v[32:35], v252, s[8:11], s96 offen sc1\n"
    "v_mfma_f32_16x16x32_bf16 v[132:135], a[40:43], a[172:175], v[132:135]\n"
    "ds_read_b128 a[28:31], v244 offset:28672\n"
    "v_mfma_f32_16x16x32_bf16 v[132:135], a[44:47], a[176:179], v[132:135]\n"
    "s_add_u32 s96, s67, 4096\n"
    "buffer_load_dwordx4 v[36:39], v252, s[8:11], s96 offen sc1\n"
    "s_sub_u32 s64, s64, 1\n"
    "s_waitcnt lgkmcnt(4)\n"
    "v_mfma_f32_16x16x32_bf16 v[64:67], a[0:3], a[180:183], v[64:67]\n"
    "ds_read_b128 a[32:35], v244 offset:2048\n"
    "v_mfma_f32_16x16x32_bf16 v[64:67], a[4:7], a[184:187], v[64:67]\n"
    "s_cmp_eq_u32 s39, 0\n"
    "s_cbranch_scc0 L_bg2_%=\n"
    "s_add_u32 s96, s65, 8192\n"
    "buffer_load_dwordx4 v[44:47], v252, s[8:11], s96 offen sc1\n"
    "s_add_u32 s96, s66, 8192\n"
    "buffer_load_dwordx4 v[48:51], v252, s[8:11], s96 offen sc1\n"
    "s_add_u32 s96, s67, 8192\n"
    "buffer_load_dwordx4 v[52:55], v252, s[8:11], s96 offen sc1\n"
    "L_bg2_%=:\n"
    "v_mfma_f32_16x16x32_bf16 v[64:67], a[8:11], a[188:191], v[64:67]\n"
    "ds_read_b128 a[36:39], v244 offset:11264\n"
    "v_mfma_f32_16x16x32_bf16 v[64:67], a[12:15], a[192:195], v[64:67]\n"
    "s_lshl_b32 s96, s39, 10\n"
    "s_add_u32 s96, s96, s85\n"
    "v_add_u32 v6, s96, v252\n"
    "v_add_u32 v6, 110592, v6\n"
    "ds_read_b128 v[8:11], v6 offset:0\n"
    "ds_read_b128 v[24:27], v6 offset:4096\n"
    "ds_read_b128 v[40:43], v6 offset:8192\n"
    "v_mfma_f32_16x16x32_bf16 v[100:103], a[0:3], a[196:199], v[100:103]\n"
    "ds_read_b128 a[40:43], v244 offset:20480\n"
    "v_mfma_f32_16x16x32_bf16 v[100:103], a[4:7], a[200:203], v[100:103]\n"
    "s_lshl_b32 s96, s53, 7\n"
    "s_lshl_b32 s97, s52, 5\n"
    "s_add_u32 s96, s96, s97\n"
    "s_mul_i32 s96, s96, 1152\n"
    "s_mul_i32 s97, s54, 288\n"
    "s_add_u32 s96, s96, s97\n"
    "v_and_b32 v2, 63, v1\n"
    "v_and_b32 v5, 15, v2\n"
    "v_mul_u32_u24 v5, 1152, v5\n"
    "v_lshrrev_b32 v6, 4, v2\n"
    "v_lshlrev_b32 v6, 3, v6\n"
    "v_add_u32 v5, v5, v6\n"
    "v_add_u32 v5, s96, v5\n"
    "s_lshl_b32 s96, s39, 1\n"
    "s_add_u32 s96, s96, 0\n"
    "s_cmp_ge_u32 s96, 18\n"
    "s_cselect_b32 s96, 0, s96\n"
    "s_cmp_ge_u32 s96, 9\n"
    "s_cselect_b32 s97, 18144, 0\n"
    "s_lshl_b32 s96, s96, 5\n"
    "s_add_u32 s96, s96, s97\n"
    "v_add_u32 v246, s96, v5\n"
    "global_load_dwordx2 v[136:137], v246, s[20:21]\n"
    "v_mfma_f32_16x16x32_bf16 v[100:103], a[8:11], a[204:207], v[100:103]\n"
    "ds_read_b128 a[44:47], v244 offset:29696\n"
    "v_mfma_f32_16x16x32_bf16 v[100:103], a[12:15], a[208:211], v[100:103]\n"
    "s_lshl_b32 s96, s39, 1\n"
    "s_add_u32 s96, s96, 1\n"
    "s_cmp_ge_u32 s96, 18\n"
    "s_cselect_b32 s96, 0, s96\n"
    "s_cmp_ge_u32 s96, 9\n"
    "s_cselect_b32 s97, 18144, 0\n"
    "s_lshl_b32 s96, s96, 5\n"
    "s_add_u32 s96, s96, s97\n"
    "v_add_u32 v247, s96, v5\n"
    "global_load_dwordx2 v[138:139], v247, s[20:21]\n"
    "s_waitcnt lgkmcnt(4)\n"
    "v_mfma_f32_16x16x32_bf16 v[68:71], a[16:19], a[180:183], v[68:71]\n"
    "ds_read_b128 a[0:3], v244 offset:3072\n"
    "v_mfma_f32_16x16x32_bf16 v[68:71], a[20:23], a[184:187], v[68:71]\n"
    "s_lshl_b32 s96, s39, 1\n"
    "s_add_u32 s96, s96, 8\n"
    "s_cmp_ge_u32 s96, 18\n"
    "s_cselect_b32 s96, 0, s96\n"
    "s_cmp_ge_u32 s96, 9\n"
    "s_cselect_b32 s97, 18144, 0\n"
    "s_lshl_b32 s96, s96, 5\n"
    "s_add_u32 s96, s96, s97\n"
    "v_add_u32 v248, s96, v5\n"
    "global_load_dwordx2 v[140:141], v248, s[20:21]\n"
    "v_mfma_f32_16x16x32_bf16 v[68:71], a[24:27], a[188:191], v[68:71]\n"
    "ds_read_b128 a[4:7], v244 offset:12288\n"
    "v_mfma_f32_16x16x32_bf16 v[68:71], a[28:31], a[192:195], v[68:71]\n"
    "s_lshl_b32 s96, s39, 1\n"
    "s_add_u32 s96, s96, 9\n"
    "s_cmp_ge_u32 s96, 18\n"
    "s_cselect_b32 s96, 0, s96\n"
    "s_cmp_ge_u32 s96, 9\n"
    "s_cselect_b32 s97, 18144, 0\n"
    "s_lshl_b32 s96, s96, 5\n"
    "s_add_u32 s96, s96, s97\n"
    "v_add_u32 v249, s96, v5\n"
    "global_load_dwordx2 v[142:143], v249, s[20:21]\n"
    "v_mfma_f32_16x16x32_bf16 v[104:107], a[16:19], a[196:199], v[104:107]\n"
    "ds_read_b128 a[8:11], v244 offset:21504\n"
    "v_mfma_f32_16x16x32_bf16 v[104:107], a[20:23], a[200:203], v[104:107]\n"
    "s_lshl_b32 s96, s39, 1\n"
    "s_add_u32 s96, s96, 16\n"
    "s_cmp_ge_u32 s96, 18\n"
    "s_cselect_b32 s96, 0, s96\n"
    "s_cmp_ge_u32 s96, 9\n"
    "s_cselect_b32 s97, 18144, 0\n"
    "s_lshl_b32 s96, s96, 5\n"
    "s_add_u32 s96, s96, s97\n"
    "v_add_u32 v250, s96, v5\n"
    "global_load_dwordx2 v[144:145], v250, s[20:21]\n"
    "v_mfma_f32_16x16x32_bf16 v[104:107], a[24:27], a[204:207], v[104:107]\n"
    "ds_read_b128 a[12:15], v244 offset:30720\n"
    "v_mfma_f32_16x16x32_bf16 v[104:107], a[28:31], a[208:211], v[104:107]\n"
    "s_lshl_b32 s96, s39, 1\n"
    "s_add_u32 s96, s96, 17\n"
    "s_cmp_ge_u32 s96, 18\n"
    "s_cselect_b32 s96, 0, s96\n"
    "s_cmp_ge_u32 s96, 9\n"
    "s_cselect_b32 s97, 18144, 0\n"
    "s_lshl_b32 s96, s96, 5\n"
    "s_add_u32 s96, s96, s97\n"
    "v_add_u32 v251, s96, v5\n"
    "global_load_dwordx2 v[146:147], v251, s[20:21]\n"
    "s_waitcnt lgkmcnt(4)\n"
    "v_mfma_f32_16x16x32_bf16 v[72:75], a[32:35], a[180:183], v[72:75]\n"
    "ds_read_b128 a[16:19], v244 offset:4096\n"
    "v_mfma_f32_16x16x32_bf16 v[72:75], a[36:39], a[184:187], v[72:75]\n"
    "v_mfma_f32_16x16x32_bf16 v[72:75], a[40:43], a[188:191], v[72:75]\n"
    "ds_read_b128 a[20:23], v244 offset:13312\n"
    "v_mfma_f32_16x16x32_bf16 v[72:75], a[44:47], a[192:195], v[72:75]\n"
    "v_mfma_f32_16x16x32_bf16 v[108:111], a[32:35], a[196:199], v[108:111]\n"
    "ds_read_b128 a[24:27], v244 offset:22528\n"
    "v_mfma_f32_16x16x32_bf16 v[108:111], a[36:39], a[200:203], v[108:111]\n"
    "v_mfma_f32_16x16x32_bf16 v[108:111], a[40:43], a[204:207], v[108:111]\n"
    "ds_read_b128 a[28:31], v244 offset:31744\n"
    "v_mfma_f32_16x16x32_bf16 v[108:111], a[44:47], a[208:211], v[108:111]\n"
    "s_waitcnt lgkmcnt(4)\n"
    "v_mfma_f32_16x16x32_bf16 v[76:79], a[0:3], a[180:183], v[76:79]\n"
    "ds_read_b128 a[32:35], v244 offset:5120\n"
    "v_mfma_f32_16x16x32_bf16 v[76:79], a[4:7], a[184:187], v[76:79]\n"
    "v_mfma_f32_16x16x32_bf16 v[76:79], a[8:11], a[188:191], v[76:79]\n"
    "ds_read_b128 a[36:39], v244 offset:14336\n"
    "v_mfma_f32_16x16x32_bf16 v[76:79], a[12:15], a[192:195], v[76:79]\n"
    "v_mfma_f32_16x16x32_bf16 v[112:115], a[0:3], a[196:199], v[112:115]\n"
    "ds_read_b128 a[40:43], v244 offset:23552\n"
    "v_mfma_f32_16x16x32_bf16 v[112:115], a[4:7], a[200:203], v[112:115]\n"
    "v_mfma_f32_16x16x32_bf16 v[112:115], a[8:11], a[204:207], v[112:115]\n"
    "ds_read_b128 a[44:47], v244 offset:32768\n"
    "v_mfma_f32_16x16x32_bf16 v[112:115], a[12:15], a[208:211], v[112:115]\n"
    "s_waitcnt lgkmcnt(4)\n"
    "v_mfma_f32_16x16x32_bf16 v[80:83], a[16:19], a[180:183], v[80:83]\n"
    "ds_read_b128 a[0:3], v244 offset:6144\n"
    "v_mfma_f32_16x16x32_bf16 v[80:83], a[20:23], a[184:187], v[80:83]\n"
    "v_mfma_f32_16x16x32_bf16 v[80:83], a[24:27], a[188:191], v[80:83]\n"
    "ds_read_b128 a[4:7], v244 offset:15360\n"
    "v_mfma_f32_16x16x32_bf16 v[80:83], a[28:31], a[192:195], v[80:83]\n"
    "v_mfma_f32_16x16x32_bf16 v[116:119], a[16:19], a[196:199], v[116:119]\n"
    "ds_read_b128 a[8:11], v244 offset:24576\n"
    "v_mfma_f32_16x16x32_bf16 v[116:119], a[20:23], a[200:203], v[116:119]\n"
    "v_mfma_f32_16x16x32_bf16 v[116:119], a[24:27], a[204:207], v[116:119]\n"
    "ds_read_b128 a[12:15], v244 offset:33792\n"
    "v_mfma_f32_16x16x32_bf16 v[116:119], a[28:31], a[208:211], v[116:119]\n"
    "s_waitcnt lgkmcnt(4)\n"
    "v_mfma_f32_16x16x32_bf16 v[84:87], a[32:35], a[180:183], v[84:87]\n"
    "ds_read_b128 a[16:19], v244 offset:7168\n"
    "v_mfma_f32_16x16x32_bf16 v[84:87], a[36:39], a[184:187], v[84:87]\n"
    "v_mfma_f32_16x16x32_bf16 v[84:87], a[40:43], a[188:191], v[84:87]\n"
    "ds_read_b128 a[20:23], v244 offset:16384\n"
    "v_mfma_f32_16x16x32_bf16 v[84:87], a[44:47], a[192:195], v[84:87]\n"
    "v_mfma_f32_16x16x32_bf16 v[120:123], a[32:35], a[196:199], v[120:123]\n"
    "ds_read_b128 a[24:27], v244 offset:25600\n"
    "v_mfma_f32_16x16x32_bf16 v[120:123], a[36:39], a[200:203], v[120:123]\n"
    "v_mfma_f32_16x16x32_bf16 v[120:123], a[40:43], a[204:207], v[120:123]\n"
    "ds_read_b128 a[28:31], v244 offset:34816\n"
    "v_mfma_f32_16x16x32_bf16 v[120:123], a[44:47], a[208:211], v[120:123]\n"
    "s_waitcnt lgkmcnt(4)\n"
    "v_mfma_f32_16x16x32_bf16 v[88:91], a[0:3], a[180:183], v[88:91]\n"
    "ds_read_b128 a[32:35], v244 offset:8192\n"
    "v_mfma_f32_16x16x32_bf16 v[88:91], a[4:7], a[184:187], v[88:91]\n"
    "v_mfma_f32_16x16x32_bf16 v[88:91], a[8:11], a[188:191], v[88:91]\n"
    "ds_read_b128 a[36:39], v244 offset:17408\n"
    "v_mfma_f32_16x16x32_bf16 v[88:91], a[12:15], a[192:195], v[88:91]\n"
    "v_mfma_f32_16x16x32_bf16 v[124:127], a[0:3], a[196:199], v[124:127]\n"
    "ds_read_b128 a[40:43], v244 offset:26624\n"
    "v_mfma_f32_16x16x32_bf16 v[124:127], a[4:7], a[200:203], v[124:127]\n"
    "v_mfma_f32_16x16x32_bf16 v[124:127], a[8:11], a[204:207], v[124:127]\n"
    "ds_read_b128 a[44:47], v244 offset:35840\n"
    "v_mfma_f32_16x16x32_bf16 v[124:127], a[12:15], a[208:211], v[124:127]\n"
    "s_waitcnt lgkmcnt(4)\n"
    "v_mfma_f32_16x16x32_bf16 v[92:95], a[16:19], a[180:183], v[92:95]\n"
    "v_mfma_f32_16x16x32_bf16 v[92:95], a[20:23], a[184:187], v[92:95]\n"
    "v_mfma_f32_16x16x32_bf16 v[92:95], a[24:27], a[188:191], v[92:95]\n"
    "v_mfma_f32_16x16x32_bf16 v[92:95], a[28:31], a[192:195], v[92:95]\n"
    "v_mfma_f32_16x16x32_bf16 v[128:131], a[16:19], a[196:199], v[128:131]\n"
    "v_mfma_f32_16x16x32_bf16 v[128:131], a[20:23], a[200:203], v[128:131]\n"
    "v_mfma_f32_16x16x32_bf16 v[128:131], a[24:27], a[204:207], v[128:131]\n"
    "v_mfma_f32_16x16x32_bf16 v[128:131], a[28:31], a[208:211], v[128:131]\n"
    "s_waitcnt lgkmcnt(0)\n"
    "v_mfma_f32_16x16x32_bf16 v[96:99], a[32:35], a[180:183], v[96:99]\n"
    "v_mfma_f32_16x16x32_bf16 v[96:99], a[36:39], a[184:187], v[96:99]\n"
    "v_mfma_f32_16x16x32_bf16 v[96:99], a[40:43], a[188:191], v[96:99]\n"
    "v_mfma_f32_16x16x32_bf16 v[96:99], a[44:47], a[192:195], v[96:99]\n"
    "v_mfma_f32_16x16x32_bf16 v[132:135], a[32:35], a[196:199], v[132:135]\n"
    "v_mfma_f32_16x16x32_bf16 v[132:135], a[36:39], a[200:203], v[132:135]\n"
    "v_mfma_f32_16x16x32_bf16 v[132:135], a[40:43], a[204:207], v[132:135]\n"
    "v_mfma_f32_16x16x32_bf16 v[132:135], a[44:47], a[208:211], v[132:135]\n"
    "s_waitcnt vmcnt(0) lgkmcnt(0)\n"
    "s_nop 7\n"
    "s_nop 7\n"
    "v_cvt_pk_bf16_f32 v200, v64, v65\n"
    "v_cvt_pk_bf16_f32 v201, v66, v67\n"
    "v_cvt_pk_bf16_f32 v202, v68, v69\n"
    "v_cvt_pk_bf16_f32 v203, v70, v71\n"
    "v_cvt_pk_bf16_f32 v204, v72, v73\n"
    "v_cvt_pk_bf16_f32 v205, v74, v75\n"
    "v_cvt_pk_bf16_f32 v206, v76, v77\n"
    "v_cvt_pk_bf16_f32 v207, v78, v79\n"
    "v_cvt_pk_bf16_f32 v208, v80, v81\n"
    "v_cvt_pk_bf16_f32 v209, v82, v83\n"
    "v_cvt_pk_bf16_f32 v210, v84, v85\n"
    "v_cvt_pk_bf16_f32 v211, v86, v87\n"
    "v_cvt_pk_bf16_f32 v212, v88, v89\n"
    "v_cvt_pk_bf16_f32 v213, v90, v91\n"
    "v_cvt_pk_bf16_f32 v214, v92, v93\n"
    "v_cvt_pk_bf16_f32 v215, v94, v95\n"
    "v_cvt_pk_bf16_f32 v216, v96, v97\n"
    "v_cvt_pk_bf16_f32 v217, v98, v99\n"
    "v_cvt_pk_bf16_f32 v218, v100, v101\n"
    "v_cvt_pk_bf16_f32 v219, v102, v103\n"
    "v_cvt_pk_bf16_f32 v220, v104, v105\n"
    "v_cvt_pk_bf16_f32 v221, v106, v107\n"
    "v_cvt_pk_bf16_f32 v222, v108, v109\n"
    "v_cvt_pk_bf16_f32 v223, v110, v111\n"
    "v_cvt_pk_bf16_f32 v224, v112, v113\n"
    "v_cvt_pk_bf16_f32 v225, v114, v115\n"
    "v_cvt_pk_bf16_f32 v226, v116, v117\n"
    "v_cvt_pk_bf16_f32 v227, v118, v119\n"
    "v_cvt_pk_bf16_f32 v228, v120, v121\n"
    "v_cvt_pk_bf16_f32 v229, v122, v123\n"
    "v_cvt_pk_bf16_f32 v230, v124, v125\n"
    "v_cvt_pk_bf16_f32 v231, v126, v127\n"
    "v_cvt_pk_bf16_f32 v232, v128, v129\n"
    "v_cvt_pk_bf16_f32 v233, v130, v131\n"
    "v_cvt_pk_bf16_f32 v234, v132, v133\n"
    "v_cvt_pk_bf16_f32 v235, v134, v135\n"
    "s_cmp_eq_u32 s39, s52\n"
    "s_cbranch_scc1 L_nost_%=\n"
    "s_lshl_b32 s96, s70, 2\n"
    "s_add_u32 s96, s96, s52\n"
    "s_lshl_b32 s96, s96, 1\n"
    "s_add_u32 s96, s96, 1\n"
    "s_lshl_b32 s96, s96, 2\n"
    "s_add_u32 s96, s96, s39\n"
    "s_mul_i32 s96, s96, 9216\n"
    "buffer_store_dwordx4 v[200:203], v252, s[8:11], s96 offen offset:0 sc1\n"
    "buffer_store_dwordx4 v[204:207], v252, s[8:11], s96 offen offset:1024 sc1\n"
    "buffer_store_dwordx4 v[208:211], v252, s[8:11], s96 offen offset:2048 sc1\n"
    "buffer_store_dwordx4 v[212:215], v252, s[8:11], s96 offen offset:3072 sc1\n"
    "s_add_u32 s96, s96, 4096\n"
    "buffer_store_dwordx4 v[216:219], v252, s[8:11], s96 offen offset:0 sc1\n"
    "buffer_store_dwordx4 v[220:223], v252, s[8:11], s96 offen offset:1024 sc1\n"
    "buffer_store_dwordx4 v[224:227], v252, s[8:11], s96 offen offset:2048 sc1\n"
    "buffer_store_dwordx4 v[228:231], v252, s[8:11], s96 offen offset:3072 sc1\n"
    "s_add_u32 s96, s96, 4096\n"
    "buffer_store_dwordx4 v[232:235], v252, s[8:11], s96 offen offset:0 sc1\n"
    "L_nost_%=:\n"
    "s_cmp_eq_u32 s39, s52\n"
    "s_cbranch_scc0 L_ows_%=\n"
    "v_add_u32 v6, s85, v252\n"
    "v_add_u32 v6, 119808, v6\n"
    "ds_write_b128 v6, v[200:203] offset:0\n"
    "ds_write_b128 v6, v[204:207] offset:1024\n"
    "ds_write_b128 v6, v[208:211] offset:2048\n"
    "ds_write_b128 v6, v[212:215] offset:3072\n"
    "ds_write_b128 v6, v[216:219] offset:4096\n"
    "ds_write_b128 v6, v[220:223] offset:5120\n"
    "ds_write_b128 v6, v[224:227] offset:6144\n"
    "ds_write_b128 v6, v[228:231] offset:7168\n"
    "ds_write_b128 v6, v[232:235] offset:8192\n"
    "L_ows_%=:\n"
    "v_lshlrev_b32 v148, 16, v8\n"
    "v_and_b32 v149, 0xffff0000, v8\n"
    "v_lshlrev_b32 v150, 16, v9\n"
    "v_and_b32 v151, 0xffff0000, v9\n"
    "v_lshlrev_b32 v104, 16, v12\n"
    "v_and_b32 v105, 0xffff0000, v12\n"
    "v_lshlrev_b32 v106, 16, v13\n"
    "v_and_b32 v107, 0xffff0000, v13\n"
    "v_add_f32 v148, v148, v104\n"
    "v_add_f32 v149, v149, v105\n"
    "v_add_f32 v150, v150, v106\n"
    "v_add_f32 v151, v151, v107\n"
    "v_lshlrev_b32 v108, 16, v16\n"
    "v_and_b32 v109, 0xffff0000, v16\n"
    "v_lshlrev_b32 v110, 16, v17\n"
    "v_and_b32 v111, 0xffff0000, v17\n"
    "v_add_f32 v148, v148, v108\n"
    "v_add_f32 v149, v149, v109\n"
    "v_add_f32 v150, v150, v110\n"
    "v_add_f32 v151, v151, v111\n"
    "v_lshlrev_b32 v104, 16, v20\n"
    "v_and_b32 v105, 0xffff0000, v20\n"
    "v_lshlrev_b32 v106, 16, v21\n"
    "v_and_b32 v107, 0xffff0000, v21\n"
    "v_add_f32 v148, v148, v104\n"
    "v_add_f32 v149, v149, v105\n"
    "v_add_f32 v150, v150, v106\n"
    "v_add_f32 v151, v151, v107\n"
    "v_mul_f32 v112, 0xbfb8aa3b, v148\n"
    "v_mul_f32 v113, 0xbfb8aa3b, v149\n"
    "v_mul_f32 v114, 0xbfb8aa3b, v150\n"
    "v_mul_f32 v115, 0xbfb8aa3b, v151\n"
    "v_exp_f32 v112, v112\n"
    "v_exp_f32 v113, v113\n"
    "v_exp_f32 v114, v114\n"
    "v_exp_f32 v115, v115\n"
    "s_nop 0\n"
    "v_add_f32 v112, 1.0, v112\n"
    "v_add_f32 v113, 1.0, v113\n"
    "v_add_f32 v114, 1.0, v114\n"
    "v_add_f32 v115, 1.0, v115\n"
    "v_rcp_f32 v112, v112\n"
    "v_rcp_f32 v113, v113\n"
    "v_rcp_f32 v114, v114\n"
    "v_rcp_f32 v115, v115\n"
    "s_nop 0\n"
    "v_mul_f32 v148, v148, v112\n"
    "v_mul_f32 v149, v149, v113\n"
    "v_mul_f32 v150, v150, v114\n"
    "v_mul_f32 v151, v151, v115\n"
    "v_lshlrev_b32 v152, 16, v10\n"
    "v_and_b32 v153, 0xffff0000, v10\n"
    "v_lshlrev_b32 v154, 16, v11\n"
    "v_and_b32 v155, 0xffff0000, v11\n"
    "v_lshlrev_b32 v104, 16, v14\n"
    "v_and_b32 v105, 0xffff0000, v14\n"
    "v_lshlrev_b32 v106, 16, v15\n"
    "v_and_b32 v107, 0xffff0000, v15\n"
    "v_add_f32 v152, v152, v104\n"
    "v_add_f32 v153, v153, v105\n"
    "v_add_f32 v154, v154, v106\n"
    "v_add_f32 v155, v155, v107\n"
    "v_lshlrev_b32 v108, 16, v18\n"
    "v_and_b32 v109, 0xffff0000, v18\n"
    "v_lshlrev_b32 v110, 16, v19\n"
    "v_and_b32 v111, 0xffff0000, v19\n"
    "v_add_f32 v152, v152, v108\n"
    "v_add_f32 v153, v153, v109\n"
    "v_add_f32 v154, v154, v110\n"
    "v_add_f32 v155, v155, v111\n"
    "v_lshlrev_b32 v104, 16, v22\n"
    "v_and_b32 v105, 0xffff0000, v22\n"
    "v_lshlrev_b32 v106, 16, v23\n"
    "v_and_b32 v107, 0xffff0000, v23\n"
    "v_add_f32 v152, v152, v104\n"
    "v_add_f32 v153, v153, v105\n"
    "v_add_f32 v154, v154, v106\n"
    "v_add_f32 v155, v155, v107\n"
    "v_mul_f32 v112, 0xbfb8aa3b, v152\n"
    "v_mul_f32 v113, 0xbfb8aa3b, v153\n"
    "v_mul_f32 v114, 0xbfb8aa3b, v154\n"
    "v_mul_f32 v115, 0xbfb8aa3b, v155\n"
    "v_exp_f32 v112, v112\n"
    "v_exp_f32 v113, v113\n"
    "v_exp_f32 v114, v114\n"
    "v_exp_f32 v115, v115\n"
    "s_nop 0\n"
    "v_add_f32 v112, 1.0, v112\n"
    "v_add_f32 v113, 1.0, v113\n"
    "v_add_f32 v114, 1.0, v114\n"
    "v_add_f32 v115, 1.0, v115\n"
    "v_rcp_f32 v112, v112\n"
    "v_rcp_f32 v113, v113\n"
    "v_rcp_f32 v114, v114\n"
    "v_rcp_f32 v115, v115\n"
    "s_nop 0\n"
    "v_mul_f32 v152, v152, v112\n"
    "v_mul_f32 v153, v153, v113\n"
    "v_mul_f32 v154, v154, v114\n"
    "v_mul_f32 v155, v155, v115\n"
    "v_lshlrev_b32 v156, 16, v24\n"
    "v_and_b32 v157, 0xffff0000, v24\n"
    "v_lshlrev_b32 v158, 16, v25\n"
    "v_and_b32 v159, 0xffff0000, v25\n"
    "v_lshlrev_b32 v104, 16, v28\n"
    "v_and_b32 v105, 0xffff0000, v28\n"
    "v_lshlrev_b32 v106, 16, v29\n"
    "v_and_b32 v107, 0xffff0000, v29\n"
    "v_add_f32 v156, v156, v104\n"
    "v_add_f32 v157, v157, v105\n"
    "v_add_f32 v158, v158, v106\n"
    "v_add_f32 v159, v159, v107\n"
    "v_lshlrev_b32 v108, 16, v32\n"
    "v_and_b32 v109, 0xffff0000, v32\n"
    "v_lshlrev_b32 v110, 16, v33\n"
    "v_and_b32 v111, 0xffff0000, v33\n"
    "v_add_f32 v156, v156, v108\n"
    "v_add_f32 v157, v157, v109\n"
    "v_add_f32 v158, v158, v110\n"
    "v_add_f32 v159, v159, v111\n"
    "v_lshlrev_b32 v104, 16, v36\n"
    "v_and_b32 v105, 0xffff0000, v36\n"
    "v_lshlrev_b32 v106, 16, v37\n"
    "v_and_b32 v107, 0xffff0000, v37\n"
    "v_add_f32 v156, v156, v104\n"
    "v_add_f32 v157, v157, v105\n"
    "v_add_f32 v158, v158, v106\n"
    "v_add_f32 v159, v159, v107\n"
    "v_mul_f32 v112, 0xbfb8aa3b, v156\n"
    "v_mul_f32 v113, 0xbfb8aa3b, v157\n"
    "v_mul_f32 v114, 0xbfb8aa3b, v158\n"
    "v_mul_f32 v115, 0xbfb8aa3b, v159\n"
    "v_exp_f32 v112, v112\n"
    "v_exp_f32 v113, v113\n"
    "v_exp_f32 v114, v114\n"
    "v_exp_f32 v115, v115\n"
    "s_nop 0\n"
    "v_add_f32 v112, 1.0, v112\n"
    "v_add_f32 v113, 1.0, v113\n"
    "v_add_f32 v114, 1.0, v114\n"
    "v_add_f32 v115, 1.0, v115\n"
    "v_rcp_f32 v112, v112\n"
    "v_rcp_f32 v113, v113\n"
    "v_rcp_f32 v114, v114\n"
    "v_rcp_f32 v115, v115\n"
    "s_nop 0\n"
    "v_mul_f32 v156, v156, v112\n"
    "v_mul_f32 v157, v157, v113\n"
    "v_mul_f32 v158, v158, v114\n"
    "v_mul_f32 v159, v159, v115\n"
    "v_lshlrev_b32 v160, 16, v26\n"
    "v_and_b32 v161, 0xffff0000, v26\n"
    "v_lshlrev_b32 v162, 16, v27\n"
    "v_and_b32 v163, 0xffff0000, v27\n"
    "v_lshlrev_b32 v104, 16, v30\n"
    "v_and_b32 v105, 0xffff0000, v30\n"
    "v_lshlrev_b32 v106, 16, v31\n"
    "v_and_b32 v107, 0xffff0000, v31\n"
    "v_add_f32 v160, v160, v104\n"
    "v_add_f32 v161, v161, v105\n"
    "v_add_f32 v162, v162, v106\n"
    "v_add_f32 v163, v163, v107\n"
    "v_lshlrev_b32 v108, 16, v34\n"
    "v_and_b32 v109, 0xffff0000, v34\n"
    "v_lshlrev_b32 v110, 16, v35\n"
    "v_and_b32 v111, 0xffff0000, v35\n"
    "v_add_f32 v160, v160, v108\n"
    "v_add_f32 v161, v161, v109\n"
    "v_add_f32 v162, v162, v110\n"
    "v_add_f32 v163, v163, v111\n"
    "v_lshlrev_b32 v104, 16, v38\n"
    "v_and_b32 v105, 0xffff0000, v38\n"
    "v_lshlrev_b32 v106, 16, v39\n"
    "v_and_b32 v107, 0xffff0000, v39\n"
    "v_add_f32 v160, v160, v104\n"
    "v_add_f32 v161, v161, v105\n"
    "v_add_f32 v162, v162, v106\n"
    "v_add_f32 v163, v163, v107\n"
    "v_mul_f32 v112, 0xbfb8aa3b, v160\n"
    "v_mul_f32 v113, 0xbfb8aa3b, v161\n"
    "v_mul_f32 v114, 0xbfb8aa3b, v162\n"
    "v_mul_f32 v115, 0xbfb8aa3b, v163\n"
    "v_exp_f32 v112, v112\n"
    "v_exp_f32 v113, v113\n"
    "v_exp_f32 v114, v114\n"
    "v_exp_f32 v115, v115\n"
    "s_nop 0\n"
    "v_add_f32 v112, 1.0, v112\n"
    "v_add_f32 v113, 1.0, v113\n"
    "v_add_f32 v114, 1.0, v114\n"
    "v_add_f32 v115, 1.0, v115\n"
    "v_rcp_f32 v112, v112\n"
    "v_rcp_f32 v113, v113\n"
    "v_rcp_f32 v114, v114\n"
    "v_rcp_f32 v115, v115\n"
    "s_nop 0\n"
    "v_mul_f32 v160, v160, v112\n"
    "v_mul_f32 v161, v161, v113\n"
    "v_mul_f32 v162, v162, v114\n"
    "v_mul_f32 v163, v163, v115\n"
    "s_cmp_eq_u32 s39, 0\n"
    "s_cbranch_scc0 L_b_gate_%=\n"
    "v_lshlrev_b32 v164, 16, v40\n"
    "v_and_b32 v165, 0xffff0000, v40\n"
    "v_lshlrev_b32 v166, 16, v41\n"
    "v_and_b32 v167, 0xffff0000, v41\n"
    "v_lshlrev_b32 v104, 16, v44\n"
    "v_and_b32 v105, 0xffff0000, v44\n"
    "v_lshlrev_b32 v106, 16, v45\n"
    "v_and_b32 v107, 0xffff0000, v45\n"
    "v_add_f32 v164, v164, v104\n"
    "v_add_f32 v165, v165, v105\n"
    "v_add_f32 v166, v166, v106\n"
    "v_add_f32 v167, v167, v107\n"
    "v_lshlrev_b32 v108, 16, v48\n"
    "v_and_b32 v109, 0xffff0000, v48\n"
    "v_lshlrev_b32 v110, 16, v49\n"
    "v_and_b32 v111, 0xffff0000, v49\n"
    "v_add_f32 v164, v164, v108\n"
    "v_add_f32 v165, v165, v109\n"
    "v_add_f32 v166, v166, v110\n"
    "v_add_f32 v167, v167, v111\n"
    "v_lshlrev_b32 v104, 16, v52\n"
    "v_and_b32 v105, 0xffff0000, v52\n"
    "v_lshlrev_b32 v106, 16, v53\n"
    "v_and_b32 v107, 0xffff0000, v53\n"
    "v_add_f32 v164, v164, v104\n"
    "v_add_f32 v165, v165, v105\n"
    "v_add_f32 v166, v166, v106\n"
    "v_add_f32 v167, v167, v107\n"
    "v_mul_f32 v112, 0xbfb8aa3b, v164\n"
    "v_mul_f32 v113, 0xbfb8aa3b, v165\n"
    "v_mul_f32 v114, 0xbfb8aa3b, v166\n"
    "v_mul_f32 v115, 0xbfb8aa3b, v167\n"
    "v_exp_f32 v112, v112\n"
    "v_exp_f32 v113, v113\n"
    "v_exp_f32 v114, v114\n"
    "v_exp_f32 v115, v115\n"
    "s_nop 0\n"
    "v_add_f32 v112, 1.0, v112\n"
    "v_add_f32 v113, 1.0, v113\n"
    "v_add_f32 v114, 1.0, v114\n"
    "v_add_f32 v115, 1.0, v115\n"
    "v_rcp_f32 v112, v112\n"
    "v_rcp_f32 v113, v113\n"
    "v_rcp_f32 v114, v114\n"
    "v_rcp_f32 v115, v115\n"
    "s_nop 0\n"
    "v_mul_f32 v164, v164, v112\n"
    "v_mul_f32 v165, v165, v113\n"
    "v_mul_f32 v166, v166, v114\n"
    "v_mul_f32 v167, v167, v115\n"
    "v_lshlrev_b32 v168, 16, v42\n"
    "v_and_b32 v169, 0xffff0000, v42\n"
    "v_lshlrev_b32 v170, 16, v43\n"
    "v_and_b32 v171, 0xffff0000, v43\n"
    "v_lshlrev_b32 v104, 16, v46\n"
    "v_and_b32 v105, 0xffff0000, v46\n"
    "v_lshlrev_b32 v106, 16, v47\n"
    "v_and_b32 v107, 0xffff0000, v47\n"
    "v_add_f32 v168, v168, v104\n"
    "v_add_f32 v169, v169, v105\n"
    "v_add_f32 v170, v170, v106\n"
    "v_add_f32 v171, v171, v107\n"
    "v_lshlrev_b32 v108, 16, v50\n"
    "v_and_b32 v109, 0xffff0000, v50\n"
    "v_lshlrev_b32 v110, 16, v51\n"
    "v_and_b32 v111, 0xffff0000, v51\n"
    "v_add_f32 v168, v168, v108\n"
    "v_add_f32 v169, v169, v109\n"
    "v_add_f32 v170, v170, v110\n"
    "v_add_f32 v171, v171, v111\n"
    "v_lshlrev_b32 v104, 16, v54\n"
    "v_and_b32 v105, 0xffff0000, v54\n"
    "v_lshlrev_b32 v106, 16, v55\n"
    "v_and_b32 v107, 0xffff0000, v55\n"
    "v_add_f32 v168, v168, v104\n"
    "v_add_f32 v169, v169, v105\n"
    "v_add_f32 v170, v170, v106\n"
    "v_add_f32 v171, v171, v107\n"
    "v_mul_f32 v112, 0xbfb8aa3b, v168\n"
    "v_mul_f32 v113, 0xbfb8aa3b, v169\n"
    "v_mul_f32 v114, 0xbfb8aa3b, v170\n"
    "v_mul_f32 v115, 0xbfb8aa3b, v171\n"
    "v_exp_f32 v112, v112\n"
    "v_exp_f32 v113, v113\n"
    "v_exp_f32 v114, v114\n"
    "v_exp_f32 v115, v115\n"
    "s_nop 0\n"
    "v_add_f32 v112, 1.0, v112\n"
    "v_add_f32 v113, 1.0, v113\n"
    "v_add_f32 v114, 1.0, v114\n"
    "v_add_f32 v115, 1.0, v115\n"
    "v_rcp_f32 v112, v112\n"
    "v_rcp_f32 v113, v113\n"
    "v_rcp_f32 v114, v114\n"
    "v_rcp_f32 v115, v115\n"
    "s_nop 0\n"
    "v_mul_f32 v168, v168, v112\n"
    "v_mul_f32 v169, v169, v113\n"
    "v_mul_f32 v170, v170, v114\n"
    "v_mul_f32 v171, v171, v115\n"
    "L_b_gate_%=:\n"
    "s_waitcnt vmcnt(0) lgkmcnt(0)\n"
    "s_barrier\n"
    "s_lshl_b32 s96, s39, 10\n"
    "s_add_u32 s96, s96, s85\n"
    "v_add_u32 v6, s96, v252\n"
    "v_add_u32 v6, 119808, v6\n"
    "ds_read_b128 v[8:11], v6 offset:0\n"
    "ds_read_b128 v[24:27], v6 offset:4096\n"
    "ds_read_b128 v[40:43], v6 offset:8192\n"
    "s_mov_b32 s68, 32768\n"
    "L_b_sld_%=:\n"
    "s_add_u32 s96, s52, 1\n"
    "s_and_b32 s96, s96, 3\n"
    "s_lshl_b32 s97, s70, 2\n"
    "s_add_u32 s96, s96, s97\n"
    "s_lshl_b32 s96, s96, 3\n"
    "s_add_u32 s96, s96, s52\n"
    "s_add_u32 s96, s96, 4\n"
    "s_mul_i32 s96, s96, 9216\n"
    "s_lshl_b32 s97, s39, 10\n"
    "s_add_u32 s65, s96, s97\n"
    "s_add_u32 s96, s52, 2\n"
    "s_and_b32 s96, s96, 3\n"
    "s_lshl_b32 s97, s70, 2\n"
    "s_add_u32 s96, s96, s97\n"
    "s_lshl_b32 s96, s96, 3\n"
    "s_add_u32 s96, s96, s52\n"
    "s_add_u32 s96, s96, 4\n"
    "s_mul_i32 s96, s96, 9216\n"
    "s_lshl_b32 s97, s39, 10\n"
    "s_add_u32 s66, s96, s97\n"
    "s_add_u32 s96, s52, 3\n"
    "s_and_b32 s96, s96, 3\n"
    "s_lshl_b32 s97, s70, 2\n"
    "s_add_u32 s96, s96, s97\n"
    "s_lshl_b32 s96, s96, 3\n"
    "s_add_u32 s96, s96, s52\n"
    "s_add_u32 s96, s96, 4\n"
    "s_mul_i32 s96, s96, 9216\n"
    "s_lshl_b32 s97, s39, 10\n"
    "s_add_u32 s67, s96, s97\n"
    "s_add_u32 s96, s65, 0\n"
    "buffer_load_dwordx4 v[12:15], v252, s[8:11], s96 offen sc1\n"
    "s_add_u32 s96, s66, 0\n"
    "buffer_load_dwordx4 v[16:19], v252, s[8:11], s96 offen sc1\n"
    "s_add_u32 s96, s67, 0\n"
    "buffer_load_dwordx4 v[20:23], v252, s[8:11], s96 offen sc1\n"
    "s_add_u32 s96, s65, 4096\n"
    "buffer_load_dwordx4 v[28:31], v252, s[8:11], s96 offen sc1\n"
    "s_add_u32 s96, s66, 4096\n"
    "buffer_load_dwordx4 v[32:35], v252, s[8:11], s96 offen sc1\n"
    "s_add_u32 s96, s67, 4096\n"
    "buffer_load_dwordx4 v[36:39], v252, s[8:11], s96 offen sc1\n"
    "s_cmp_eq_u32 s39, 0\n"
    "s_cbranch_scc0 L_lds_%=\n"
    "s_add_u32 s96, s65, 8192\n"
    "buffer_load_dwordx4 v[44:47], v252, s[8:11], s96 offen sc1\n"
    "s_add_u32 s96, s66, 8192\n"
    "buffer_load_dwordx4 v[48:51], v252, s[8:11], s96 offen sc1\n"
    "s_add_u32 s96, s67, 8192\n"
    "buffer_load_dwordx4 v[52:55], v252, s[8:11], s96 offen sc1\n"
    "L_lds_%=:\n"
    "s_waitcnt vmcnt(0) lgkmcnt(0)\n"
    "v_max3_u32 v2, v12, v13, v14\n"
    "v_max3_u32 v2, v2, v15, v16\n"
    "v_max3_u32 v2, v2, v17, v18\n"
    "v_max3_u32 v2, v2, v19, v20\n"
    "v_max3_u32 v2, v2, v21, v22\n"
    "v_max3_u32 v2, v2, v23, v28\n"
    "v_max3_u32 v2, v2, v29, v30\n"
    "v_max3_u32 v2, v2, v31, v32\n"
    "v_max3_u32 v2, v2, v33, v34\n"
    "v_max3_u32 v2, v2, v35, v36\n"
    "v_max3_u32 v2, v2, v37, v38\n"
    "v_max_u32 v2, v2, v39\n"
    "s_cmp_eq_u32 s39, 0\n"
    "s_cbranch_scc0 L_b_schk_%=\n"
    "v_max3_u32 v2, v2, v44, v45\n"
    "v_max3_u32 v2, v2, v46, v47\n"
    "v_max3_u32 v2, v2, v48, v49\n"
    "v_max3_u32 v2, v2, v50, v51\n"
    "v_max3_u32 v2, v2, v52, v53\n"
    "v_max3_u32 v2, v2, v54, v55\n"
    "L_b_schk_%=:\n"
    "v_cmp_eq_u32 vcc, -1, v2\n"
    "s_nop 1\n"
    "s_cmp_eq_u64 vcc, 0\n"
    "s_cbranch_scc1 L_b_sok_%=\n"
    "s_sub_u32 s68, s68, 1\n"
    "s_cmp_lg_u32 s68, 0\n"
    "s_cbranch_scc1 L_b_sld_%=\n"
    "L_b_sok_%=:\n"
    "v_lshlrev_b32 v112, 16, v8\n"
    "v_and_b32 v113, 0xffff0000, v8\n"
    "v_lshlrev_b32 v114, 16, v9\n"
    "v_and_b32 v115, 0xffff0000, v9\n"
    "v_lshlrev_b32 v104, 16, v12\n"
    "v_and_b32 v105, 0xffff0000, v12\n"
    "v_lshlrev_b32 v106, 16, v13\n"
    "v_and_b32 v107, 0xffff0000, v13\n"
    "v_add_f32 v112, v112, v104\n"
    "v_add_f32 v113, v113, v105\n"
    "v_add_f32 v114, v114, v106\n"
    "v_add_f32 v115, v115, v107\n"
    "v_lshlrev_b32 v108, 16, v16\n"
    "v_and_b32 v109, 0xffff0000, v16\n"
    "v_lshlrev_b32 v110, 16, v17\n"
    "v_and_b32 v111, 0xffff0000, v17\n"
    "v_add_f32 v112, v112, v108\n"
    "v_add_f32 v113, v113, v109\n"
    "v_add_f32 v114, v114, v110\n"
    "v_add_f32 v115, v115, v111\n"
    "v_lshlrev_b32 v104, 16, v20\n"
    "v_and_b32 v105, 0xffff0000, v20\n"
    "v_lshlrev_b32 v106, 16, v21\n"
    "v_and_b32 v107, 0xffff0000, v21\n"
    "v_add_f32 v112, v112, v104\n"
    "v_add_f32 v113, v113, v105\n"
    "v_add_f32 v114, v114, v106\n"
    "v_add_f32 v115, v115, v107\n"
    "v_lshlrev_b32 v120, 16, v136\n"
    "v_and_b32 v121, 0xffff0000, v136\n"
    "v_lshlrev_b32 v122, 16, v137\n"
    "v_and_b32 v123, 0xffff0000, v137\n"
    "v_fma_f32 v112, v112, v148, v120\n"
    "v_fma_f32 v113, v113, v149, v121\n"
    "v_fma_f32 v114, v114, v150, v122\n"
    "v_fma_f32 v115, v115, v151, v123\n"
    "v_cvt_pk_bf16_f32 v124, v112, v113\n"
    "v_cvt_pk_bf16_f32 v125, v114, v115\n"
    "global_store_dwordx2 v246, v[124:125], s[26:27] sc1\n"
    "v_lshlrev_b32 v112, 16, v10\n"
    "v_and_b32 v113, 0xffff0000, v10\n"
    "v_lshlrev_b32 v114, 16, v11\n"
    "v_and_b32 v115, 0xffff0000, v11\n"
    "v_lshlrev_b32 v104, 16, v14\n"
    "v_and_b32 v105, 0xffff0000, v14\n"
    "v_lshlrev_b32 v106, 16, v15\n"
    "v_and_b32 v107, 0xffff0000, v15\n"
    "v_add_f32 v112, v112, v104\n"
    "v_add_f32 v113, v113, v105\n"
    "v_add_f32 v114, v114, v106\n"
    "v_add_f32 v115, v115, v107\n"
    "v_lshlrev_b32 v108, 16, v18\n"
    "v_and_b32 v109, 0xffff0000, v18\n"
    "v_lshlrev_b32 v110, 16, v19\n"
    "v_and_b32 v111, 0xffff0000, v19\n"
    "v_add_f32 v112, v112, v108\n"
    "v_add_f32 v113, v113, v109\n"
    "v_add_f32 v114, v114, v110\n"
    "v_add_f32 v115, v115, v111\n"
    "v_lshlrev_b32 v104, 16, v22\n"
    "v_and_b32 v105, 0xffff0000, v22\n"
    "v_lshlrev_b32 v106, 16, v23\n"
    "v_and_b32 v107, 0xffff0000, v23\n"
    "v_add_f32 v112, v112, v104\n"
    "v_add_f32 v113, v113, v105\n"
    "v_add_f32 v114, v114, v106\n"
    "v_add_f32 v115, v115, v107\n"
    "v_lshlrev_b32 v120, 16, v138\n"
    "v_and_b32 v121, 0xffff0000, v138\n"
    "v_lshlrev_b32 v122, 16, v139\n"
    "v_and_b32 v123, 0xffff0000, v139\n"
    "v_fma_f32 v112, v112, v152, v120\n"
    "v_fma_f32 v113, v113, v153, v121\n"
    "v_fma_f32 v114, v114, v154, v122\n"
    "v_fma_f32 v115, v115, v155, v123\n"
    "v_cvt_pk_bf16_f32 v126, v112, v113\n"
    "v_cvt_pk_bf16_f32 v127, v114, v115\n"
    "global_store_dwordx2 v247, v[126:127], s[26:27] sc1\n"
    "v_lshlrev_b32 v112, 16, v24\n"
    "v_and_b32 v113, 0xffff0000, v24\n"
    "v_lshlrev_b32 v114, 16, v25\n"
    "v_and_b32 v115, 0xffff0000, v25\n"
    "v_lshlrev_b32 v104, 16, v28\n"
    "v_and_b32 v105, 0xffff0000, v28\n"
    "v_lshlrev_b32 v106, 16, v29\n"
    "v_and_b32 v107, 0xffff0000, v29\n"
    "v_add_f32 v112, v112, v104\n"
    "v_add_f32 v113, v113, v105\n"
    "v_add_f32 v114, v114, v106\n"
    "v_add_f32 v115, v115, v107\n"
    "v_lshlrev_b32 v108, 16, v32\n"
    "v_and_b32 v109, 0xffff0000, v32\n"
    "v_lshlrev_b32 v110, 16, v33\n"
    "v_and_b32 v111, 0xffff0000, v33\n"
    "v_add_f32 v112, v112, v108\n"
    "v_add_f32 v113, v113, v109\n"
    "v_add_f32 v114, v114, v110\n"
    "v_add_f32 v115, v115, v111\n"
    "v_lshlrev_b32 v104, 16, v36\n"
    "v_and_b32 v105, 0xffff0000, v36\n"
    "v_lshlrev_b32 v106, 16, v37\n"
    "v_and_b32 v107, 0xffff0000, v37\n"
    "v_add_f32 v112, v112, v104\n"
    "v_add_f32 v113, v113, v105\n"
    "v_add_f32 v114, v114, v106\n"
    "v_add_f32 v115, v115, v107\n"
    "v_lshlrev_b32 v120, 16, v140\n"
    "v_and_b32 v121, 0xffff0000, v140\n"
    "v_lshlrev_b32 v122, 16, v141\n"
    "v_and_b32 v123, 0xffff0000, v141\n"
    "v_fma_f32 v112, v112, v156, v120\n"
    "v_fma_f32 v113, v113, v157, v121\n"
    "v_fma_f32 v114, v114, v158, v122\n"
    "v_fma_f32 v115, v115, v159, v123\n"
    "v_cvt_pk_bf16_f32 v124, v112, v113\n"
    "v_cvt_pk_bf16_f32 v125, v114, v115\n"
    "global_store_dwordx2 v248, v[124:125], s[26:27] sc1\n"
    "v_lshlrev_b32 v112, 16, v26\n"
    "v_and_b32 v113, 0xffff0000, v26\n"
    "v_lshlrev_b32 v114, 16, v27\n"
    "v_and_b32 v115, 0xffff0000, v27\n"
    "v_lshlrev_b32 v104, 16, v30\n"
    "v_and_b32 v105, 0xffff0000, v30\n"
    "v_lshlrev_b32 v106, 16, v31\n"
    "v_and_b32 v107, 0xffff0000, v31\n"
    "v_add_f32 v112, v112, v104\n"
    "v_add_f32 v113, v113, v105\n"
    "v_add_f32 v114, v114, v106\n"
    "v_add_f32 v115, v115, v107\n"
    "v_lshlrev_b32 v108, 16, v34\n"
    "v_and_b32 v109, 0xffff0000, v34\n"
    "v_lshlrev_b32 v110, 16, v35\n"
    "v_and_b32 v111, 0xffff0000, v35\n"
    "v_add_f32 v112, v112, v108\n"
    "v_add_f32 v113, v113, v109\n"
    "v_add_f32 v114, v114, v110\n"
    "v_add_f32 v115, v115, v111\n"
    "v_lshlrev_b32 v104, 16, v38\n"
    "v_and_b32 v105, 0xffff0000, v38\n"
    "v_lshlrev_b32 v106, 16, v39\n"
    "v_and_b32 v107, 0xffff0000, v39\n"
    "v_add_f32 v112, v112, v104\n"
    "v_add_f32 v113, v113, v105\n"
    "v_add_f32 v114, v114, v106\n"
    "v_add_f32 v115, v115, v107\n"
    "v_lshlrev_b32 v120, 16, v142\n"
    "v_and_b32 v121, 0xffff0000, v142\n"
    "v_lshlrev_b32 v122, 16, v143\n"
    "v_and_b32 v123, 0xffff0000, v143\n"
    "v_fma_f32 v112, v112, v160, v120\n"
    "v_fma_f32 v113, v113, v161, v121\n"
    "v_fma_f32 v114, v114, v162, v122\n"
    "v_fma_f32 v115, v115, v163, v123\n"
    "v_cvt_pk_bf16_f32 v126, v112, v113\n"
    "v_cvt_pk_bf16_f32 v127, v114, v115\n"
    "global_store_dwordx2 v249, v[126:127], s[26:27] sc1\n"
    "s_cmp_eq_u32 s39, 0\n"
    "s_cbranch_scc0 L_b_fin_%=\n"
    "v_lshlrev_b32 v112, 16, v40\n"
    "v_and_b32 v113, 0xffff0000, v40\n"
    "v_lshlrev_b32 v114, 16, v41\n"
    "v_and_b32 v115, 0xffff0000, v41\n"
    "v_lshlrev_b32 v104, 16, v44\n"
    "v_and_b32 v105, 0xffff0000, v44\n"
    "v_lshlrev_b32 v106, 16, v45\n"
    "v_and_b32 v107, 0xffff0000, v45\n"
    "v_add_f32 v112, v112, v104\n"
    "v_add_f32 v113, v113, v105\n"
    "v_add_f32 v114, v114, v106\n"
    "v_add_f32 v115, v115, v107\n"
    "v_lshlrev_b32 v108, 16, v48\n"
    "v_and_b32 v109, 0xffff0000, v48\n"
    "v_lshlrev_b32 v110, 16, v49\n"
    "v_and_b32 v111, 0xffff0000, v49\n"
    "v_add_f32 v112, v112, v108\n"
    "v_add_f32 v113, v113, v109\n"
    "v_add_f32 v114, v114, v110\n"
    "v_add_f32 v115, v115, v111\n"
    "v_lshlrev_b32 v104, 16, v52\n"
    "v_and_b32 v105, 0xffff0000, v52\n"
    "v_lshlrev_b32 v106, 16, v53\n"
    "v_and_b32 v107, 0xffff0000, v53\n"
    "v_add_f32 v112, v112, v104\n"
    "v_add_f32 v113, v113, v105\n"
    "v_add_f32 v114, v114, v106\n"
    "v_add_f32 v115, v115, v107\n"
    "v_lshlrev_b32 v120, 16, v144\n"
    "v_and_b32 v121, 0xffff0000, v144\n"
    "v_lshlrev_b32 v122, 16, v145\n"
    "v_and_b32 v123, 0xffff0000, v145\n"
    "v_fma_f32 v112, v112, v164, v120\n"
    "v_fma_f32 v113, v113, v165, v121\n"
    "v_fma_f32 v114, v114, v166, v122\n"
    "v_fma_f32 v115, v115, v167, v123\n"
    "v_cvt_pk_bf16_f32 v124, v112, v113\n"
    "v_cvt_pk_bf16_f32 v125, v114, v115\n"
    "global_store_dwordx2 v250, v[124:125], s[26:27] sc1\n"
    "v_lshlrev_b32 v112, 16, v42\n"
    "v_and_b32 v113, 0xffff0000, v42\n"
    "v_lshlrev_b32 v114, 16, v43\n"
    "v_and_b32 v115, 0xffff0000, v43\n"
    "v_lshlrev_b32 v104, 16, v46\n"
    "v_and_b32 v105, 0xffff0000, v46\n"
    "v_lshlrev_b32 v106, 16, v47\n"
    "v_and_b32 v107, 0xffff0000, v47\n"
    "v_add_f32 v112, v112, v104\n"
    "v_add_f32 v113, v113, v105\n"
    "v_add_f32 v114, v114, v106\n"
    "v_add_f32 v115, v115, v107\n"
    "v_lshlrev_b32 v108, 16, v50\n"
    "v_and_b32 v109, 0xffff0000, v50\n"
    "v_lshlrev_b32 v110, 16, v51\n"
    "v_and_b32 v111, 0xffff0000, v51\n"
    "v_add_f32 v112, v112, v108\n"
    "v_add_f32 v113, v113, v109\n"
    "v_add_f32 v114, v114, v110\n"
    "v_add_f32 v115, v115, v111\n"
    "v_lshlrev_b32 v104, 16, v54\n"
    "v_and_b32 v105, 0xffff0000, v54\n"
    "v_lshlrev_b32 v106, 16, v55\n"
    "v_and_b32 v107, 0xffff0000, v55\n"
    "v_add_f32 v112, v112, v104\n"
    "v_add_f32 v113, v113, v105\n"
    "v_add_f32 v114, v114, v106\n"
    "v_add_f32 v115, v115, v107\n"
    "v_lshlrev_b32 v120, 16, v146\n"
    "v_and_b32 v121, 0xffff0000, v146\n"
    "v_lshlrev_b32 v122, 16, v147\n"
    "v_and_b32 v123, 0xffff0000, v147\n"
    "v_fma_f32 v112, v112, v168, v120\n"
    "v_fma_f32 v113, v113, v169, v121\n"
    "v_fma_f32 v114, v114, v170, v122\n"
    "v_fma_f32 v115, v115, v171, v123\n"
    "v_cvt_pk_bf16_f32 v126, v112, v113\n"
    "v_cvt_pk_bf16_f32 v127, v114, v115\n"
    "global_store_dwordx2 v251, v[126:127], s[26:27] sc1\n"
    "L_b_fin_%=:\n"
    "s_waitcnt vmcnt(0)\n"
    "s_endpgm\n"
    "L_fhalf_%=:\n"
    "buffer_store_dwordx2 v[178:179], v252, s[52:55], s77 offen sc1\n"
    "buffer_store_dwordx2 v[182:183], v252, s[52:55], s78 offen sc1\n"
    "buffer_store_dwordx2 v[186:187], v252, s[52:55], s79 offen sc1\n"
    "buffer_store_dwordx2 v[190:191], v252, s[52:55], s80 offen sc1\n"
    "s_branch L_fhalf_ret_%=\n"
    "L_evt_%=:\n"
    "s_cmp_eq_u32 s71, 16\n"
    "s_cbranch_scc0 L_evt_n0_%=\n"
    "s_add_u32 s96, s38, 0\n"
    "s_cmp_lt_u32 s96, 144\n"
    "s_cselect_b32 s40, s8, s10\n"
    "s_cselect_b32 s41, s9, s11\n"
    "s_cselect_b32 s97, 0, 144\n"
    "s_sub_u32 s96, s96, s97\n"
    "s_mul_i32 s96, s96, 36864\n"
    "s_add_u32 s40, s40, s96\n"
    "s_addc_u32 s41, s41, 0\n"
    "s_and_b32 s41, s41, 0xffff\n"
    "s_mov_b32 s42, 36864\n"
    "s_mov_b32 s43, 0x20000\n"
    "s_mul_i32 s96, s39, 9216\n"
    "s_add_u32 s97, s96, s85\n"
    "s_add_u32 m0, s97, 0\n"
    "s_nop 0\n"
    "buffer_load_dwordx4 v252, s[40:43], s96 offen offset:0 lds\n"
    "buffer_load_dwordx4 v252, s[40:43], s96 offen offset:1024 lds\n"
    "buffer_load_dwordx4 v252, s[40:43], s96 offen offset:2048 lds\n"
    "buffer_load_dwordx4 v252, s[40:43], s96 offen offset:3072 lds\n"
    "s_add_u32 s96, s96, 4096\n"
    "s_add_u32 m0, s97, 4096\n"
    "s_nop 0\n"
    "buffer_load_dwordx4 v252, s[40:43], s96 offen offset:0 lds\n"
    "buffer_load_dwordx4 v252, s[40:43], s96 offen offset:1024 lds\n"
    "buffer_load_dwordx4 v252, s[40:43], s96 offen offset:2048 lds\n"
    "buffer_load_dwordx4 v252, s[40:43], s96 offen offset:3072 lds\n"
    "s_add_u32 s96, s96, 4096\n"
    "s_add_u32 m0, s97, 8192\n"
    "s_nop 0\n"
    "buffer_load_dwordx4 v252, s[40:43], s96 offen offset:0 lds\n"
    "s_cmp_lt_u32 s38, 16\n"
    "s_cbranch_scc0 L_rt_dma_done_%=\n"
    "s_add_u32 s96, s38, 256\n"
    "s_cmp_lt_u32 s96, 144\n"
    "s_cselect_b32 s40, s8, s10\n"
    "s_cselect_b32 s41, s9, s11\n"
    "s_cselect_b32 s97, 0, 144\n"
    "s_sub_u32 s96, s96, s97\n"
    "s_mul_i32 s96, s96, 36864\n"
    "s_add_u32 s40, s40, s96\n"
    "s_addc_u32 s41, s41, 0\n"
    "s_and_b32 s41, s41, 0xffff\n"
    "s_mov_b32 s42, 36864\n"
    "s_mov_b32 s43, 0x20000\n"
    "s_mul_i32 s96, s39, 9216\n"
    "s_add_u32 s97, s96, s85\n"
    "s_add_u32 m0, s97, 36864\n"
    "s_nop 0\n"
    "buffer_load_dwordx4 v252, s[40:43], s96 offen offset:0 lds\n"
    "buffer_load_dwordx4 v252, s[40:43], s96 offen offset:1024 lds\n"
    "buffer_load_dwordx4 v252, s[40:43], s96 offen offset:2048 lds\n"
    "buffer_load_dwordx4 v252, s[40:43], s96 offen offset:3072 lds\n"
    "s_add_u32 s96, s96, 4096\n"
    "s_add_u32 m0, s97, 40960\n"
    "s_nop 0\n"
    "buffer_load_dwordx4 v252, s[40:43], s96 offen offset:0 lds\n"
    "buffer_load_dwordx4 v252, s[40:43], s96 offen offset:1024 lds\n"
    "buffer_load_dwordx4 v252, s[40:43], s96 offen offset:2048 lds\n"
    "buffer_load_dwordx4 v252, s[40:43], s96 offen offset:3072 lds\n"
    "s_add_u32 s96, s96, 4096\n"
    "s_add_u32 m0, s97, 45056\n"
    "s_nop 0\n"
    "buffer_load_dwordx4 v252, s[40:43], s96 offen offset:0 lds\n"
    "L_rt_dma_done_%=:\n"
    "s_load_dwordx2 s[26:27], s[0:1], 112\n"
    "s_add_u32 s24, s34, 54525952\n"
    "s_addc_u32 s25, s35, 0\n"
    "s_and_b32 s65, s38, 7\n"
    "s_lshr_b32 s52, s65, 1\n"
    "s_and_b32 s65, s65, 1\n"
    "s_lshl_b32 s65, s65, 3\n"
    "s_lshr_b32 s66, s38, 3\n"
    "s_and_b32 s54, s66, 3\n"
    "s_lshr_b32 s66, s66, 2\n"
    "s_add_u32 s53, s65, s66\n"
    "s_lshl_b32 s65, s53, 2\n"
    "s_add_u32 s70, s65, s54\n"
    "s_mov_b32 s8, s24\n"
    "s_and_b32 s9, s25, 0xffff\n"
    "s_mov_b32 s10, 18874368\n"
    "s_mov_b32 s11, 0x20000\n"
    "s_mul_i32 s65, s52, 1327104\n"
    "s_add_u32 s65, s65, 0\n"
    "s_mul_i32 s66, s54, 9216\n"
    "s_add_u32 s65, s65, s66\n"
    "s_add_u32 s40, s12, s65\n"
    "s_addc_u32 s41, s13, 0\n"
    "s_and_b32 s41, s41, 0xffff\n"
    "s_sub_u32 s42, 1327104, s66\n"
    "s_mov_b32 s43, 0x20000\n"
    "s_mul_i32 s48, s39, 36864\n"
    "s_mov_b32 s44, s34\n"
    "s_and_b32 s45, s35, 0xffff\n"
    "s_mov_b32 s46, 18874368\n"
    "s_mov_b32 s47, 0x20000\n"
    "s_lshl_b32 s65, s53, 3\n"
    "s_lshl_b32 s66, s39, 1\n"
    "s_add_u32 s65, s65, s66\n"
    "s_mul_i32 s65, s65, 144\n"
    "s_mul_i32 s66, s52, 36\n"
    "s_add_u32 s65, s65, s66\n"
    "s_lshl_b32 s49, s65, 10\n"
    "s_add_u32 s50, s49, 147456\n"
    "s_mul_i32 s65, s39, 9216\n"
    "s_add_u32 s51, s85, s65\n"
    "s_mov_b32 s72, 15\n"
    "s_branch L_evt_ret_%=\n"
    "L_evt_n0_%=:\n"
    "s_cmp_eq_u32 s71, 15\n"
    "s_cbranch_scc0 L_evt_n1_%=\n"
    "s_mul_i32 s96, s38, 36\n"
    "s_add_u32 s96, s96, s39\n"
    "s_lshl_b32 s96, s96, 10\n"
    "s_add_u32 s16, s12, s96\n"
    "s_addc_u32 s17, s13, 0\n"
    "s_and_b32 s17, s17, 0xffff\n"
    "s_mov_b32 s18, 33792\n"
    "s_mov_b32 s14, 0\n"
    "v_and_b32 v2, 63, v1\n"
    "v_lshrrev_b32 v250, 2, v2\n"
    "v_mul_u32_u24 v250, 1152, v250\n"
    "v_and_b32 v4, 3, v2\n"
    "v_lshl_add_u32 v250, v4, 3, v250\n"
    "s_lshl_b32 s96, s39, 5\n"
    "s_add_u32 s96, s96, s85\n"
    "v_add_u32 v250, s96, v250\n"
    "s_lshl_b32 s96, s70, 2\n"
    "s_add_u32 s96, s96, s52\n"
    "s_lshl_b32 s96, s96, 1\n"
    "s_add_u32 s96, s96, 1\n"
    "s_lshl_b32 s96, s96, 2\n"
    "s_add_u32 s96, s96, s39\n"
    "s_mul_i32 s96, s96, 9216\n"
    "s_add_u32 s76, s24, s96\n"
    "s_addc_u32 s77, s25, 0\n"
    "s_and_b32 s77, s77, 0xffff\n"
    "s_mov_b32 s78, 9216\n"
    "s_mov_b32 s80, 0\n"
    "s_mov_b32 s72, 10\n"
    "s_branch L_evt_ret_%=\n"
    "L_evt_n1_%=:\n"
    "s_cmp_eq_u32 s71, 10\n"
    "s_cbranch_scc0 L_evt_n2_%=\n"
    "s_cmp_lt_u32 s38, 16\n"
    "s_cbranch_scc0 L_rt2_done_%=\n"
    "s_add_u32 s96, s38, 256\n"
    "s_mul_i32 s96, s96, 36\n"
    "s_add_u32 s96, s96, s39\n"
    "s_lshl_b32 s96, s96, 10\n"
    "s_add_u32 s16, s12, s96\n"
    "s_addc_u32 s17, s13, 0\n"
    "s_and_b32 s17, s17, 0xffff\n"
    "s_mov_b32 s14, 0\n"
    "v_and_b32 v2, 63, v1\n"
    "v_lshrrev_b32 v250, 2, v2\n"
    "v_mul_u32_u24 v250, 1152, v250\n"
    "v_and_b32 v4, 3, v2\n"
    "v_lshl_add_u32 v250, v4, 3, v250\n"
    "s_lshl_b32 s96, s39, 5\n"
    "s_add_u32 s96, s96, s85\n"
    "s_add_u32 s96, s96, 36864\n"
    "v_add_u32 v250, s96, v250\n"
    "L_rt2_done_%=:\n"
    "s_mov_b32 s72, 5\n"
    "s_branch L_evt_ret_%=\n"
    "L_evt_n2_%=:\n"
    "s_cmp_eq_u32 s71, 5\n"
    "s_cbranch_scc0 L_evt_n3_%=\n"
    "s_cmp_eq_u32 s39, 0\n"
    "s_cbranch_scc0 L_ep_done_%=\n"
    "s_lshl_b32 s96, s38, 2\n"
    "s_add_u32 s96, s96, 1024\n"
    "v_mov_b32 v4, s96\n"
    "v_mov_b32 v5, s3\n"
    "v_and_b32 v6, 63, v1\n"
    "v_cmp_eq_u32 vcc, 0, v6\n"
    "s_and_saveexec_b64 s[98:99], vcc\n"
    "global_store_dword v4, v5, s[6:7] sc1\n"
    "s_mov_b64 exec, s[98:99]\n"
    "L_ep_done_%=:\n"
    "s_mov_b32 s72, 4\n"
    "s_branch L_evt_ret_%=\n"
    "L_evt_n3_%=:\n"
    "s_cmp_eq_u32 s71, 4\n"
    "s_cbranch_scc0 L_evt_n4_%=\n"
    "s_cmp_eq_u32 s39, 0\n"
    "s_cbranch_scc0 L_epi_done_%=\n"
    "v_and_b32 v2, 63, v1\n"
    "v_lshlrev_b32 v2, 4, v2\n"
    "v_add_u32 v2, 1024, v2\n"
    "global_load_dwordx4 v[4:7], v2, s[6:7] sc1\n"
    "L_epi_done_%=:\n"
    "s_mov_b32 s72, 3\n"
    "s_branch L_evt_ret_%=\n"
    "L_evt_n4_%=:\n"
    "s_cmp_eq_u32 s71, 3\n"
    "s_cbranch_scc0 L_evt_n5_%=\n"
    "s_waitcnt lgkmcnt(0)\n"
    "s_cmp_eq_u32 s39, 0\n"
    "s_cbranch_scc0 L_epl_done_%=\n"
    "s_mov_b32 s97, 32768\n"
    "L_epl_chk_%=:\n"
    "v_cmp_ne_u32 vcc, s3, v4\n"
    "v_cmp_ne_u32 s[98:99], s3, v5\n"
    "s_nop 1\n"
    "s_or_b64 vcc, vcc, s[98:99]\n"
    "v_cmp_ne_u32 s[98:99], s3, v6\n"
    "s_nop 1\n"
    "s_or_b64 vcc, vcc, s[98:99]\n"
    "v_cmp_ne_u32 s[98:99], s3, v7\n"
    "s_nop 1\n"
    "s_or_b64 vcc, vcc, s[98:99]\n"
    "s_cmp_eq_u64 vcc, 0\n"
    "s_cbranch_scc1 L_epl_ok_%=\n"
    "s_sub_u32 s97, s97, 1\n"
    "s_cmp_eq_u32 s97, 0\n"
    "s_cbranch_scc1 L_epl_tmo_%=\n"
    "s_sleep 1\n"
    "global_load_dwordx4 v[4:7], v2, s[6:7] sc1\n"
    "s_waitcnt vmcnt(0)\n"
    "s_branch L_epl_chk_%=\n"
    "L_epl_tmo_%=:\n"
    "L_epl_ok_%=:\n"
    "s_cmp_eq_u32 s38, 0\n"
    "s_cbranch_scc0 L_epl_inv_%=\n"
    "s_mov_b32 s96, 0\n"
    "v_mov_b32 v4, s96\n"
    "v_mov_b32 v5, s3\n"
    "v_and_b32 v6, 63, v1\n"
    "v_cmp_eq_u32 vcc, 0, v6\n"
    "s_and_saveexec_b64 s[98:99], vcc\n"
    "global_store_dword v4, v5, s[6:7] sc1\n"
    "s_mov_b64 exec, s[98:99]\n"
    "L_epl_inv_%=:\n"
    "buffer_inv sc1\n"
    "L_epl_done_%=:\n"
    "s_mov_b32 s72, 999\n"
    "s_branch L_evt_ret_%=\n"
    "L_evt_n5_%=:\n"
    "s_branch L_evt_ret_%=\n"
    "L_attn_fb_%=:\n"
    "s_mov_b32 s15, 1\n"
    "s_waitcnt vmcnt(0) lgkmcnt(0)\n"
    "s_barrier\n"
    "s_lshr_b32 s96, s87, 1\n"
    "s_lshl_b32 s96, s96, 19\n"
    "s_add_u32 s56, s28, s96\n"
    "s_addc_u32 s57, s29, 0\n"
    "s_and_b32 s57, s57, 0xffff\n"
    "s_mov_b32 s58, 524288\n"
    "s_mov_b32 s59, 0x20000\n"
    "s_add_u32 s60, s30, s96\n"
    "s_addc_u32 s61, s31, 0\n"
    "s_and_b32 s61, s61, 0xffff\n"
    "s_mov_b32 s62, 524288\n"
    "s_mov_b32 s63, 0x20000\n"
    "v_and_b32 v2, 63, v1\n"
    "v_lshrrev_b32 v5, 4, v2\n"
    "v_and_b32 v4, 15, v2\n"
    "s_lshl_b32 s86, s39, 12\n"
    "v_lshlrev_b32 v7, 8, v5\n"
    "v_add_u32 v7, s86, v7\n"
    "v_or_b32 v6, 0, v5\n"
    "v_xor_b32 v6, v6, v4\n"
    "v_lshl_add_u32 v244, v6, 4, v7\n"
    "v_or_b32 v6, 4, v5\n"
    "v_xor_b32 v6, v6, v4\n"
    "v_lshl_add_u32 v245, v6, 4, v7\n"
    "v_or_b32 v6, 8, v5\n"
    "v_xor_b32 v6, v6, v4\n"
    "v_lshl_add_u32 v246, v6, 4, v7\n"
    "v_or_b32 v6, 12, v5\n"
    "v_xor_b32 v6, v6, v4\n"
    "v_lshl_add_u32 v247, v6, 4, v7\n"
    "v_or_b32 v6, 0, v5\n"
    "v_lshlrev_b32 v6, 1, v6\n"
    "v_xor_b32 v6, v6, v4\n"
    "v_lshl_add_u32 v248, v6, 4, v7\n"
    "v_or_b32 v6, 4, v5\n"
    "v_lshlrev_b32 v6, 1, v6\n"
    "v_xor_b32 v6, v6, v4\n"
    "v_lshl_add_u32 v249, v6, 4, v7\n"
    "s_add_u32 s98, s85, 73728\n"
    "s_add_u32 s84, s98, s86\n"
    "v_and_b32 v2, 63, v1\n"
    "v_and_b32 v4, 15, v2\n"
    "v_lshrrev_b32 v5, 4, v2\n"
    "v_or_b32 v6, 0, v5\n"
    "v_xor_b32 v6, v6, v4\n"
    "v_lshlrev_b32 v6, 4, v6\n"
    "v_lshl_add_u32 v232, v4, 8, v6\n"
    "v_add_u32 v232, s98, v232\n"
    "v_or_b32 v6, 4, v5\n"
    "v_xor_b32 v6, v6, v4\n"
    "v_lshlrev_b32 v6, 4, v6\n"
    "v_lshl_add_u32 v233, v4, 8, v6\n"
    "v_add_u32 v233, s98, v233\n"
    "v_or_b32 v6, 8, v5\n"
    "v_xor_b32 v6, v6, v4\n"
    "v_lshlrev_b32 v6, 4, v6\n"
    "v_lshl_add_u32 v234, v4, 8, v6\n"
    "v_add_u32 v234, s98, v234\n"
    "v_or_b32 v6, 12, v5\n"
    "v_xor_b32 v6, v6, v4\n"
    "v_lshlrev_b32 v6, 4, v6\n"
    "v_lshl_add_u32 v235, v4, 8, v6\n"
    "v_add_u32 v235, s98, v235\n"
    "v_lshrrev_b32 v4, 2, v2\n"
    "v_and_b32 v6, 7, v4\n"
    "v_and_b32 v7, 3, v2\n"
    "v_lshlrev_b32 v7, 3, v7\n"
    "v_lshl_add_u32 v7, v4, 8, v7\n"
    "v_add_u32 v7, s98, v7\n"
    "v_xor_b32 v4, 0, v6\n"
    "v_lshl_add_u32 v236, v4, 5, v7\n"
    "v_xor_b32 v4, 1, v6\n"
    "v_lshl_add_u32 v237, v4, 5, v7\n"
    "v_xor_b32 v4, 2, v6\n"
    "v_lshl_add_u32 v238, v4, 5, v7\n"
    "v_xor_b32 v4, 3, v6\n"
    "v_lshl_add_u32 v239, v4, 5, v7\n"
    "v_xor_b32 v4, 4, v6\n"
    "v_lshl_add_u32 v240, v4, 5, v7\n"
    "v_xor_b32 v4, 5, v6\n"
    "v_lshl_add_u32 v241, v4, 5, v7\n"
    "v_xor_b32 v4, 6, v6\n"
    "v_lshl_add_u32 v242, v4, 5, v7\n"
    "v_xor_b32 v4, 7, v6\n"
    "v_lshl_add_u32 v243, v4, 5, v7\n"
    "v_mov_b32 v8, 0\n"
    "v_mov_b32 v9, 0\n"
    "v_mov_b32 v10, 0\n"
    "v_mov_b32 v11, 0\n"
    "v_mov_b32 v12, 0\n"
    "v_mov_b32 v13, 0\n"
    "v_mov_b32 v14, 0\n"
    "v_mov_b32 v15, 0\n"
    "v_mov_b32 v16, 0\n"
    "v_mov_b32 v17, 0\n"
    "v_mov_b32 v18, 0\n"
    "v_mov_b32 v19, 0\n"
    "v_mov_b32 v20, 0\n"
    "v_mov_b32 v21, 0\n"
    "v_mov_b32 v22, 0\n"
    "v_mov_b32 v23, 0\n"
    "v_mov_b32 v24, 0\n"
    "v_mov_b32 v25, 0\n"
    "v_mov_b32 v26, 0\n"
    "v_mov_b32 v27, 0\n"
    "v_mov_b32 v28, 0\n"
    "v_mov_b32 v29, 0\n"
    "v_mov_b32 v30, 0\n"
    "v_mov_b32 v31, 0\n"
    "v_mov_b32 v32, 0\n"
    "v_mov_b32 v33, 0\n"
    "v_mov_b32 v34, 0\n"
    "v_mov_b32 v35, 0\n"
    "v_mov_b32 v36, 0\n"
    "v_mov_b32 v37, 0\n"
    "v_mov_b32 v38, 0\n"
    "v_mov_b32 v39, 0\n"
    "v_mov_b32 v40, 0\n"
    "v_mov_b32 v41, 0\n"
    "v_mov_b32 v42, 0\n"
    "v_mov_b32 v43, 0\n"
    "v_mov_b32 v44, 0\n"
    "v_mov_b32 v45, 0\n"
    "v_mov_b32 v46, 0\n"
    "v_mov_b32 v47, 0\n"
    "v_mov_b32 v48, 0\n"
    "v_mov_b32 v49, 0\n"
    "v_mov_b32 v50, 0\n"
    "v_mov_b32 v51, 0\n"
    "v_mov_b32 v52, 0\n"
    "v_mov_b32 v53, 0\n"
    "v_mov_b32 v54, 0\n"
    "v_mov_b32 v55, 0\n"
    "v_mov_b32 v56, 0\n"
    "v_mov_b32 v57, 0\n"
    "v_mov_b32 v58, 0\n"
    "v_mov_b32 v59, 0\n"
    "v_mov_b32 v60, 0\n"
    "v_mov_b32 v61, 0\n"
    "v_mov_b32 v62, 0\n"
    "v_mov_b32 v63, 0\n"
    "v_mov_b32 v64, 0\n"
    "v_mov_b32 v65, 0\n"
    "v_mov_b32 v66, 0\n"
    "v_mov_b32 v67, 0\n"
    "v_mov_b32 v68, 0\n"
    "v_mov_b32 v69, 0\n"
    "v_mov_b32 v70, 0\n"
    "v_mov_b32 v71, 0\n"
    "v_mov_b32 v72, 0\n"
    "v_mov_b32 v73, 0\n"
    "v_mov_b32 v74, 0\n"
    "v_mov_b32 v75, 0\n"
    "v_mov_b32 v76, 0\n"
    "v_mov_b32 v77, 0\n"
    "v_mov_b32 v78, 0\n"
    "v_mov_b32 v79, 0\n"
    "v_mov_b32 v80, 0\n"
    "v_mov_b32 v81, 0\n"
    "v_mov_b32 v82, 0\n"
    "v_mov_b32 v83, 0\n"
    "v_mov_b32 v84, 0\n"
    "v_mov_b32 v85, 0\n"
    "v_mov_b32 v86, 0\n"
    "v_mov_b32 v87, 0\n"
    "v_mov_b32 v88, 0\n"
    "v_mov_b32 v89, 0\n"
    "v_mov_b32 v90, 0\n"
    "v_mov_b32 v91, 0\n"
    "v_mov_b32 v92, 0\n"
    "v_mov_b32 v93, 0\n"
    "v_mov_b32 v94, 0\n"
    "v_mov_b32 v95, 0\n"
    "v_mov_b32 v96, 0\n"
    "v_mov_b32 v97, 0\n"
    "v_mov_b32 v98, 0\n"
    "v_mov_b32 v99, 0\n"
    "v_mov_b32 v100, 0\n"
    "v_mov_b32 v101, 0\n"
    "v_mov_b32 v102, 0\n"
    "v_mov_b32 v103, 0\n"
    "v_mov_b32 v104, 0\n"
    "v_mov_b32 v105, 0\n"
    "v_mov_b32 v106, 0\n"
    "v_mov_b32 v107, 0\n"
    "v_mov_b32 v108, 0\n"
    "v_mov_b32 v109, 0\n"
    "v_mov_b32 v110, 0\n"
    "v_mov_b32 v111, 0\n"
    "v_mov_b32 v112, 0\n"
    "v_mov_b32 v113, 0\n"
    "v_mov_b32 v114, 0\n"
    "v_mov_b32 v115, 0\n"
    "v_mov_b32 v116, 0\n"
    "v_mov_b32 v117, 0\n"
    "v_mov_b32 v118, 0\n"
    "v_mov_b32 v119, 0\n"
    "v_mov_b32 v120, 0\n"
    "v_mov_b32 v121, 0\n"
    "v_mov_b32 v122, 0\n"
    "v_mov_b32 v123, 0\n"
    "v_mov_b32 v124, 0\n"
    "v_mov_b32 v125, 0\n"
    "v_mov_b32 v126, 0\n"
    "v_mov_b32 v127, 0\n"
    "v_mov_b32 v128, 0\n"
    "v_mov_b32 v129, 0\n"
    "v_mov_b32 v130, 0\n"
    "v_mov_b32 v131, 0\n"
    "v_mov_b32 v132, 0\n"
    "v_mov_b32 v133, 0\n"
    "v_mov_b32 v134, 0\n"
    "v_mov_b32 v135, 0\n"
    "v_accvgpr_write_b32 a0, 0\n"
    "v_accvgpr_write_b32 a1, 0\n"
    "v_accvgpr_write_b32 a2, 0\n"
    "v_accvgpr_write_b32 a3, 0\n"
    "v_accvgpr_write_b32 a4, 0\n"
    "v_accvgpr_write_b32 a5, 0\n"
    "v_accvgpr_write_b32 a6, 0\n"
    "v_accvgpr_write_b32 a7, 0\n"
    "v_accvgpr_write_b32 a8, 0\n"
    "v_accvgpr_write_b32 a9, 0\n"
    "v_accvgpr_write_b32 a10, 0\n"
    "v_accvgpr_write_b32 a11, 0\n"
    "v_accvgpr_write_b32 a12, 0\n"
    "v_accvgpr_write_b32 a13, 0\n"
    "v_accvgpr_write_b32 a14, 0\n"
    "v_accvgpr_write_b32 a15, 0\n"
    "v_mov_b32 v250, 0xff7fffff\n"
    "v_mov_b32 v251, 0xff7fffff\n"
    "v_mov_b32 v253, 0xff7fffff\n"
    "v_mov_b32 v255, 0xff7fffff\n"
    "s_mov_b32 s82, 0\n"
    "s_mov_b32 s83, 0\n"
    "s_mov_b32 s71, 32\n"
    "L_attn_fb_loop_%=:\n"
    "s_add_u32 m0, s84, 0\n"
    "s_nop 0\n"
    "buffer_load_dwordx4 v244, s[56:59], s82 offen lds\n"
    "buffer_load_dwordx4 v245, s[56:59], s82 offen offset:1024 lds\n"
    "buffer_load_dwordx4 v246, s[56:59], s82 offen offset:2048 lds\n"
    "buffer_load_dwordx4 v247, s[56:59], s82 offen offset:3072 lds\n"
    "s_add_u32 m0, s84, 32768\n"
    "s_nop 0\n"
    "buffer_load_dwordx4 v248, s[60:63], s83 offen lds\n"
    "buffer_load_dwordx4 v249, s[60:63], s83 offen offset:1024 lds\n"
    "buffer_load_dwordx4 v248, s[60:63], s83 offen offset:2048 lds\n"
    "buffer_load_dwordx4 v249, s[60:63], s83 offen offset:3072 lds\n"
    "s_add_u32 s82, s82, 16384\n"
    "s_add_u32 s83, s83, 16384\n"
    "s_waitcnt vmcnt(0)\n"
    "s_barrier\n"
    "ds_read_b128 a[80:83], v232 offset:0\n"
    "ds_read_b128 a[84:87], v233 offset:0\n"
    "ds_read_b128 a[88:91], v234 offset:0\n"
    "ds_read_b128 a[92:95], v235 offset:0\n"
    "ds_read_b128 a[96:99], v232 offset:4096\n"
    "ds_read_b128 a[100:103], v233 offset:4096\n"
    "ds_read_b128 a[104:107], v234 offset:4096\n"
    "ds_read_b128 a[108:111], v235 offset:4096\n"
    "s_waitcnt lgkmcnt(0)\n"
    "v_mfma_f32_16x16x32_bf16 v[168:171], a[80:83], a[16:19], 0\n"
    "v_mfma_f32_16x16x32_bf16 v[168:171], a[84:87], a[20:23], v[168:171]\n"
    "v_mfma_f32_16x16x32_bf16 v[168:171], a[88:91], a[24:27], v[168:171]\n"
    "v_mfma_f32_16x16x32_bf16 v[168:171], a[92:95], a[28:31], v[168:171]\n"
    "v_mfma_f32_16x16x32_bf16 v[184:187], a[80:83], a[32:35], 0\n"
    "v_mfma_f32_16x16x32_bf16 v[184:187], a[84:87], a[36:39], v[184:187]\n"
    "v_mfma_f32_16x16x32_bf16 v[184:187], a[88:91], a[40:43], v[184:187]\n"
    "v_mfma_f32_16x16x32_bf16 v[184:187], a[92:95], a[44:47], v[184:187]\n"
    "v_mfma_f32_16x16x32_bf16 v[200:203], a[80:83], a[48:51], 0\n"
    "v_mfma_f32_16x16x32_bf16 v[200:203], a[84:87], a[52:55], v[200:203]\n"
    "v_mfma_f32_16x16x32_bf16 v[200:203], a[88:91], a[56:59], v[200:203]\n"
    "v_mfma_f32_16x16x32_bf16 v[200:203], a[92:95], a[60:63], v[200:203]\n"
    "v_mfma_f32_16x16x32_bf16 v[216:219], a[80:83], a[64:67], 0\n"
    "v_mfma_f32_16x16x32_bf16 v[216:219], a[84:87], a[68:71], v[216:219]\n"
    "v_mfma_f32_16x16x32_bf16 v[216:219], a[88:91], a[72:75], v[216:219]\n"
    "v_mfma_f32_16x16x32_bf16 v[216:219], a[92:95], a[76:79], v[216:219]\n"
    "v_mfma_f32_16x16x32_bf16 v[172:175], a[96:99], a[16:19], 0\n"
    "v_mfma_f32_16x16x32_bf16 v[172:175], a[100:103], a[20:23], v[172:175]\n"
    "v_mfma_f32_16x16x32_bf16 v[172:175], a[104:107], a[24:27], v[172:175]\n"
    "v_mfma_f32_16x16x32_bf16 v[172:175], a[108:111], a[28:31], v[172:175]\n"
    "v_mfma_f32_16x16x32_bf16 v[188:191], a[96:99], a[32:35], 0\n"
    "v_mfma_f32_16x16x32_bf16 v[188:191], a[100:103], a[36:39], v[188:191]\n"
    "v_mfma_f32_16x16x32_bf16 v[188:191], a[104:107], a[40:43], v[188:191]\n"
    "v_mfma_f32_16x16x32_bf16 v[188:191], a[108:111], a[44:47], v[188:191]\n"
    "v_mfma_f32_16x16x32_bf16 v[204:207], a[96:99], a[48:51], 0\n"
    "v_mfma_f32_16x16x32_bf16 v[204:207], a[100:103], a[52:55], v[204:207]\n"
    "v_mfma_f32_16x16x32_bf16 v[204:207], a[104:107], a[56:59], v[204:207]\n"
    "v_mfma_f32_16x16x32_bf16 v[204:207], a[108:111], a[60:63], v[204:207]\n"
    "v_mfma_f32_16x16x32_bf16 v[220:223], a[96:99], a[64:67], 0\n"
    "v_mfma_f32_16x16x32_bf16 v[220:223], a[100:103], a[68:71], v[220:223]\n"
    "v_mfma_f32_16x16x32_bf16 v[220:223], a[104:107], a[72:75], v[220:223]\n"
    "v_mfma_f32_16x16x32_bf16 v[220:223], a[108:111], a[76:79], v[220:223]\n"
    "ds_read_b128 a[80:83], v232 offset:8192\n"
    "ds_read_b128 a[84:87], v233 offset:8192\n"
    "ds_read_b128 a[88:91], v234 offset:8192\n"
    "ds_read_b128 a[92:95], v235 offset:8192\n"
    "ds_read_b128 a[96:99], v232 offset:12288\n"
    "ds_read_b128 a[100:103], v233 offset:12288\n"
    "ds_read_b128 a[104:107], v234 offset:12288\n"
    "ds_read_b128 a[108:111], v235 offset:12288\n"
    "s_waitcnt lgkmcnt(0)\n"
    "v_mfma_f32_16x16x32_bf16 v[176:179], a[80:83], a[16:19], 0\n"
    "v_mfma_f32_16x16x32_bf16 v[176:179], a[84:87], a[20:23], v[176:179]\n"
    "v_mfma_f32_16x16x32_bf16 v[176:179], a[88:91], a[24:27], v[176:179]\n"
    "v_mfma_f32_16x16x32_bf16 v[176:179], a[92:95], a[28:31], v[176:179]\n"
    "v_mfma_f32_16x16x32_bf16 v[192:195], a[80:83], a[32:35], 0\n"
    "v_mfma_f32_16x16x32_bf16 v[192:195], a[84:87], a[36:39], v[192:195]\n"
    "v_mfma_f32_16x16x32_bf16 v[192:195], a[88:91], a[40:43], v[192:195]\n"
    "v_mfma_f32_16x16x32_bf16 v[192:195], a[92:95], a[44:47], v[192:195]\n"
    "v_mfma_f32_16x16x32_bf16 v[208:211], a[80:83], a[48:51], 0\n"
    "v_mfma_f32_16x16x32_bf16 v[208:211], a[84:87], a[52:55], v[208:211]\n"
    "v_mfma_f32_16x16x32_bf16 v[208:211], a[88:91], a[56:59], v[208:211]\n"
    "v_mfma_f32_16x16x32_bf16 v[208:211], a[92:95], a[60:63], v[208:211]\n"
    "v_mfma_f32_16x16x32_bf16 v[224:227], a[80:83], a[64:67], 0\n"
    "v_mfma_f32_16x16x32_bf16 v[224:227], a[84:87], a[68:71], v[224:227]\n"
    "v_mfma_f32_16x16x32_bf16 v[224:227], a[88:91], a[72:75], v[224:227]\n"
    "v_mfma_f32_16x16x32_bf16 v[224:227], a[92:95], a[76:79], v[224:227]\n"
    "v_mfma_f32_16x16x32_bf16 v[180:183], a[96:99], a[16:19], 0\n"
    "v_mfma_f32_16x16x32_bf16 v[180:183], a[100:103], a[20:23], v[180:183]\n"
    "v_mfma_f32_16x16x32_bf16 v[180:183], a[104:107], a[24:27], v[180:183]\n"
    "v_mfma_f32_16x16x32_bf16 v[180:183], a[108:111], a[28:31], v[180:183]\n"
    "v_mfma_f32_16x16x32_bf16 v[196:199], a[96:99], a[32:35], 0\n"
    "v_mfma_f32_16x16x32_bf16 v[196:199], a[100:103], a[36:39], v[196:199]\n"
    "v_mfma_f32_16x16x32_bf16 v[196:199], a[104:107], a[40:43], v[196:199]\n"
    "v_mfma_f32_16x16x32_bf16 v[196:199], a[108:111], a[44:47], v[196:199]\n"
    "v_mfma_f32_16x16x32_bf16 v[212:215], a[96:99], a[48:51], 0\n"
    "v_mfma_f32_16x16x32_bf16 v[212:215], a[100:103], a[52:55], v[212:215]\n"
    "v_mfma_f32_16x16x32_bf16 v[212:215], a[104:107], a[56:59], v[212:215]\n"
    "v_mfma_f32_16x16x32_bf16 v[212:215], a[108:111], a[60:63], v[212:215]\n"
    "v_mfma_f32_16x16x32_bf16 v[228:231], a[96:99], a[64:67], 0\n"
    "v_mfma_f32_16x16x32_bf16 v[228:231], a[100:103], a[68:71], v[228:231]\n"
    "v_mfma_f32_16x16x32_bf16 v[228:231], a[104:107], a[72:75], v[228:231]\n"
    "v_mfma_f32_16x16x32_bf16 v[228:231], a[108:111], a[76:79], v[228:231]\n"
    "s_nop 7\n"
    "v_max3_f32 v2, v168, v169, v170\n"
    "v_max3_f32 v2, v2, v171, v172\n"
    "v_max3_f32 v2, v2, v173, v174\n"
    "v_max3_f32 v2, v2, v175, v176\n"
    "v_max3_f32 v2, v2, v177, v178\n"
    "v_max3_f32 v2, v2, v179, v180\n"
    "v_max3_f32 v2, v2, v181, v182\n"
    "v_max_f32 v2, v2, v183\n"
    "v_mov_b32 v3, v2\n"
    "s_nop 1\n"
    "v_permlane16_swap_b32 v2, v3\n"
    "s_nop 1\n"
    "v_max_f32 v2, v2, v3\n"
    "v_mov_b32 v3, v2\n"
    "s_nop 1\n"
    "v_permlane32_swap_b32 v2, v3\n"
    "s_nop 1\n"
    "v_max_f32 v2, v2, v3\n"
    "v_max_f32 v2, v2, v250\n"
    "v_sub_f32 v4, v250, v2\n"
    "v_exp_f32 v4, v4\n"
    "v_mov_b32 v250, v2\n"
    "v_sub_f32 v168, v168, v250\n"
    "v_sub_f32 v169, v169, v250\n"
    "v_sub_f32 v170, v170, v250\n"
    "v_sub_f32 v171, v171, v250\n"
    "v_sub_f32 v172, v172, v250\n"
    "v_sub_f32 v173, v173, v250\n"
    "v_sub_f32 v174, v174, v250\n"
    "v_sub_f32 v175, v175, v250\n"
    "v_sub_f32 v176, v176, v250\n"
    "v_sub_f32 v177, v177, v250\n"
    "v_sub_f32 v178, v178, v250\n"
    "v_sub_f32 v179, v179, v250\n"
    "v_sub_f32 v180, v180, v250\n"
    "v_sub_f32 v181, v181, v250\n"
    "v_sub_f32 v182, v182, v250\n"
    "v_sub_f32 v183, v183, v250\n"
    "v_exp_f32 v168, v168\n"
    "v_exp_f32 v169, v169\n"
    "v_exp_f32 v170, v170\n"
    "v_exp_f32 v171, v171\n"
    "v_exp_f32 v172, v172\n"
    "v_exp_f32 v173, v173\n"
    "v_exp_f32 v174, v174\n"
    "v_exp_f32 v175, v175\n"
    "v_exp_f32 v176, v176\n"
    "v_exp_f32 v177, v177\n"
    "v_exp_f32 v178, v178\n"
    "v_exp_f32 v179, v179\n"
    "v_exp_f32 v180, v180\n"
    "v_exp_f32 v181, v181\n"
    "v_exp_f32 v182, v182\n"
    "v_exp_f32 v183, v183\n"
    "s_nop 0\n"
    "v_cvt_pk_bf16_f32 v136, v168, v169\n"
    "v_cvt_pk_bf16_f32 v137, v170, v171\n"
    "v_cvt_pk_bf16_f32 v138, v172, v173\n"
    "v_cvt_pk_bf16_f32 v139, v174, v175\n"
    "v_cvt_pk_bf16_f32 v140, v176, v177\n"
    "v_cvt_pk_bf16_f32 v141, v178, v179\n"
    "v_cvt_pk_bf16_f32 v142, v180, v181\n"
    "v_cvt_pk_bf16_f32 v143, v182, v183\n"
    "v_mul_f32 v8, v8, v4\n"
    "v_mul_f32 v9, v9, v4\n"
    "v_mul_f32 v10, v10, v4\n"
    "v_mul_f32 v11, v11, v4\n"
    "v_mul_f32 v24, v24, v4\n"
    "v_mul_f32 v25, v25, v4\n"
    "v_mul_f32 v26, v26, v4\n"
    "v_mul_f32 v27, v27, v4\n"
    "v_mul_f32 v40, v40, v4\n"
    "v_mul_f32 v41, v41, v4\n"
    "v_mul_f32 v42, v42, v4\n"
    "v_mul_f32 v43, v43, v4\n"
    "v_mul_f32 v56, v56, v4\n"
    "v_mul_f32 v57, v57, v4\n"
    "v_mul_f32 v58, v58, v4\n"
    "v_mul_f32 v59, v59, v4\n"
    "v_mul_f32 v72, v72, v4\n"
    "v_mul_f32 v73, v73, v4\n"
    "v_mul_f32 v74, v74, v4\n"
    "v_mul_f32 v75, v75, v4\n"
    "v_mul_f32 v88, v88, v4\n"
    "v_mul_f32 v89, v89, v4\n"
    "v_mul_f32 v90, v90, v4\n"
    "v_mul_f32 v91, v91, v4\n"
    "v_mul_f32 v104, v104, v4\n"
    "v_mul_f32 v105, v105, v4\n"
    "v_mul_f32 v106, v106, v4\n"
    "v_mul_f32 v107, v107, v4\n"
    "v_mul_f32 v120, v120, v4\n"
    "v_mul_f32 v121, v121, v4\n"
    "v_mul_f32 v122, v122, v4\n"
    "v_mul_f32 v123, v123, v4\n"
    "v_accvgpr_read_b32 v2, a0\n"
    "s_nop 0\n"
    "v_mul_f32 v2, v2, v4\n"
    "s_nop 0\n"
    "v_accvgpr_write_b32 a0, v2\n"
    "v_accvgpr_read_b32 v2, a1\n"
    "s_nop 0\n"
    "v_mul_f32 v2, v2, v4\n"
    "s_nop 0\n"
    "v_accvgpr_write_b32 a1, v2\n"
    "v_accvgpr_read_b32 v2, a2\n"
    "s_nop 0\n"
    "v_mul_f32 v2, v2, v4\n"
    "s_nop 0\n"
    "v_accvgpr_write_b32 a2, v2\n"
    "v_accvgpr_read_b32 v2, a3\n"
    "s_nop 0\n"
    "v_mul_f32 v2, v2, v4\n"
    "s_nop 0\n"
    "v_accvgpr_write_b32 a3, v2\n"
    "v_max3_f32 v2, v184, v185, v186\n"
    "v_max3_f32 v2, v2, v187, v188\n"
    "v_max3_f32 v2, v2, v189, v190\n"
    "v_max3_f32 v2, v2, v191, v192\n"
    "v_max3_f32 v2, v2, v193, v194\n"
    "v_max3_f32 v2, v2, v195, v196\n"
    "v_max3_f32 v2, v2, v197, v198\n"
    "v_max_f32 v2, v2, v199\n"
    "v_mov_b32 v3, v2\n"
    "s_nop 1\n"
    "v_permlane16_swap_b32 v2, v3\n"
    "s_nop 1\n"
    "v_max_f32 v2, v2, v3\n"
    "v_mov_b32 v3, v2\n"
    "s_nop 1\n"
    "v_permlane32_swap_b32 v2, v3\n"
    "s_nop 1\n"
    "v_max_f32 v2, v2, v3\n"
    "v_max_f32 v2, v2, v251\n"
    "v_sub_f32 v5, v251, v2\n"
    "v_exp_f32 v5, v5\n"
    "v_mov_b32 v251, v2\n"
    "v_sub_f32 v184, v184, v251\n"
    "v_sub_f32 v185, v185, v251\n"
    "v_sub_f32 v186, v186, v251\n"
    "v_sub_f32 v187, v187, v251\n"
    "v_sub_f32 v188, v188, v251\n"
    "v_sub_f32 v189, v189, v251\n"
    "v_sub_f32 v190, v190, v251\n"
    "v_sub_f32 v191, v191, v251\n"
    "v_sub_f32 v192, v192, v251\n"
    "v_sub_f32 v193, v193, v251\n"
    "v_sub_f32 v194, v194, v251\n"
    "v_sub_f32 v195, v195, v251\n"
    "v_sub_f32 v196, v196, v251\n"
    "v_sub_f32 v197, v197, v251\n"
    "v_sub_f32 v198, v198, v251\n"
    "v_sub_f32 v199, v199, v251\n"
    "v_exp_f32 v184, v184\n"
    "v_exp_f32 v185, v185\n"
    "v_exp_f32 v186, v186\n"
    "v_exp_f32 v187, v187\n"
    "v_exp_f32 v188, v188\n"
    "v_exp_f32 v189, v189\n"
    "v_exp_f32 v190, v190\n"
    "v_exp_f32 v191, v191\n"
    "v_exp_f32 v192, v192\n"
    "v_exp_f32 v193, v193\n"
    "v_exp_f32 v194, v194\n"
    "v_exp_f32 v195, v195\n"
    "v_exp_f32 v196, v196\n"
    "v_exp_f32 v197, v197\n"
    "v_exp_f32 v198, v198\n"
    "v_exp_f32 v199, v199\n"
    "s_nop 0\n"
    "v_cvt_pk_bf16_f32 v144, v184, v185\n"
    "v_cvt_pk_bf16_f32 v145, v186, v187\n"
    "v_cvt_pk_bf16_f32 v146, v188, v189\n"
    "v_cvt_pk_bf16_f32 v147, v190, v191\n"
    "v_cvt_pk_bf16_f32 v148, v192, v193\n"
    "v_cvt_pk_bf16_f32 v149, v194, v195\n"
    "v_cvt_pk_bf16_f32 v150, v196, v197\n"
    "v_cvt_pk_bf16_f32 v151, v198, v199\n"
    "v_mul_f32 v12, v12, v5\n"
    "v_mul_f32 v13, v13, v5\n"
    "v_mul_f32 v14, v14, v5\n"
    "v_mul_f32 v15, v15, v5\n"
    "v_mul_f32 v28, v28, v5\n"
    "v_mul_f32 v29, v29, v5\n"
    "v_mul_f32 v30, v30, v5\n"
    "v_mul_f32 v31, v31, v5\n"
    "v_mul_f32 v44, v44, v5\n"
    "v_mul_f32 v45, v45, v5\n"
    "v_mul_f32 v46, v46, v5\n"
    "v_mul_f32 v47, v47, v5\n"
    "v_mul_f32 v60, v60, v5\n"
    "v_mul_f32 v61, v61, v5\n"
    "v_mul_f32 v62, v62, v5\n"
    "v_mul_f32 v63, v63, v5\n"
    "v_mul_f32 v76, v76, v5\n"
    "v_mul_f32 v77, v77, v5\n"
    "v_mul_f32 v78, v78, v5\n"
    "v_mul_f32 v79, v79, v5\n"
    "v_mul_f32 v92, v92, v5\n"
    "v_mul_f32 v93, v93, v5\n"
    "v_mul_f32 v94, v94, v5\n"
    "v_mul_f32 v95, v95, v5\n"
    "v_mul_f32 v108, v108, v5\n"
    "v_mul_f32 v109, v109, v5\n"
    "v_mul_f32 v110, v110, v5\n"
    "v_mul_f32 v111, v111, v5\n"
    "v_mul_f32 v124, v124, v5\n"
    "v_mul_f32 v125, v125, v5\n"
    "v_mul_f32 v126, v126, v5\n"
    "v_mul_f32 v127, v127, v5\n"
    "v_accvgpr_read_b32 v2, a4\n"
    "s_nop 0\n"
    "v_mul_f32 v2, v2, v5\n"
    "s_nop 0\n"
    "v_accvgpr_write_b32 a4, v2\n"
    "v_accvgpr_read_b32 v2, a5\n"
    "s_nop 0\n"
    "v_mul_f32 v2, v2, v5\n"
    "s_nop 0\n"
    "v_accvgpr_write_b32 a5, v2\n"
    "v_accvgpr_read_b32 v2, a6\n"
    "s_nop 0\n"
    "v_mul_f32 v2, v2, v5\n"
    "s_nop 0\n"
    "v_accvgpr_write_b32 a6, v2\n"
    "v_accvgpr_read_b32 v2, a7\n"
    "s_nop 0\n"
    "v_mul_f32 v2, v2, v5\n"
    "s_nop 0\n"
    "v_accvgpr_write_b32 a7, v2\n"
    "v_max3_f32 v2, v200, v201, v202\n"
    "v_max3_f32 v2, v2, v203, v204\n"
    "v_max3_f32 v2, v2, v205, v206\n"
    "v_max3_f32 v2, v2, v207, v208\n"
    "v_max3_f32 v2, v2, v209, v210\n"
    "v_max3_f32 v2, v2, v211, v212\n"
    "v_max3_f32 v2, v2, v213, v214\n"
    "v_max_f32 v2, v2, v215\n"
    "v_mov_b32 v3, v2\n"
    "s_nop 1\n"
    "v_permlane16_swap_b32 v2, v3\n"
    "s_nop 1\n"
    "v_max_f32 v2, v2, v3\n"
    "v_mov_b32 v3, v2\n"
    "s_nop 1\n"
    "v_permlane32_swap_b32 v2, v3\n"
    "s_nop 1\n"
    "v_max_f32 v2, v2, v3\n"
    "v_max_f32 v2, v2, v253\n"
    "v_sub_f32 v6, v253, v2\n"
    "v_exp_f32 v6, v6\n"
    "v_mov_b32 v253, v2\n"
    "v_sub_f32 v200, v200, v253\n"
    "v_sub_f32 v201, v201, v253\n"
    "v_sub_f32 v202, v202, v253\n"
    "v_sub_f32 v203, v203, v253\n"
    "v_sub_f32 v204, v204, v253\n"
    "v_sub_f32 v205, v205, v253\n"
    "v_sub_f32 v206, v206, v253\n"
    "v_sub_f32 v207, v207, v253\n"
    "v_sub_f32 v208, v208, v253\n"
    "v_sub_f32 v209, v209, v253\n"
    "v_sub_f32 v210, v210, v253\n"
    "v_sub_f32 v211, v211, v253\n"
    "v_sub_f32 v212, v212, v253\n"
    "v_sub_f32 v213, v213, v253\n"
    "v_sub_f32 v214, v214, v253\n"
    "v_sub_f32 v215, v215, v253\n"
    "v_exp_f32 v200, v200\n"
    "v_exp_f32 v201, v201\n"
    "v_exp_f32 v202, v202\n"
    "v_exp_f32 v203, v203\n"
    "v_exp_f32 v204, v204\n"
    "v_exp_f32 v205, v205\n"
    "v_exp_f32 v206, v206\n"
    "v_exp_f32 v207, v207\n"
    "v_exp_f32 v208, v208\n"
    "v_exp_f32 v209, v209\n"
    "v_exp_f32 v210, v210\n"
    "v_exp_f32 v211, v211\n"
    "v_exp_f32 v212, v212\n"
    "v_exp_f32 v213, v213\n"
    "v_exp_f32 v214, v214\n"
    "v_exp_f32 v215, v215\n"
    "s_nop 0\n"
    "v_cvt_pk_bf16_f32 v152, v200, v201\n"
    "v_cvt_pk_bf16_f32 v153, v202, v203\n"
    "v_cvt_pk_bf16_f32 v154, v204, v205\n"
    "v_cvt_pk_bf16_f32 v155, v206, v207\n"
    "v_cvt_pk_bf16_f32 v156, v208, v209\n"
    "v_cvt_pk_bf16_f32 v157, v210, v211\n"
    "v_cvt_pk_bf16_f32 v158, v212, v213\n"
    "v_cvt_pk_bf16_f32 v159, v214, v215\n"
    "v_mul_f32 v16, v16, v6\n"
    "v_mul_f32 v17, v17, v6\n"
    "v_mul_f32 v18, v18, v6\n"
    "v_mul_f32 v19, v19, v6\n"
    "v_mul_f32 v32, v32, v6\n"
    "v_mul_f32 v33, v33, v6\n"
    "v_mul_f32 v34, v34, v6\n"
    "v_mul_f32 v35, v35, v6\n"
    "v_mul_f32 v48, v48, v6\n"
    "v_mul_f32 v49, v49, v6\n"
    "v_mul_f32 v50, v50, v6\n"
    "v_mul_f32 v51, v51, v6\n"
    "v_mul_f32 v64, v64, v6\n"
    "v_mul_f32 v65, v65, v6\n"
    "v_mul_f32 v66, v66, v6\n"
    "v_mul_f32 v67, v67, v6\n"
    "v_mul_f32 v80, v80, v6\n"
    "v_mul_f32 v81, v81, v6\n"
    "v_mul_f32 v82, v82, v6\n"
    "v_mul_f32 v83, v83, v6\n"
    "v_mul_f32 v96, v96, v6\n"
    "v_mul_f32 v97, v97, v6\n"
    "v_mul_f32 v98, v98, v6\n"
    "v_mul_f32 v99, v99, v6\n"
    "v_mul_f32 v112, v112, v6\n"
    "v_mul_f32 v113, v113, v6\n"
    "v_mul_f32 v114, v114, v6\n"
    "v_mul_f32 v115, v115, v6\n"
    "v_mul_f32 v128, v128, v6\n"
    "v_mul_f32 v129, v129, v6\n"
    "v_mul_f32 v130, v130, v6\n"
    "v_mul_f32 v131, v131, v6\n"
    "v_accvgpr_read_b32 v2, a8\n"
    "s_nop 0\n"
    "v_mul_f32 v2, v2, v6\n"
    "s_nop 0\n"
    "v_accvgpr_write_b32 a8, v2\n"
    "v_accvgpr_read_b32 v2, a9\n"
    "s_nop 0\n"
    "v_mul_f32 v2, v2, v6\n"
    "s_nop 0\n"
    "v_accvgpr_write_b32 a9, v2\n"
    "v_accvgpr_read_b32 v2, a10\n"
    "s_nop 0\n"
    "v_mul_f32 v2, v2, v6\n"
    "s_nop 0\n"
    "v_accvgpr_write_b32 a10, v2\n"
    "v_accvgpr_read_b32 v2, a11\n"
    "s_nop 0\n"
    "v_mul_f32 v2, v2, v6\n"
    "s_nop 0\n"
    "v_accvgpr_write_b32 a11, v2\n"
    "v_max3_f32 v2, v216, v217, v218\n"
    "v_max3_f32 v2, v2, v219, v220\n"
    "v_max3_f32 v2, v2, v221, v222\n"
    "v_max3_f32 v2, v2, v223, v224\n"
    "v_max3_f32 v2, v2, v225, v226\n"
    "v_max3_f32 v2, v2, v227, v228\n"
    "v_max3_f32 v2, v2, v229, v230\n"
    "v_max_f32 v2, v2, v231\n"
    "v_mov_b32 v3, v2\n"
    "s_nop 1\n"
    "v_permlane16_swap_b32 v2, v3\n"
    "s_nop 1\n"
    "v_max_f32 v2, v2, v3\n"
    "v_mov_b32 v3, v2\n"
    "s_nop 1\n"
    "v_permlane32_swap_b32 v2, v3\n"
    "s_nop 1\n"
    "v_max_f32 v2, v2, v3\n"
    "v_max_f32 v2, v2, v255\n"
    "v_sub_f32 v7, v255, v2\n"
    "v_exp_f32 v7, v7\n"
    "v_mov_b32 v255, v2\n"
    "v_sub_f32 v216, v216, v255\n"
    "v_sub_f32 v217, v217, v255\n"
    "v_sub_f32 v218, v218, v255\n"
    "v_sub_f32 v219, v219, v255\n"
    "v_sub_f32 v220, v220, v255\n"
    "v_sub_f32 v221, v221, v255\n"
    "v_sub_f32 v222, v222, v255\n"
    "v_sub_f32 v223, v223, v255\n"
    "v_sub_f32 v224, v224, v255\n"
    "v_sub_f32 v225, v225, v255\n"
    "v_sub_f32 v226, v226, v255\n"
    "v_sub_f32 v227, v227, v255\n"
    "v_sub_f32 v228, v228, v255\n"
    "v_sub_f32 v229, v229, v255\n"
    "v_sub_f32 v230, v230, v255\n"
    "v_sub_f32 v231, v231, v255\n"
    "v_exp_f32 v216, v216\n"
    "v_exp_f32 v217, v217\n"
    "v_exp_f32 v218, v218\n"
    "v_exp_f32 v219, v219\n"
    "v_exp_f32 v220, v220\n"
    "v_exp_f32 v221, v221\n"
    "v_exp_f32 v222, v222\n"
    "v_exp_f32 v223, v223\n"
    "v_exp_f32 v224, v224\n"
    "v_exp_f32 v225, v225\n"
    "v_exp_f32 v226, v226\n"
    "v_exp_f32 v227, v227\n"
    "v_exp_f32 v228, v228\n"
    "v_exp_f32 v229, v229\n"
    "v_exp_f32 v230, v230\n"
    "v_exp_f32 v231, v231\n"
    "s_nop 0\n"
    "v_cvt_pk_bf16_f32 v160, v216, v217\n"
    "v_cvt_pk_bf16_f32 v161, v218, v219\n"
    "v_cvt_pk_bf16_f32 v162, v220, v221\n"
    "v_cvt_pk_bf16_f32 v163, v222, v223\n"
    "v_cvt_pk_bf16_f32 v164, v224, v225\n"
    "v_cvt_pk_bf16_f32 v165, v226, v227\n"
    "v_cvt_pk_bf16_f32 v166, v228, v229\n"
    "v_cvt_pk_bf16_f32 v167, v230, v231\n"
    "v_mul_f32 v20, v20, v7\n"
    "v_mul_f32 v21, v21, v7\n"
    "v_mul_f32 v22, v22, v7\n"
    "v_mul_f32 v23, v23, v7\n"
    "v_mul_f32 v36, v36, v7\n"
    "v_mul_f32 v37, v37, v7\n"
    "v_mul_f32 v38, v38, v7\n"
    "v_mul_f32 v39, v39, v7\n"
    "v_mul_f32 v52, v52, v7\n"
    "v_mul_f32 v53, v53, v7\n"
    "v_mul_f32 v54, v54, v7\n"
    "v_mul_f32 v55, v55, v7\n"
    "v_mul_f32 v68, v68, v7\n"
    "v_mul_f32 v69, v69, v7\n"
    "v_mul_f32 v70, v70, v7\n"
    "v_mul_f32 v71, v71, v7\n"
    "v_mul_f32 v84, v84, v7\n"
    "v_mul_f32 v85, v85, v7\n"
    "v_mul_f32 v86, v86, v7\n"
    "v_mul_f32 v87, v87, v7\n"
    "v_mul_f32 v100, v100, v7\n"
    "v_mul_f32 v101, v101, v7\n"
    "v_mul_f32 v102, v102, v7\n"
    "v_mul_f32 v103, v103, v7\n"
    "v_mul_f32 v116, v116, v7\n"
    "v_mul_f32 v117, v117, v7\n"
    "v_mul_f32 v118, v118, v7\n"
    "v_mul_f32 v119, v119, v7\n"
    "v_mul_f32 v132, v132, v7\n"
    "v_mul_f32 v133, v133, v7\n"
    "v_mul_f32 v134, v134, v7\n"
    "v_mul_f32 v135, v135, v7\n"
    "v_accvgpr_read_b32 v2, a12\n"
    "s_nop 0\n"
    "v_mul_f32 v2, v2, v7\n"
    "s_nop 0\n"
    "v_accvgpr_write_b32 a12, v2\n"
    "v_accvgpr_read_b32 v2, a13\n"
    "s_nop 0\n"
    "v_mul_f32 v2, v2, v7\n"
    "s_nop 0\n"
    "v_accvgpr_write_b32 a13, v2\n"
    "v_accvgpr_read_b32 v2, a14\n"
    "s_nop 0\n"
    "v_mul_f32 v2, v2, v7\n"
    "s_nop 0\n"
    "v_accvgpr_write_b32 a14, v2\n"
    "v_accvgpr_read_b32 v2, a15\n"
    "s_nop 0\n"
    "v_mul_f32 v2, v2, v7\n"
    "s_nop 0\n"
    "v_accvgpr_write_b32 a15, v2\n"
    "s_nop 1\n"
    "ds_read_b64_tr_b16 a[112:113], v236 offset:32768\n"
    "ds_read_b64_tr_b16 a[114:115], v236 offset:36864\n"
    "ds_read_b64_tr_b16 a[116:117], v236 offset:40960\n"
    "ds_read_b64_tr_b16 a[118:119], v236 offset:45056\n"
    "s_waitcnt lgkmcnt(0)\n"
    "v_mfma_f32_16x16x32_bf16 v[8:11], a[112:115], v[136:139], v[8:11]\n"
    "v_mfma_f32_16x16x32_bf16 v[8:11], a[116:119], v[140:143], v[8:11]\n"
    "v_mfma_f32_16x16x32_bf16 v[12:15], a[112:115], v[144:147], v[12:15]\n"
    "v_mfma_f32_16x16x32_bf16 v[12:15], a[116:119], v[148:151], v[12:15]\n"
    "v_mfma_f32_16x16x32_bf16 v[16:19], a[112:115], v[152:155], v[16:19]\n"
    "v_mfma_f32_16x16x32_bf16 v[16:19], a[116:119], v[156:159], v[16:19]\n"
    "v_mfma_f32_16x16x32_bf16 v[20:23], a[112:115], v[160:163], v[20:23]\n"
    "v_mfma_f32_16x16x32_bf16 v[20:23], a[116:119], v[164:167], v[20:23]\n"
    "ds_read_b64_tr_b16 a[120:121], v237 offset:32768\n"
    "ds_read_b64_tr_b16 a[122:123], v237 offset:36864\n"
    "ds_read_b64_tr_b16 a[124:125], v237 offset:40960\n"
    "ds_read_b64_tr_b16 a[126:127], v237 offset:45056\n"
    "s_waitcnt lgkmcnt(0)\n"
    "v_mfma_f32_16x16x32_bf16 v[24:27], a[120:123], v[136:139], v[24:27]\n"
    "v_mfma_f32_16x16x32_bf16 v[24:27], a[124:127], v[140:143], v[24:27]\n"
    "v_mfma_f32_16x16x32_bf16 v[28:31], a[120:123], v[144:147], v[28:31]\n"
    "v_mfma_f32_16x16x32_bf16 v[28:31], a[124:127], v[148:151], v[28:31]\n"
    "v_mfma_f32_16x16x32_bf16 v[32:35], a[120:123], v[152:155], v[32:35]\n"
    "v_mfma_f32_16x16x32_bf16 v[32:35], a[124:127], v[156:159], v[32:35]\n"
    "v_mfma_f32_16x16x32_bf16 v[36:39], a[120:123], v[160:163], v[36:39]\n"
    "v_mfma_f32_16x16x32_bf16 v[36:39], a[124:127], v[164:167], v[36:39]\n"
    "ds_read_b64_tr_b16 a[128:129], v238 offset:32768\n"
    "ds_read_b64_tr_b16 a[130:131], v238 offset:36864\n"
    "ds_read_b64_tr_b16 a[132:133], v238 offset:40960\n"
    "ds_read_b64_tr_b16 a[134:135], v238 offset:45056\n"
    "s_waitcnt lgkmcnt(0)\n"
    "v_mfma_f32_16x16x32_bf16 v[40:43], a[128:131], v[136:139], v[40:43]\n"
    "v_mfma_f32_16x16x32_bf16 v[40:43], a[132:135], v[140:143], v[40:43]\n"
    "v_mfma_f32_16x16x32_bf16 v[44:47], a[128:131], v[144:147], v[44:47]\n"
    "v_mfma_f32_16x16x32_bf16 v[44:47], a[132:135], v[148:151], v[44:47]\n"
    "v_mfma_f32_16x16x32_bf16 v[48:51], a[128:131], v[152:155], v[48:51]\n"
    "v_mfma_f32_16x16x32_bf16 v[48:51], a[132:135], v[156:159], v[48:51]\n"
    "v_mfma_f32_16x16x32_bf16 v[52:55], a[128:131], v[160:163], v[52:55]\n"
    "v_mfma_f32_16x16x32_bf16 v[52:55], a[132:135], v[164:167], v[52:55]\n"
    "ds_read_b64_tr_b16 a[136:137], v239 offset:32768\n"
    "ds_read_b64_tr_b16 a[138:139], v239 offset:36864\n"
    "ds_read_b64_tr_b16 a[140:141], v239 offset:40960\n"
    "ds_read_b64_tr_b16 a[142:143], v239 offset:45056\n"
    "s_waitcnt lgkmcnt(0)\n"
    "v_mfma_f32_16x16x32_bf16 v[56:59], a[136:139], v[136:139], v[56:59]\n"
    "v_mfma_f32_16x16x32_bf16 v[56:59], a[140:143], v[140:143], v[56:59]\n"
    "v_mfma_f32_16x16x32_bf16 v[60:63], a[136:139], v[144:147], v[60:63]\n"
    "v_mfma_f32_16x16x32_bf16 v[60:63], a[140:143], v[148:151], v[60:63]\n"
    "v_mfma_f32_16x16x32_bf16 v[64:67], a[136:139], v[152:155], v[64:67]\n"
    "v_mfma_f32_16x16x32_bf16 v[64:67], a[140:143], v[156:159], v[64:67]\n"
    "v_mfma_f32_16x16x32_bf16 v[68:71], a[136:139], v[160:163], v[68:71]\n"
    "v_mfma_f32_16x16x32_bf16 v[68:71], a[140:143], v[164:167], v[68:71]\n"
    "ds_read_b64_tr_b16 a[112:113], v240 offset:32768\n"
    "ds_read_b64_tr_b16 a[114:115], v240 offset:36864\n"
    "ds_read_b64_tr_b16 a[116:117], v240 offset:40960\n"
    "ds_read_b64_tr_b16 a[118:119], v240 offset:45056\n"
    "s_waitcnt lgkmcnt(0)\n"
    "v_mfma_f32_16x16x32_bf16 v[72:75], a[112:115], v[136:139], v[72:75]\n"
    "v_mfma_f32_16x16x32_bf16 v[72:75], a[116:119], v[140:143], v[72:75]\n"
    "v_mfma_f32_16x16x32_bf16 v[76:79], a[112:115], v[144:147], v[76:79]\n"
    "v_mfma_f32_16x16x32_bf16 v[76:79], a[116:119], v[148:151], v[76:79]\n"
    "v_mfma_f32_16x16x32_bf16 v[80:83], a[112:115], v[152:155], v[80:83]\n"
    "v_mfma_f32_16x16x32_bf16 v[80:83], a[116:119], v[156:159], v[80:83]\n"
    "v_mfma_f32_16x16x32_bf16 v[84:87], a[112:115], v[160:163], v[84:87]\n"
    "v_mfma_f32_16x16x32_bf16 v[84:87], a[116:119], v[164:167], v[84:87]\n"
    "ds_read_b64_tr_b16 a[120:121], v241 offset:32768\n"
    "ds_read_b64_tr_b16 a[122:123], v241 offset:36864\n"
    "ds_read_b64_tr_b16 a[124:125], v241 offset:40960\n"
    "ds_read_b64_tr_b16 a[126:127], v241 offset:45056\n"
    "s_waitcnt lgkmcnt(0)\n"
    "v_mfma_f32_16x16x32_bf16 v[88:91], a[120:123], v[136:139], v[88:91]\n"
    "v_mfma_f32_16x16x32_bf16 v[88:91], a[124:127], v[140:143], v[88:91]\n"
    "v_mfma_f32_16x16x32_bf16 v[92:95], a[120:123], v[144:147], v[92:95]\n"
    "v_mfma_f32_16x16x32_bf16 v[92:95], a[124:127], v[148:151], v[92:95]\n"
    "v_mfma_f32_16x16x32_bf16 v[96:99], a[120:123], v[152:155], v[96:99]\n"
    "v_mfma_f32_16x16x32_bf16 v[96:99], a[124:127], v[156:159], v[96:99]\n"
    "v_mfma_f32_16x16x32_bf16 v[100:103], a[120:123], v[160:163], v[100:103]\n"
    "v_mfma_f32_16x16x32_bf16 v[100:103], a[124:127], v[164:167], v[100:103]\n"
    "ds_read_b64_tr_b16 a[128:129], v242 offset:32768\n"
    "ds_read_b64_tr_b16 a[130:131], v242 offset:36864\n"
    "ds_read_b64_tr_b16 a[132:133], v242 offset:40960\n"
    "ds_read_b64_tr_b16 a[134:135], v242 offset:45056\n"
    "s_waitcnt lgkmcnt(0)\n"
    "v_mfma_f32_16x16x32_bf16 v[104:107], a[128:131], v[136:139], v[104:107]\n"
    "v_mfma_f32_16x16x32_bf16 v[104:107], a[132:135], v[140:143], v[104:107]\n"
    "v_mfma_f32_16x16x32_bf16 v[108:111], a[128:131], v[144:147], v[108:111]\n"
    "v_mfma_f32_16x16x32_bf16 v[108:111], a[132:135], v[148:151], v[108:111]\n"
    "v_mfma_f32_16x16x32_bf16 v[112:115], a[128:131], v[152:155], v[112:115]\n"
    "v_mfma_f32_16x16x32_bf16 v[112:115], a[132:135], v[156:159], v[112:115]\n"
    "v_mfma_f32_16x16x32_bf16 v[116:119], a[128:131], v[160:163], v[116:119]\n"
    "v_mfma_f32_16x16x32_bf16 v[116:119], a[132:135], v[164:167], v[116:119]\n"
    "ds_read_b64_tr_b16 a[136:137], v243 offset:32768\n"
    "ds_read_b64_tr_b16 a[138:139], v243 offset:36864\n"
    "ds_read_b64_tr_b16 a[140:141], v243 offset:40960\n"
    "ds_read_b64_tr_b16 a[142:143], v243 offset:45056\n"
    "s_waitcnt lgkmcnt(0)\n"
    "v_mfma_f32_16x16x32_bf16 v[120:123], a[136:139], v[136:139], v[120:123]\n"
    "v_mfma_f32_16x16x32_bf16 v[120:123], a[140:143], v[140:143], v[120:123]\n"
    "v_mfma_f32_16x16x32_bf16 v[124:127], a[136:139], v[144:147], v[124:127]\n"
    "v_mfma_f32_16x16x32_bf16 v[124:127], a[140:143], v[148:151], v[124:127]\n"
    "v_mfma_f32_16x16x32_bf16 v[128:131], a[136:139], v[152:155], v[128:131]\n"
    "v_mfma_f32_16x16x32_bf16 v[128:131], a[140:143], v[156:159], v[128:131]\n"
    "v_mfma_f32_16x16x32_bf16 v[132:135], a[136:139], v[160:163], v[132:135]\n"
    "v_mfma_f32_16x16x32_bf16 v[132:135], a[140:143], v[164:167], v[132:135]\n"
    "v_mfma_f32_16x16x32_bf16 a[0:3], a[144:147], v[136:139], a[0:3]\n"
    "v_mfma_f32_16x16x32_bf16 a[0:3], a[144:147], v[140:143], a[0:3]\n"
    "v_mfma_f32_16x16x32_bf16 a[4:7], a[144:147], v[144:147], a[4:7]\n"
    "v_mfma_f32_16x16x32_bf16 a[4:7], a[144:147], v[148:151], a[4:7]\n"
    "v_mfma_f32_16x16x32_bf16 a[8:11], a[144:147], v[152:155], a[8:11]\n"
    "v_mfma_f32_16x16x32_bf16 a[8:11], a[144:147], v[156:159], a[8:11]\n"
    "v_mfma_f32_16x16x32_bf16 a[12:15], a[144:147], v[160:163], a[12:15]\n"
    "v_mfma_f32_16x16x32_bf16 a[12:15], a[144:147], v[164:167], a[12:15]\n"
    "s_nop 7\n"
    "s_barrier\n"
    "s_sub_u32 s71, s71, 1\n"
    "s_cmp_lg_u32 s71, 0\n"
    "s_cbranch_scc1 L_attn_fb_loop_%=\n"
    "s_nop 7\n"
    "s_branch L_attn_fb_ret_%=\n"
    "L_attn_fb_ret_%=:\n"
    "s_nop 7\n"
    "v_accvgpr_read_b32 v240, a0\n"
    "v_accvgpr_read_b32 v241, a4\n"
    "v_accvgpr_read_b32 v242, a8\n"
    "v_accvgpr_read_b32 v243, a12\n"
    "s_nop 1\n"
    "s_mov_b32 s96, 0x4f800000\n"
    "v_cmp_nge_f32 vcc, s96, v240\n"
    "v_cmp_nge_f32 s[98:99], s96, v241\n"
    "s_nop 1\n"
    "s_or_b64 vcc, vcc, s[98:99]\n"
    "v_cmp_nge_f32 s[98:99], s96, v242\n"
    "s_nop 1\n"
    "s_or_b64 vcc, vcc, s[98:99]\n"
    "v_cmp_nge_f32 s[98:99], s96, v243\n"
    "s_nop 1\n"
    "s_or_b64 vcc, vcc, s[98:99]\n"
    "s_cmp_lg_u64 vcc, 0\n"
    "s_cselect_b32 s96, 1, 0\n"
    "v_mov_b32 v4, s96\n"
    "v_mov_b32 v5, s85\n"
    "v_add_u32 v5, 162816, v5\n"
    "ds_or_b32 v5, v4\n"
    "v_rcp_f32 v240, v240\n"
    "v_rcp_f32 v241, v241\n"
    "v_rcp_f32 v242, v242\n"
    "v_rcp_f32 v243, v243\n"
    "s_nop 0\n"
    "v_mul_f32 v8, v8, v240\n"
    "v_mul_f32 v9, v9, v240\n"
    "v_mul_f32 v10, v10, v240\n"
    "v_mul_f32 v11, v11, v240\n"
    "v_cvt_pk_bf16_f32 v136, v8, v9\n"
    "v_cvt_pk_bf16_f32 v137, v10, v11\n"
    "v_mul_f32 v24, v24, v240\n"
    "v_mul_f32 v25, v25, v240\n"
    "v_mul_f32 v26, v26, v240\n"
    "v_mul_f32 v27, v27, v240\n"
    "v_cvt_pk_bf16_f32 v138, v24, v25\n"
    "v_cvt_pk_bf16_f32 v139, v26, v27\n"
    "v_mul_f32 v40, v40, v240\n"
    "v_mul_f32 v41, v41, v240\n"
    "v_mul_f32 v42, v42, v240\n"
    "v_mul_f32 v43, v43, v240\n"
    "v_cvt_pk_bf16_f32 v140, v40, v41\n"
    "v_cvt_pk_bf16_f32 v141, v42, v43\n"
    "v_mul_f32 v56, v56, v240\n"
    "v_mul_f32 v57, v57, v240\n"
    "v_mul_f32 v58, v58, v240\n"
    "v_mul_f32 v59, v59, v240\n"
    "v_cvt_pk_bf16_f32 v142, v56, v57\n"
    "v_cvt_pk_bf16_f32 v143, v58, v59\n"
    "v_mul_f32 v72, v72, v240\n"
    "v_mul_f32 v73, v73, v240\n"
    "v_mul_f32 v74, v74, v240\n"
    "v_mul_f32 v75, v75, v240\n"
    "v_cvt_pk_bf16_f32 v144, v72, v73\n"
    "v_cvt_pk_bf16_f32 v145, v74, v75\n"
    "v_mul_f32 v88, v88, v240\n"
    "v_mul_f32 v89, v89, v240\n"
    "v_mul_f32 v90, v90, v240\n"
    "v_mul_f32 v91, v91, v240\n"
    "v_cvt_pk_bf16_f32 v146, v88, v89\n"
    "v_cvt_pk_bf16_f32 v147, v90, v91\n"
    "v_mul_f32 v104, v104, v240\n"
    "v_mul_f32 v105, v105, v240\n"
    "v_mul_f32 v106, v106, v240\n"
    "v_mul_f32 v107, v107, v240\n"
    "v_cvt_pk_bf16_f32 v148, v104, v105\n"
    "v_cvt_pk_bf16_f32 v149, v106, v107\n"
    "v_mul_f32 v120, v120, v240\n"
    "v_mul_f32 v121, v121, v240\n"
    "v_mul_f32 v122, v122, v240\n"
    "v_mul_f32 v123, v123, v240\n"
    "v_cvt_pk_bf16_f32 v150, v120, v121\n"
    "v_cvt_pk_bf16_f32 v151, v122, v123\n"
    "v_mul_f32 v12, v12, v241\n"
    "v_mul_f32 v13, v13, v241\n"
    "v_mul_f32 v14, v14, v241\n"
    "v_mul_f32 v15, v15, v241\n"
    "v_cvt_pk_bf16_f32 v152, v12, v13\n"
    "v_cvt_pk_bf16_f32 v153, v14, v15\n"
    "v_mul_f32 v28, v28, v241\n"
    "v_mul_f32 v29, v29, v241\n"
    "v_mul_f32 v30, v30, v241\n"
    "v_mul_f32 v31, v31, v241\n"
    "v_cvt_pk_bf16_f32 v154, v28, v29\n"
    "v_cvt_pk_bf16_f32 v155, v30, v31\n"
    "v_mul_f32 v44, v44, v241\n"
    "v_mul_f32 v45, v45, v241\n"
    "v_mul_f32 v46, v46, v241\n"
    "v_mul_f32 v47, v47, v241\n"
    "v_cvt_pk_bf16_f32 v156, v44, v45\n"
    "v_cvt_pk_bf16_f32 v157, v46, v47\n"
    "v_mul_f32 v60, v60, v241\n"
    "v_mul_f32 v61, v61, v241\n"
    "v_mul_f32 v62, v62, v241\n"
    "v_mul_f32 v63, v63, v241\n"
    "v_cvt_pk_bf16_f32 v158, v60, v61\n"
    "v_cvt_pk_bf16_f32 v159, v62, v63\n"
    "v_mul_f32 v76, v76, v241\n"
    "v_mul_f32 v77, v77, v241\n"
    "v_mul_f32 v78, v78, v241\n"
    "v_mul_f32 v79, v79, v241\n"
    "v_cvt_pk_bf16_f32 v160, v76, v77\n"
    "v_cvt_pk_bf16_f32 v161, v78, v79\n"
    "v_mul_f32 v92, v92, v241\n"
    "v_mul_f32 v93, v93, v241\n"
    "v_mul_f32 v94, v94, v241\n"
    "v_mul_f32 v95, v95, v241\n"
    "v_cvt_pk_bf16_f32 v162, v92, v93\n"
    "v_cvt_pk_bf16_f32 v163, v94, v95\n"
    "v_mul_f32 v108, v108, v241\n"
    "v_mul_f32 v109, v109, v241\n"
    "v_mul_f32 v110, v110, v241\n"
    "v_mul_f32 v111, v111, v241\n"
    "v_cvt_pk_bf16_f32 v164, v108, v109\n"
    "v_cvt_pk_bf16_f32 v165, v110, v111\n"
    "v_mul_f32 v124, v124, v241\n"
    "v_mul_f32 v125, v125, v241\n"
    "v_mul_f32 v126, v126, v241\n"
    "v_mul_f32 v127, v127, v241\n"
    "v_cvt_pk_bf16_f32 v166, v124, v125\n"
    "v_cvt_pk_bf16_f32 v167, v126, v127\n"
    "v_mul_f32 v16, v16, v242\n"
    "v_mul_f32 v17, v17, v242\n"
    "v_mul_f32 v18, v18, v242\n"
    "v_mul_f32 v19, v19, v242\n"
    "v_cvt_pk_bf16_f32 v168, v16, v17\n"
    "v_cvt_pk_bf16_f32 v169, v18, v19\n"
    "v_mul_f32 v32, v32, v242\n"
    "v_mul_f32 v33, v33, v242\n"
    "v_mul_f32 v34, v34, v242\n"
    "v_mul_f32 v35, v35, v242\n"
    "v_cvt_pk_bf16_f32 v170, v32, v33\n"
    "v_cvt_pk_bf16_f32 v171, v34, v35\n"
    "v_mul_f32 v48, v48, v242\n"
    "v_mul_f32 v49, v49, v242\n"
    "v_mul_f32 v50, v50, v242\n"
    "v_mul_f32 v51, v51, v242\n"
    "v_cvt_pk_bf16_f32 v172, v48, v49\n"
    "v_cvt_pk_bf16_f32 v173, v50, v51\n"
    "v_mul_f32 v64, v64, v242\n"
    "v_mul_f32 v65, v65, v242\n"
    "v_mul_f32 v66, v66, v242\n"
    "v_mul_f32 v67, v67, v242\n"
    "v_cvt_pk_bf16_f32 v174, v64, v65\n"
    "v_cvt_pk_bf16_f32 v175, v66, v67\n"
    "v_mul_f32 v80, v80, v242\n"
    "v_mul_f32 v81, v81, v242\n"
    "v_mul_f32 v82, v82, v242\n"
    "v_mul_f32 v83, v83, v242\n"
    "v_cvt_pk_bf16_f32 v176, v80, v81\n"
    "v_cvt_pk_bf16_f32 v177, v82, v83\n"
    "v_mul_f32 v96, v96, v242\n"
    "v_mul_f32 v97, v97, v242\n"
    "v_mul_f32 v98, v98, v242\n"
    "v_mul_f32 v99, v99, v242\n"
    "v_cvt_pk_bf16_f32 v178, v96, v97\n"
    "v_cvt_pk_bf16_f32 v179, v98, v99\n"
    "v_mul_f32 v112, v112, v242\n"
    "v_mul_f32 v113, v113, v242\n"
    "v_mul_f32 v114, v114, v242\n"
    "v_mul_f32 v115, v115, v242\n"
    "v_cvt_pk_bf16_f32 v180, v112, v113\n"
    "v_cvt_pk_bf16_f32 v181, v114, v115\n"
    "v_mul_f32 v128, v128, v242\n"
    "v_mul_f32 v129, v129, v242\n"
    "v_mul_f32 v130, v130, v242\n"
    "v_mul_f32 v131, v131, v242\n"
    "v_cvt_pk_bf16_f32 v182, v128, v129\n"
    "v_cvt_pk_bf16_f32 v183, v130, v131\n"
    "v_mul_f32 v20, v20, v243\n"
    "v_mul_f32 v21, v21, v243\n"
    "v_mul_f32 v22, v22, v243\n"
    "v_mul_f32 v23, v23, v243\n"
    "v_cvt_pk_bf16_f32 v184, v20, v21\n"
    "v_cvt_pk_bf16_f32 v185, v22, v23\n"
    "v_mul_f32 v36, v36, v243\n"
    "v_mul_f32 v37, v37, v243\n"
    "v_mul_f32 v38, v38, v243\n"
    "v_mul_f32 v39, v39, v243\n"
    "v_cvt_pk_bf16_f32 v186, v36, v37\n"
    "v_cvt_pk_bf16_f32 v187, v38, v39\n"
    "v_mul_f32 v52, v52, v243\n"
    "v_mul_f32 v53, v53, v243\n"
    "v_mul_f32 v54, v54, v243\n"
    "v_mul_f32 v55, v55, v243\n"
    "v_cvt_pk_bf16_f32 v188, v52, v53\n"
    "v_cvt_pk_bf16_f32 v189, v54, v55\n"
    "v_mul_f32 v68, v68, v243\n"
    "v_mul_f32 v69, v69, v243\n"
    "v_mul_f32 v70, v70, v243\n"
    "v_mul_f32 v71, v71, v243\n"
    "v_cvt_pk_bf16_f32 v190, v68, v69\n"
    "v_cvt_pk_bf16_f32 v191, v70, v71\n"
    "v_mul_f32 v84, v84, v243\n"
    "v_mul_f32 v85, v85, v243\n"
    "v_mul_f32 v86, v86, v243\n"
    "v_mul_f32 v87, v87, v243\n"
    "v_cvt_pk_bf16_f32 v192, v84, v85\n"
    "v_cvt_pk_bf16_f32 v193, v86, v87\n"
    "v_mul_f32 v100, v100, v243\n"
    "v_mul_f32 v101, v101, v243\n"
    "v_mul_f32 v102, v102, v243\n"
    "v_mul_f32 v103, v103, v243\n"
    "v_cvt_pk_bf16_f32 v194, v100, v101\n"
    "v_cvt_pk_bf16_f32 v195, v102, v103\n"
    "v_mul_f32 v116, v116, v243\n"
    "v_mul_f32 v117, v117, v243\n"
    "v_mul_f32 v118, v118, v243\n"
    "v_mul_f32 v119, v119, v243\n"
    "v_cvt_pk_bf16_f32 v196, v116, v117\n"
    "v_cvt_pk_bf16_f32 v197, v118, v119\n"
    "v_mul_f32 v132, v132, v243\n"
    "v_mul_f32 v133, v133, v243\n"
    "v_mul_f32 v134, v134, v243\n"
    "v_mul_f32 v135, v135, v243\n"
    "v_cvt_pk_bf16_f32 v198, v132, v133\n"
    "v_cvt_pk_bf16_f32 v199, v134, v135\n"
    "s_branch L_oepi_done_%=\n"
    "L_btop_%=:\n"
    "s_cmp_eq_u32 s64, 14\n"
    "s_cbranch_scc0 L_bt11_%=\n"
    "s_cmp_eq_u32 s80, 0\n"
    "s_cbranch_scc1 L_bN_%=\n"
    "s_branch L_bS_%=\n"
    "L_bt11_%=:\n"
    "s_cmp_eq_u32 s64, 11\n"
    "s_cbranch_scc0 L_bt8_%=\n"
    "s_cmp_eq_u32 s39, 0\n"
    "s_cbranch_scc0 L_bN_%=\n"
    "s_lshr_b32 s96, s53, 1\n"
    "s_lshl_b32 s96, s96, 5\n"
    "s_lshl_b32 s97, s52, 3\n"
    "s_add_u32 s96, s96, s97\n"
    "s_add_u32 s96, s96, 0\n"
    "s_lshl_b32 s96, s96, 2\n"
    "s_add_u32 s96, s96, 4096\n"
    "v_and_b32 v4, 3, v1\n"
    "v_lshlrev_b32 v4, 2, v4\n"
    "v_add_u32 v4, s96, v4\n"
    "global_load_dword v6, v4, s[6:7] sc1\n"
    "s_branch L_bN_%=\n"
    "L_bt8_%=:\n"
    "s_cmp_eq_u32 s64, 8\n"
    "s_cbranch_scc0 L_bt5_%=\n"
    "s_nop 7\n"
    "v_cvt_pk_bf16_f32 v136, v64, v65\n"
    "v_cvt_pk_bf16_f32 v137, v66, v67\n"
    "v_cvt_pk_bf16_f32 v138, v68, v69\n"
    "v_cvt_pk_bf16_f32 v139, v70, v71\n"
    "v_cvt_pk_bf16_f32 v140, v72, v73\n"
    "v_cvt_pk_bf16_f32 v141, v74, v75\n"
    "v_cvt_pk_bf16_f32 v142, v76, v77\n"
    "v_cvt_pk_bf16_f32 v143, v78, v79\n"
    "v_cvt_pk_bf16_f32 v144, v80, v81\n"
    "v_cvt_pk_bf16_f32 v145, v82, v83\n"
    "v_cvt_pk_bf16_f32 v146, v84, v85\n"
    "v_cvt_pk_bf16_f32 v147, v86, v87\n"
    "v_cvt_pk_bf16_f32 v148, v88, v89\n"
    "v_cvt_pk_bf16_f32 v149, v90, v91\n"
    "v_cvt_pk_bf16_f32 v150, v92, v93\n"
    "v_cvt_pk_bf16_f32 v151, v94, v95\n"
    "v_cvt_pk_bf16_f32 v152, v96, v97\n"
    "v_cvt_pk_bf16_f32 v153, v98, v99\n"
    "v_cvt_pk_bf16_f32 v154, v100, v101\n"
    "v_cvt_pk_bf16_f32 v155, v102, v103\n"
    "v_cvt_pk_bf16_f32 v156, v104, v105\n"
    "v_cvt_pk_bf16_f32 v157, v106, v107\n"
    "v_cvt_pk_bf16_f32 v158, v108, v109\n"
    "v_cvt_pk_bf16_f32 v159, v110, v111\n"
    "v_cvt_pk_bf16_f32 v160, v112, v113\n"
    "v_cvt_pk_bf16_f32 v161, v114, v115\n"
    "v_cvt_pk_bf16_f32 v162, v116, v117\n"
    "v_cvt_pk_bf16_f32 v163, v118, v119\n"
    "v_cvt_pk_bf16_f32 v164, v120, v121\n"
    "v_cvt_pk_bf16_f32 v165, v122, v123\n"
    "v_cvt_pk_bf16_f32 v166, v124, v125\n"
    "v_cvt_pk_bf16_f32 v167, v126, v127\n"
    "v_cvt_pk_bf16_f32 v168, v128, v129\n"
    "v_cvt_pk_bf16_f32 v169, v130, v131\n"
    "v_cvt_pk_bf16_f32 v170, v132, v133\n"
    "v_cvt_pk_bf16_f32 v171, v134, v135\n"
    "v_mov_b32 v2, 0\n"
    "v_mov_b32 v3, 0\n"
    "v_mov_b32 v4, 0\n"
    "v_mov_b32 v5, 0\n"
    "v_mov_b32 v128, 0\n"
    "v_mov_b32 v129, 0\n"
    "v_mov_b32 v130, 0\n"
    "v_mov_b32 v131, 0\n"
    "v_mov_b32 v132, 0\n"
    "v_mov_b32 v133, 0\n"
    "v_mov_b32 v134, 0\n"
    "v_mov_b32 v135, 0\n"
    "v_mfma_f32_32x32x16_bf16 v[64:79], v[2:5], v[2:5], 0\n"
    "v_mfma_f32_32x32x16_bf16 v[80:95], v[2:5], v[2:5], 0\n"
    "v_mfma_f32_32x32x16_bf16 v[96:111], v[2:5], v[2:5], 0\n"
    "v_mfma_f32_32x32x16_bf16 v[112:127], v[2:5], v[2:5], 0\n"
    "s_lshl_b32 s96, s70, 2\n"
    "s_add_u32 s96, s96, s52\n"
    "s_lshl_b32 s96, s96, 3\n"
    "s_add_u32 s96, s96, s39\n"
    "s_mul_i32 s96, s96, 9216\n"
    "s_add_u32 s56, s24, s96\n"
    "s_addc_u32 s57, s25, 0\n"
    "s_and_b32 s57, s57, 0xffff\n"
    "s_cmp_eq_u32 s39, s52\n"
    "s_cselect_b32 s58, 0, 9216\n"
    "s_cmp_eq_u32 s39, s52\n"
    "s_cbranch_scc0 L_owg_%=\n"
    "v_add_u32 v6, s85, v252\n"
    "v_add_u32 v6, 110592, v6\n"
    "ds_write_b128 v6, v[136:139] offset:0\n"
    "ds_write_b128 v6, v[140:143] offset:1024\n"
    "ds_write_b128 v6, v[144:147] offset:2048\n"
    "ds_write_b128 v6, v[148:151] offset:3072\n"
    "ds_write_b128 v6, v[152:155] offset:4096\n"
    "ds_write_b128 v6, v[156:159] offset:5120\n"
    "ds_write_b128 v6, v[160:163] offset:6144\n"
    "ds_write_b128 v6, v[164:167] offset:7168\n"
    "ds_write_b128 v6, v[168:171] offset:8192\n"
    "L_owg_%=:\n"
    "s_mov_b32 s73, 0\n"
    "s_mov_b32 s74, 4096\n"
    "s_mov_b32 s75, 8192\n"
    "s_mov_b32 s76, 12288\n"
    "s_cmp_eq_u32 s39, 0\n"
    "s_cbranch_scc0 L_bt8x_%=\n"
    "s_lshr_b32 s96, s53, 1\n"
    "s_lshl_b32 s96, s96, 5\n"
    "s_lshl_b32 s97, s52, 3\n"
    "s_add_u32 s96, s96, s97\n"
    "s_add_u32 s96, s96, 4\n"
    "s_lshl_b32 s96, s96, 2\n"
    "s_add_u32 s96, s96, 4096\n"
    "v_and_b32 v4, 3, v1\n"
    "v_lshlrev_b32 v4, 2, v4\n"
    "v_add_u32 v4, s96, v4\n"
    "global_load_dword v6, v4, s[6:7] sc1\n"
    "L_bt8x_%=:\n"
    "s_nop 7\n"
    "s_branch L_bS_%=\n"
    "L_bt5_%=:\n"
    "s_cmp_eq_u32 s64, 5\n"
    "s_cbranch_scc1 L_bN_%=\n"
    "s_cmp_eq_u32 s39, 0\n"
    "s_cbranch_scc0 L_bF_%=\n"
    "v_cmp_ne_u32 vcc, s3, v6\n"
    "s_nop 1\n"
    "s_cmp_eq_u64 vcc, 0\n"
    "s_cbranch_scc1 L_bF_%=\n"
    "s_mov_b32 s97, 32768\n"
    "L_poll_g_%=:\n"
    "global_load_dword v6, v4, s[6:7] sc1\n"
    "s_waitcnt vmcnt(0)\n"
    "v_cmp_ne_u32 vcc, s3, v6\n"
    "s_nop 1\n"
    "s_cmp_eq_u64 vcc, 0\n"
    "s_cbranch_scc1 L_polld_g_%=\n"
    "s_sub_u32 s97, s97, 1\n"
    "s_cmp_eq_u32 s97, 0\n"
    "s_cbranch_scc1 L_pollt_g_%=\n"
    "s_sleep 1\n"
    "s_branch L_poll_g_%=\n"
    "L_pollt_g_%=:\n"
    "L_polld_g_%=:\n"
    "s_branch L_bF_%=\n"
    "L_bb0_%=:\n"
    "s_cmp_eq_u32 s64, 5\n"
    "s_cbranch_scc1 L_bb0g_%=\n"
    "s_mov_b32 s77, 5\n"
    "s_cmp_eq_u32 s39, 0\n"
    "s_cbranch_scc0 L_bb0_ret_%=\n"
    "s_lshl_b32 s96, s88, 5\n"
    "s_add_u32 s96, s96, s87\n"
    "s_lshl_b32 s96, s96, 2\n"
    "s_add_u32 s96, s96, 4096\n"
    "v_mov_b32 v8, s96\n"
    "v_mov_b32 v9, s3\n"
    "v_and_b32 v10, 63, v1\n"
    "v_cmp_eq_u32 vcc, 0, v10\n"
    "s_and_saveexec_b64 s[98:99], vcc\n"
    "global_store_dword v8, v9, s[6:7] sc1\n"
    "s_mov_b64 exec, s[98:99]\n"
    "s_branch L_bb0_ret_%=\n"
    "L_bb0g_%=:\n"
    "s_cmp_eq_u32 s39, 0\n"
    "s_cbranch_scc0 L_bb0_ret_%=\n"
    "s_lshl_b32 s96, s70, 2\n"
    "s_add_u32 s96, s96, s52\n"
    "s_lshl_b32 s96, s96, 2\n"
    "s_add_u32 s96, s96, 6144\n"
    "v_mov_b32 v8, s96\n"
    "v_mov_b32 v9, s3\n"
    "v_and_b32 v10, 63, v1\n"
    "v_cmp_eq_u32 vcc, 0, v10\n"
    "s_and_saveexec_b64 s[98:99], vcc\n"
    "global_store_dword v8, v9, s[6:7] sc1\n"
    "s_mov_b64 exec, s[98:99]\n"
    "s_branch L_bb0_ret_%=\n"
    "L_bb1_%=:\n"
    "s_cmp_eq_u32 s39, 0\n"
    "s_cbranch_scc0 L_bb1_ret_%=\n"
    "s_lshl_b32 s96, s70, 4\n"
    "s_add_u32 s96, s96, 6144\n"
    "v_and_b32 v4, 3, v1\n"
    "v_lshlrev_b32 v4, 2, v4\n"
    "v_add_u32 v4, s96, v4\n"
    "global_load_dword v6, v4, s[6:7] sc1\n"
    "s_branch L_bb1_ret_%=\n"
    "L_bswS_%=:\n"
    "s_cmp_eq_u32 s39, 0\n"
    "s_cbranch_scc0 L_bswS_bar_%=\n"
    "v_cmp_ne_u32 vcc, s3, v6\n"
    "s_nop 1\n"
    "s_cmp_eq_u64 vcc, 0\n"
    "s_cbranch_scc1 L_bswS_bar_%=\n"
    "s_mov_b32 s97, 32768\n"
    "L_poll_oS_%=:\n"
    "global_load_dword v6, v4, s[6:7] sc1\n"
    "s_waitcnt vmcnt(0)\n"
    "v_cmp_ne_u32 vcc, s3, v6\n"
    "s_nop 1\n"
    "s_cmp_eq_u64 vcc, 0\n"
    "s_cbranch_scc1 L_polld_oS_%=\n"
    "s_sub_u32 s97, s97, 1\n"
    "s_cmp_eq_u32 s97, 0\n"
    "s_cbranch_scc1 L_pollt_oS_%=\n"
    "s_sleep 1\n"
    "s_branch L_poll_oS_%=\n"
    "L_pollt_oS_%=:\n"
    "L_polld_oS_%=:\n"
    "L_bswS_bar_%=:\n"
    "s_barrier\n"
    "s_mov_b32 s79, 99\n"
    "s_branch L_bswS_ret_%=\n"
    "L_bswN_%=:\n"
    "s_cmp_eq_u32 s39, 0\n"
    "s_cbranch_scc0 L_bswN_bar_%=\n"
    "v_cmp_ne_u32 vcc, s3, v6\n"
    "s_nop 1\n"
    "s_cmp_eq_u64 vcc, 0\n"
    "s_cbranch_scc1 L_bswN_bar_%=\n"
    "s_mov_b32 s97, 32768\n"
    "L_poll_oN_%=:\n"
    "global_load_dword v6, v4, s[6:7] sc1\n"
    "s_waitcnt vmcnt(0)\n"
    "v_cmp_ne_u32 vcc, s3, v6\n"
    "s_nop 1\n"
    "s_cmp_eq_u64 vcc, 0\n"
    "s_cbranch_scc1 L_polld_oN_%=\n"
    "s_sub_u32 s97, s97, 1\n"
    "s_cmp_eq_u32 s97, 0\n"
    "s_cbranch_scc1 L_pollt_oN_%=\n"
    "s_sleep 1\n"
    "s_branch L_poll_oN_%=\n"
    "L_pollt_oN_%=:\n"
    "L_polld_oN_%=:\n"
    "L_bswN_bar_%=:\n"
    "s_barrier\n"
    "s_mov_b32 s79, 8\n"
    "s_mul_i32 s65, s52, 1179648\n"
    "s_add_u32 s65, s65, 5308416\n"
    "s_mul_i32 s66, s54, 9216\n"
    "s_add_u32 s65, s65, s66\n"
    "s_add_u32 s40, s12, s65\n"
    "s_addc_u32 s41, s13, 0\n"
    "s_and_b32 s41, s41, 0xffff\n"
    "s_sub_u32 s42, 1179648, s66\n"
    "s_mov_b32 s43, 0x20000\n"
    "s_mul_i32 s48, s39, 36864\n"
    "s_mov_b32 s44, s36\n"
    "s_and_b32 s45, s37, 0xffff\n"
    "s_mov_b32 s46, 16777216\n"
    "s_mov_b32 s47, 0x20000\n"
    "s_lshl_b32 s65, s53, 3\n"
    "s_lshl_b32 s66, s39, 1\n"
    "s_add_u32 s65, s65, s66\n"
    "s_mul_i32 s65, s65, 128\n"
    "s_mul_i32 s66, s52, 32\n"
    "s_add_u32 s65, s65, s66\n"
    "s_lshl_b32 s49, s65, 10\n"
    "s_add_u32 s50, s49, 131072\n"
    "s_branch L_bswN_ret_%=\n"
    "L_code_end_%=:\n"
    : : "v"(threadIdx.x), "s"(__builtin_amdgcn_kernarg_segment_ptr()), "s"(blockIdx.x), "s"(ldsb)
    : "memory","vcc","scc","v1","v2","v3","v4","v5","v6","v7","v8","v9","v10","v11","v12","v13","v14","v15","v16","v17","v18","v19","v20","v21","v22","v23",
      "v24","v25","v26","v27","v28","v29","v30","v31","v32","v33","v34","v35","v36","v37","v38","v39","v40","v41","v42","v43","v44","v45","v46","v47","v48",
      "v49","v50","v51","v52","v53","v54","v55","v56","v57","v58","v59","v60","v61","v62","v63","v64","v65","v66","v67","v68","v69","v70","v71","v72","v73",
      "v74","v75","v76","v77","v78","v79","v80","v81","v82","v83","v84","v85","v86","v87","v88","v89","v90","v91","v92","v93","v94","v95","v96","v97","v98",
      "v99","v100","v101","v102","v103","v104","v105","v106","v107","v108","v109","v110","v111","v112","v113","v114","v115","v116","v117","v118","v119",
      "v120","v121","v122","v123","v124","v125","v126","v127","v128","v129","v130","v131","v132","v133","v134","v135","v136","v137","v138","v139","v140",
      "v141","v142","v143","v144","v145","v146","v147","v148","v149","v150","v151","v152","v153","v154","v155","v156","v157","v158","v159","v160","v161",
      "v162","v163","v164","v165","v166","v167","v168","v169","v170","v171","v172","v173","v174","v175","v176","v177","v178","v179","v180","v181","v182",
      "v183","v184","v185","v186","v187","v188","v189","v190","v191","v192","v193","v194","v195","v196","v197","v198","v199","v200","v201","v202","v203",
      "v204","v205","v206","v207","v208","v209","v210","v211","v212","v213","v214","v215","v216","v217","v218","v219","v220","v221","v222","v223","v224",
      "v225","v226","v227","v228","v229","v230","v231","v232","v233","v234","v235","v236","v237","v238","v239","v240","v241","v242","v243","v244","v245",
      "v246","v247","v248","v249","v250","v251","v252","v253","v254","v255","a0","a1","a2","a3","a4","a5","a6","a7","a8","a9","a10","a11","a12","a13","a14",
      "a15","a16","a17","a18","a19","a20","a21","a22","a23","a24","a25","a26","a27","a28","a29","a30","a31","a32","a33","a34","a35","a36","a37","a38","a39",
      "a40","a41","a42","a43","a44","a45","a46","a47","a48","a49","a50","a51","a52","a53","a54","a55","a56","a57","a58","a59","a60","a61","a62","a63","a64",
      "a65","a66","a67","a68","a69","a70","a71","a72","a73","a74","a75","a76","a77","a78","a79","a80","a81","a82","a83","a84","a85","a86","a87","a88","a89",
      "a90","a91","a92","a93","a94","a95","a96","a97","a98","a99","a100","a101","a102","a103","a104","a105","a106","a107","a108","a109","a110","a111","a112",
      "a113","a114","a115","a116","a117","a118","a119","a120","a121","a122","a123","a124","a125","a126","a127","a128","a129","a130","a131","a132","a133",
      "a134","a135","a136","a137","a138","a139","a140","a141","a142","a143","a144","a145","a146","a147","a148","a149","a150","a151","a152","a153","a154",
      "a155","a156","a157","a158","a159","a160","a161","a162","a163","a164","a165","a166","a167","a168","a169","a170","a171","a172","a173","a174","a175",
      "a176","a177","a178","a179","a180","a181","a182","a183","a184","a185","a186","a187","a188","a189","a190","a191","a192","a193","a194","a195","a196",
      "a197","a198","a199","a200","a201","a202","a203","a204","a205","a206","a207","a208","a209","a210","a211","a212","a213","a214","a215","a216","a217",
      "a218","a219","a220","a221","a222","a223","a224","a225","a226","a227","a228","a229","a230","a231","a232","a233","a234","a235","a236","a237","a238",
      "a239","a240","a241","a242","a243","a244","a245","a246","a247","a248","a249","a250","a251","a252","a253","a254","a255","s3","s4","s5","s6","s7","s8",
      "s9","s10","s11","s12","s13","s14","s15","s16","s17","s18","s19","s20","s21","s22","s23","s24","s25","s26","s27","s28","s29","s30","s31","s34","s35",
      "s36","s37","s38","s39","s40","s41","s42","s43","s44","s45","s46","s47","s48","s49","s50","s51","s52","s53","s54","s55","s56","s57","s58","s59","s60",
      "s61","s62","s63","s64","s65","s66","s67","s68","s69","s70","s71","s72","s73","s74","s75","s76","s77","s78","s79","s80","s81","s82","s83","s84","s85",
      "s86","s87","s88","s89","s90","s91","s92","s93","s94","s95","s96","s97","s98","s99");
}

constexpr size_t WS_HF = 4u << 20, WS_OF = 24u << 20, WS_WF = 44u << 20, WS_PF = 56u << 20, WS_FL = 80u << 20, WS_DBG = 81u << 20;
extern "C" void kernel_launch(void* const* d_in, const int* in_sizes, int n_in, void* d_out, int out_size, void* d_ws, size_t ws_size, hipStream_t stream) {
  (void)in_sizes; (void)n_in; (void)out_size; (void)ws_size;
  (void)hipFuncSetAttribute((const void*)fused_block, hipFuncAttributeMaxDynamicSharedMemorySize, 163840);
  char* ws = (char*)d_ws;
  hipLaunchKernelGGL(fused_block, dim3(256), dim3(256), 163840, stream, (const void*)d_in[0], (const void*)d_in[6], (const void*)d_in[7], (const void*)d_in[3],
                     (const void*)d_in[1], (const void*)d_in[2], (void*)(ws + WS_HF), (void*)(ws + WS_OF), (void*)(ws + WS_DBG), (const void*)d_in[8], (const void*)d_in[4],
                     (void*)(ws + WS_WF), (void*)(ws + WS_FL), (void*)(ws + WS_PF), d_out);
}
```
